```python
import torch

def _softmax(x, axis=-1):
    return torch.softmax(x, dim=axis)
def _gelu(x):
    return torch.nn.functional.gelu(x, approximate="tanh")
def _silu(x):
    return torch.nn.functional.silu(x)
def _sigmoid(x):
    return torch.sigmoid(x)
def _layernorm(x, axis=-1, eps=1e-5):
    m = x.mean(dim=axis, keepdim=True)
    v = x.var(dim=axis, keepdim=True, unbiased=False)
    return (x - m) / torch.sqrt(v + eps)
def _scatter_add(tgt, idx, src, axis=0):
    return tgt.clone().index_add_(axis, idx, src)
def _segment_sum(src, seg, n):
    out = torch.zeros((n,) + src.shape[1:], dtype=src.dtype)
    return out.index_add_(0, seg, src)
def _pad(x, pw):
    flat = tuple(v for pair in reversed(pw) for v in pair)
    return torch.nn.functional.pad(x, flat)

kernel_name = 'sequential_preLN__ssm_conv_h3__dense_activation_silu__prompt'

def setup_inputs():

    _w = (lambda a: (lambda t: t.to(torch.bfloat16) if t.is_floating_point() else t)(torch.as_tensor(a)))
    def _A_setup():
        B, S, D = 1, 2048, 512
        ks = 4
        return {
            "q": _w((lambda *s: torch.randn(*s, dtype=torch.bfloat16) * 0.05)(B, S, D)),
            "k": _w((lambda *s: torch.randn(*s, dtype=torch.bfloat16) * 0.2)(B, S, D)),
            "v": _w((lambda *s: torch.randn(*s, dtype=torch.bfloat16) * 0.2)(B, S, D)),
            "shift_k": _w((lambda *s: torch.randn(*s, dtype=torch.bfloat16) * 0.2)(D, ks) * 0.5),
            "ssm_k": _w((lambda *s: torch.randn(*s, dtype=torch.bfloat16) * 0.8)(D, S) * (1.0 / S ** 0.5)),
            "ssm_D": _w((lambda *s: torch.randn(*s, dtype=torch.bfloat16) * 0.2)(D)),
        }
    _A_in = _A_setup()

    def _B_setup():
        B, S, D, F = 1, 2048, 512, 1376
        return {
            "x": _w((lambda *s: torch.randn(*s, dtype=torch.bfloat16) * 0.05)(B, S, D)),
            "w1": _w((lambda *s: torch.randn(*s, dtype=torch.bfloat16) * 0.8)(D, F) * (D ** -0.5)),
            "w2": _w((lambda *s: torch.randn(*s, dtype=torch.bfloat16) * 0.1)(F, D) * (F ** -0.5)),
        }
    _B_in = _B_setup()

    return {
        "A_q": _A_in["q"],
        "A_k": _A_in["k"],
        "A_v": _A_in["v"],
        "A_shift_k": _A_in["shift_k"],
        "A_ssm_k": _A_in["ssm_k"],
        "A_ssm_D": _A_in["ssm_D"],
        "B_x": _B_in["x"],
        "B_w1": _B_in["w1"],
        "B_w2": _B_in["w2"],
    }

def reference(A_q, A_k, A_v, A_shift_k, A_ssm_k, A_ssm_D, B_x, B_w1, B_w2):
    def _A_ref(q, k, v, shift_k, ssm_k, ssm_D):
        B, S, D = q.shape
        ks = shift_k.shape[-1]
        k_pad = _pad(k, ((0, 0), (ks - 1, 0), (0, 0)))
        k_shift = torch.zeros_like(k)
        for t in range(ks):
            k_shift = k_shift + k_pad[:, t:t + S, :] * shift_k[:, ks - 1 - t][None, None, :]
        kv = k_shift * v
        L = 2 * S
        kv_pad = _pad(kv, ((0, 0), (0, S), (0, 0)))
        kf_pad = _pad(ssm_k, ((0, 0), (0, S)))
        kv_f = (lambda x, axes=(-2,-1): torch.fft.rfft2(x.float(), dim=axes))(kv_pad[:, None, :, :], axes=(1, 2))
        kf_f = (lambda x, axes=(-2,-1): torch.fft.rfft2(x.float(), dim=axes))(kf_pad[:, None, :], axes=(1, 2))
        kf_f = (lambda x, *d: x.permute(*d))(kf_f, 1, 2, 0)
        y_f = kv_f * kf_f[None, :, :, :]
        y = (lambda x, s, axes=(-2,-1): torch.fft.irfft2(x, s=s, dim=axes).to(torch.bfloat16))(y_f, (1, L), axes=(1, 2))[:, 0, :S, :]
        y = y + kv * ssm_D[None, None, :]
        return q * y

    def _B_ref(x, w1, w2):
        h = torch.einsum("bsd,df->bsf", x, w1)
        h = _silu(h)
        out = torch.einsum("bsf,fd->bsd", h, w2)
        return out

    x = A_q
    h = x + _A_ref(_layernorm(x, axis=-1), A_k, A_v, A_shift_k, A_ssm_k, A_ssm_D)
    h2 = h + _B_ref(_layernorm(h, axis=-1), B_w1, B_w2)
    return h2
```

```cpp
#include <hip/hip_runtime.h>
#include <hip/hip_bf16.h>
#include <stdint.h>

typedef unsigned short u16;
static __device__ __forceinline__ float bf2f(u16 v){ return __uint_as_float(((unsigned)v)<<16); }
static __device__ __forceinline__ u16 f2bf(float f){ __hip_bfloat16 b = __float2bfloat16(f); return *reinterpret_cast<u16*>(&b); }
static __device__ __forceinline__ float rb(float f){ return bf2f(f2bf(f)); }

constexpr int S=2048, D=512, F=1376;

__global__ void k_kv(const u16* k, const u16* v, const u16* sk, u16* kv){
  int idx = blockIdx.x*blockDim.x+threadIdx.x; if(idx>=S*D) return;
  int s = idx/D, d = idx%D;
  float ks=0.f;
  for(int t=0;t<4;t++){ int ss=s+t-3; float kk = ss>=0? bf2f(k[ss*D+d]):0.f; ks = rb(ks + rb(kk*bf2f(sk[d*4+3-t]))); }
  kv[idx]=f2bf(ks*bf2f(v[idx]));
}
__global__ void k_conv(const u16* kv, const u16* hk, const u16* Dv, u16* y2){
  __shared__ float u[S]; __shared__ float h[S];
  int d = blockIdx.x;
  for(int i=threadIdx.x;i<S;i+=256){ u[i]=bf2f(kv[i*D+d]); h[i]=bf2f(hk[d*S+i]); }
  __syncthreads();
  for(int s=threadIdx.x;s<S;s+=256){ float acc=0.f; for(int j=0;j<=s;j++) acc += h[j]*u[s-j];
    float y = rb(acc); y = rb(y + rb(u[s]*bf2f(Dv[d]))); y2[s*D+d]=f2bf(y); }
}
__device__ float blk_sum(float v, float* sm){
  for(int o=32;o;o>>=1) v+=__shfl_xor(v,o);
  __syncthreads();
  if((threadIdx.x&63)==0) sm[threadIdx.x>>6]=v;
  __syncthreads();
  float r=0; for(int i=0;i<(int)blockDim.x/64;i++) r+=sm[i];
  return r;
}
__global__ void k_ln(const u16* x, const u16* y2, u16* h, u16* A){
  __shared__ float sm[8];
  int s=blockIdx.x, d=threadIdx.x;
  float xv=bf2f(x[s*D+d]);
  float m=blk_sum(xv,sm)/D; float dv=xv-m; float var=blk_sum(dv*dv,sm)/D;
  float mb=rb(m), vb=rb(var);
  float q=rb(rb(xv-mb)/rb(sqrtf(rb(vb+1e-5f))));
  float a=rb(q*bf2f(y2[s*D+d]));
  float hv=rb(xv+a);
  h[s*D+d]=f2bf(hv);
  m=blk_sum(hv,sm)/D; dv=hv-m; var=blk_sum(dv*dv,sm)/D;
  mb=rb(m); vb=rb(var);
  A[s*D+d]=f2bf(rb(hv-mb)/rb(sqrtf(rb(vb+1e-5f))));
}
__global__ void k_g1(const u16* A, const u16* W1, u16* H){
  int f = blockIdx.x*blockDim.x+threadIdx.x; int s=blockIdx.y; if(f>=F) return;
  float acc=0; for(int k=0;k<D;k++) acc+=bf2f(A[s*D+k])*bf2f(W1[k*F+f]);
  float z=rb(acc); H[s*F+f]=f2bf(z/(1.f+__expf(-z)));
}
__global__ void k_g2(const u16* H, const u16* W2, const u16* h, u16* out){
  int d = blockIdx.x*blockDim.x+threadIdx.x; int s=blockIdx.y;
  float acc=0; for(int k=0;k<F;k++) acc+=bf2f(H[s*F+k])*bf2f(W2[k*D+d]);
  out[s*D+d]=f2bf(bf2f(h[s*D+d])+rb(acc));
}

extern "C" void kernel_launch(void* const* d_in, const int* in_sizes, int n_in,
                              void* d_out, int out_size, void* d_ws, size_t ws_size,
                              hipStream_t stream) {
  const u16* q=(const u16*)d_in[0]; const u16* k=(const u16*)d_in[1]; const u16* v=(const u16*)d_in[2];
  const u16* sk=(const u16*)d_in[3]; const u16* hk=(const u16*)d_in[4]; const u16* Dv=(const u16*)d_in[5];
  const u16* W1=(const u16*)d_in[7]; const u16* W2=(const u16*)d_in[8];
  u16* ws=(u16*)d_ws;
  u16* kv=ws; u16* y2=kv+S*D; u16* h=y2+S*D; u16* A=h+S*D; u16* H=A+S*D;
  k_kv<<<S*D/256,256,0,stream>>>(k,v,sk,kv);
  k_conv<<<D,256,0,stream>>>(kv,hk,Dv,y2);
  k_ln<<<S,512,0,stream>>>(q,y2,h,A);
  k_g1<<<dim3((F+255)/256,S),256,0,stream>>>(A,W1,H);
  k_g2<<<dim3(D/256,S),256,0,stream>>>(H,W2,h,(u16*)d_out);
}
```

```cpp
#include <hip/hip_runtime.h>
#include <stdint.h>

typedef unsigned short u16;
typedef unsigned int u32;
typedef unsigned long long u64;
typedef __bf16 bf16x8 __attribute__((ext_vector_type(8)));
typedef __bf16 bf16x2 __attribute__((ext_vector_type(2)));
typedef float f32x4 __attribute__((ext_vector_type(4)));
typedef float f32x2 __attribute__((ext_vector_type(2)));
typedef u32 u32x4 __attribute__((ext_vector_type(4)));
typedef u32 u32x2 __attribute__((ext_vector_type(2)));
#define GAS __attribute__((address_space(1)))
#define LAS __attribute__((address_space(3)))
#define RLX_AGENT __ATOMIC_RELAXED, __HIP_MEMORY_SCOPE_AGENT

constexpr int S = 2048, D = 512, F = 1376;

constexpr size_t WS_FLAG = 0;
constexpr size_t WS_KVT  = 1 << 16;
constexpr size_t WS_YT   = WS_KVT + (size_t)D * S * 2;
constexpr size_t WS_W1T  = WS_YT + (size_t)D * S * 2;
constexpr size_t WS_W2T  = WS_W1T + 8 * 11 * 16 * 1024;
constexpr size_t WS_CSP  = WS_W2T + 8 * 4 * 44 * 1024;
constexpr size_t WS_ST1  = WS_CSP + 16 * 8 * 11 * 16 * 4;
constexpr size_t WS_HT   = WS_ST1 + S * 8;
constexpr size_t WS_END  = WS_HT + (size_t)32 * 44 * 4 * 1024;

__device__ __forceinline__ float bf_lo(u32 v) { return __uint_as_float(v << 16); }
__device__ __forceinline__ float bf_hi(u32 v) { return __uint_as_float(v & 0xffff0000u); }
__device__ __forceinline__ u32 pack_bf(float a, float b) {
  f32x2 z = {a, b};
  return __builtin_bit_cast(u32, __builtin_convertvector(z, bf16x2));
}
__device__ __forceinline__ float rb(float a) { return bf_lo(pack_bf(a, 0.f)); }
__device__ __forceinline__ float dot2(u32 a, u32 b, float c) {
  return __builtin_amdgcn_fdot2_f32_bf16(__builtin_bit_cast(bf16x2, a), __builtin_bit_cast(bf16x2, b), c, false);
}
__device__ __forceinline__ u32 perm_lo(u32 a, u32 b) { return __builtin_amdgcn_perm(b, a, 0x05040100u); }
__device__ __forceinline__ u32 perm_hi(u32 a, u32 b) { return __builtin_amdgcn_perm(b, a, 0x07060302u); }
__device__ __forceinline__ f32x4 mfma(u32x4 a, u32x4 b, f32x4 c) {
  return __builtin_amdgcn_mfma_f32_16x16x32_bf16(__builtin_bit_cast(bf16x8, a), __builtin_bit_cast(bf16x8, b), c, 0, 0, 0);
}
constexpr u32 ONES = 0x3F803F80u;

__device__ __forceinline__ int fcol(int ks, int g, int j) {
  if (ks < 40) { int ct = ks / 5, p = ks - 5 * ct; return 128 * (2 * p + (j >> 2)) + 8 * (4 * g + (j & 3)) + ct; }
  return 1280 + 8 * (4 * g + (j & 3)) + 2 * (ks - 40) + (j >> 2);
}

__global__ __launch_bounds__(512, 2) void k1_prep(const u16* __restrict__ X, const u16* __restrict__ K, const u16* __restrict__ V,
                                                  const u16* __restrict__ SK, const u16* __restrict__ W1, const u16* __restrict__ W2,
                                                  char* __restrict__ ws) {
  __shared__ __attribute__((aligned(16))) char sm[8192];
  const int b = blockIdx.x, t = threadIdx.x, lane = t & 63;
  const int w = __builtin_amdgcn_readfirstlane(t >> 6);
  const int dt = b & 7, st = b >> 3;
  const int r = t >> 3, c = t & 7;
  const int s = st * 64 + r, ch0 = dt * 64 + c * 8;
  const u32x4 z4 = {0, 0, 0, 0};
  u32x4 k0 = *(const u32x4*)(K + (size_t)s * D + ch0);
  u32x4 k1 = s >= 1 ? *(const u32x4*)(K + (size_t)(s - 1) * D + ch0) : z4;
  u32x4 k2 = s >= 2 ? *(const u32x4*)(K + (size_t)(s - 2) * D + ch0) : z4;
  u32x4 k3 = s >= 3 ? *(const u32x4*)(K + (size_t)(s - 3) * D + ch0) : z4;
  u32x4 vv = *(const u32x4*)(V + (size_t)s * D + ch0);
  u32x4 wa[4];
#pragma unroll
  for (int i = 0; i < 4; i++) wa[i] = *(const u32x4*)(SK + ch0 * 4 + i * 8);
  const int lrow = 256 * (b & 7) + 8 * (b >> 3) + w;
  u32x4 xr = *(const u32x4*)(X + (size_t)lrow * D + lane * 8);
  const int unit = (w >= 4) ? b + 256 * (w - 4) : 1 << 20;
  const int lc = lane & 15, lg = lane >> 4;
  u32x4 wl[8];
  if (unit < 176) {
    const int blk = unit >> 4, ks = unit & 15;
    const int col = 128 * blk + 8 * lc;
#pragma unroll
    for (int j = 0; j < 8; j++) wl[j] = col < F ? *(const u32x4*)(W1 + (size_t)(32 * ks + 8 * lg + j) * F + col) : z4;
  } else if (unit < 528) {
    const int u2 = unit - 176, ks = u2 >> 3, c2 = u2 & 7;
#pragma unroll
    for (int j = 0; j < 8; j++) {
      const int f = fcol(ks, lg, j);
      u32x2 v2 = {0, 0};
      if (f < F) v2 = *(const u32x2*)(W2 + (size_t)f * D + 64 * c2 + 4 * lc);
      wl[j].x = v2.x; wl[j].y = v2.y; wl[j].z = 0; wl[j].w = 0;
    }
  }
  if (t == 0) __hip_atomic_store((GAS u32*)(ws + WS_FLAG) + b * 64, 0u, RLX_AGENT);

  u32 kvp[4];
#pragma unroll
  for (int i = 0; i < 4; i++) {
    const u32 wA0 = wa[i][0], wA1 = wa[i][1], wB0 = wa[i][2], wB1 = wa[i][3];
    float a = dot2(perm_lo(k0[i], k1[i]), wA0, dot2(perm_lo(k2[i], k3[i]), wA1, 0.f));
    float bq = dot2(perm_hi(k0[i], k1[i]), wB0, dot2(perm_hi(k2[i], k3[i]), wB1, 0.f));
    kvp[i] = pack_bf(a * bf_lo(vv[i]), bq * bf_hi(vv[i]));
  }
  {
    const int base = (((r >> 3) ^ c) << 4) + ((7 - (r & 7)) << 1);
#pragma unroll
    for (int i = 0; i < 4; i++) {
      *(u16*)(sm + (8 * c + 2 * i) * 128 + base) = (u16)(kvp[i] & 0xffff);
      *(u16*)(sm + (8 * c + 2 * i + 1) * 128 + base) = (u16)(kvp[i] >> 16);
    }
  }
  __syncthreads();
  {
    const int ch = t >> 3, cc = t & 7;
    u32x4 o = *(const u32x4*)(sm + ch * 128 + cc * 16);
    const int grp = cc ^ ((ch >> 3) & 7);
    *(u32x4*)((u16*)(ws + WS_KVT) + (size_t)(dt * 64 + ch) * S + st * 64 + grp * 8) = o;
  }
  {
    float sum = 0.f;
#pragma unroll
    for (int i = 0; i < 4; i++) sum = dot2(xr[i], ONES, sum);
#pragma unroll
    for (int o = 32; o; o >>= 1) sum += __shfl_xor(sum, o);
    const float mean = sum * (1.f / D);
    float sq = 0.f;
#pragma unroll
    for (int i = 0; i < 4; i++) { float d0 = bf_lo(xr[i]) - mean, d1 = bf_hi(xr[i]) - mean; sq += d0 * d0 + d1 * d1; }
#pragma unroll
    for (int o = 32; o; o >>= 1) sq += __shfl_xor(sq, o);
    const float sd = rb(sqrtf(rb(rb(sq * (1.f / D)) + 1e-5f)));
    if (lane == 0) { f32x2 o2 = {rb(mean), 1.f / sd}; *(f32x2*)(ws + WS_ST1 + (size_t)lrow * 8) = o2; }
  }
  if (unit < 176) {
    const int blk = unit >> 4, ks = unit & 15;
#pragma unroll
    for (int i = 0; i < 8; i++) {
      u32x4 o;
#pragma unroll
      for (int a = 0; a < 4; a++) o[a] = (i & 1) ? perm_hi(wl[2 * a][i >> 1], wl[2 * a + 1][i >> 1]) : perm_lo(wl[2 * a][i >> 1], wl[2 * a + 1][i >> 1]);
      *(u32x4*)(ws + WS_W1T + ((size_t)((i * 11 + blk) * 16 + ks) * 64 + lane) * 16) = o;
      float cs = dot2(o[0], ONES, dot2(o[1], ONES, dot2(o[2], ONES, dot2(o[3], ONES, 0.f))));
      cs += __shfl_xor(cs, 16);
      cs += __shfl_xor(cs, 32);
      if (lane < 16) *(float*)(ws + WS_CSP + ((size_t)((ks * 8 + i) * 11 + blk) * 16 + lane) * 4) = cs;
    }
  } else if (unit < 528) {
    const int u2 = unit - 176, ks = u2 >> 3, c2 = u2 & 7;
#pragma unroll
    for (int i = 0; i < 4; i++) {
      u32x4 o;
#pragma unroll
      for (int a = 0; a < 4; a++) o[a] = (i & 1) ? perm_hi(wl[2 * a][i >> 1], wl[2 * a + 1][i >> 1]) : perm_lo(wl[2 * a][i >> 1], wl[2 * a + 1][i >> 1]);
      *(u32x4*)(ws + WS_W2T + ((size_t)((c2 * 4 + i) * 44 + ks) * 64 + lane) * 16) = o;
    }
  }
}

constexpr int HC_STRIDE = 4608;
constexpr int HC_CH = 8 * HC_STRIDE;
constexpr int U_CH = (240 + 2048) * 2;
constexpr int K2_H = 0, K2_U = 2 * HC_CH, K2_R = K2_U + 2 * U_CH + 32, K2_END = K2_R + 4 * 4096;
__device__ __forceinline__ constexpr int hoff(int c) {
  return c == 0 ? 4 : c == 1 ? 2 : c == 2 ? 8 : c == 3 ? 3 : c == 4 ? 7 : c == 5 ? 0 : c == 6 ? 15 : 12;
}

__global__ __launch_bounds__(512, 2) void k2_conv(const u16* __restrict__ HK, const u16* __restrict__ DV, char* __restrict__ ws) {
  __shared__ __attribute__((aligned(16))) char sm[K2_END];
  const int b = blockIdx.x, t = threadIdx.x, lane = t & 63;
  const int w = __builtin_amdgcn_readfirstlane(t >> 6);
  const int chp = 64 * (b & 7) + 2 * (b >> 3);
  {
    const int cl = t >> 8, j = t & 255, ch = chp + cl;
    const u32x4 z4 = {0, 0, 0, 0};
    u32x4 ha = j > 0 ? *(const u32x4*)(HK + (size_t)ch * S + 8 * (j - 1)) : z4;
    u32x4 hb = *(const u32x4*)(HK + (size_t)ch * S + 8 * j);
    u32x4 uu = *(const u32x4*)((const u16*)(ws + WS_KVT) + (size_t)ch * S + 8 * j);
    char* hb_base = sm + K2_H + cl * HC_CH;
    u32 W[8] = {ha[0], ha[1], ha[2], ha[3], hb[0], hb[1], hb[2], hb[3]};
#pragma unroll
    for (int c = 0; c < 8; c++) {
      u32x4 o;
#pragma unroll
      for (int a = 0; a < 4; a++) o[a] = (c & 1) ? __builtin_amdgcn_alignbit(W[(c >> 1) + a + 1], W[(c >> 1) + a], 16) : W[(c >> 1) + a];
      *(u32x4*)(hb_base + c * HC_STRIDE + 16 * (j + 1 + hoff(c))) = o;
    }
    if (j < 8) {
      int ho = 4; ho = j == 1 ? 2 : ho; ho = j == 2 ? 8 : ho; ho = j == 3 ? 3 : ho; ho = j == 4 ? 7 : ho; ho = j == 5 ? 0 : ho; ho = j == 6 ? 15 : ho; ho = j == 7 ? 12 : ho;
      *(u32x4*)(hb_base + j * HC_STRIDE + 16 * ho) = z4;
    }
    if (j == 255) *(u32x4*)(hb_base + 16 * (257 + hoff(0))) = hb;
    char* ub = sm + K2_U + cl * U_CH;
    *(u32x4*)(ub + 480 + 16 * j) = uu;
    if (j < 30) *(u32x4*)(ub + 16 * j) = z4;
  }
  __syncthreads();
  const int cl = (w >> 1) & 1, rh = w & 1;
  const int n = lane & 15, g = lane >> 4;
  f32x4 acc[8];
  if (w < 4) {
#pragma unroll
    for (int i = 0; i < 8; i++) acc[i] = f32x4{0.f, 0.f, 0.f, 0.f};
    const int idx0 = 25 + n - 8 * g;
    const int c = idx0 & 7;
    int ho = 4; ho = c == 1 ? 2 : ho; ho = c == 2 ? 8 : ho; ho = c == 3 ? 3 : ho; ho = c == 4 ? 7 : ho; ho = c == 5 ? 0 : ho; ho = c == 6 ? 15 : ho; ho = c == 7 ? 12 : ho;
    const char* fbase = sm + K2_H + cl * HC_CH + c * HC_STRIDE + 16 * ((idx0 >> 3) + ho);
    const char* gbase = sm + K2_U + cl * U_CH + 2 * (16 * n + 8 * g + 224);
#pragma unroll
    for (int rr = 0; rr < 4; rr++) {
      const int r = 4 * rh + rr;
      u32x4 Ff[8];
#pragma unroll
      for (int a = 0; a < 8; a++) Ff[a] = *(const u32x4*)(fbase + 64 * (8 * a + r));
#pragma unroll
      for (int bb = 0; bb < 8; bb++) {
        u32x4 G = *(const u32x4*)(gbase + 64 * (8 * bb - r));
#pragma unroll
        for (int a = 0; a + bb < 8; a++) acc[a + bb] = mfma(Ff[a], G, acc[a + bb]);
      }
    }
    f32x4* rbuf = (f32x4*)(sm + K2_R) + w * 256;
    if (rh) {
#pragma unroll
      for (int i = 0; i < 4; i++) rbuf[i * 64 + lane] = acc[i];
#pragma unroll
      for (int i = 0; i < 4; i++) acc[i] = acc[4 + i];
    } else {
#pragma unroll
      for (int i = 0; i < 4; i++) rbuf[i * 64 + lane] = acc[4 + i];
    }
  }
  __syncthreads();
  if (w < 4) {
    const f32x4* pbuf = (const f32x4*)(sm + K2_R) + (w ^ 1) * 256;
    const int ch = chp + cl;
    const float dv = bf_lo((u32)DV[ch]);
    const char* ub = sm + K2_U + cl * U_CH;
#pragma unroll
    for (int i = 0; i < 4; i++) {
      const int T = 4 * rh + i;
      f32x4 a = acc[i] + pbuf[i * 64 + lane];
      const int pos = 256 * T + 16 * n + 4 * g;
      u32x2 uq = *(const u32x2*)(ub + 2 * (240 + 256 * T + 16 * n + 8 * (g >> 1) + ((g & 1) ? 0 : 4)));
      const float u0 = bf_hi(uq[1]), u1 = bf_lo(uq[1]), u2 = bf_hi(uq[0]), u3 = bf_lo(uq[0]);
      const float y0 = rb(a[0]) + rb(u0 * dv), y1 = rb(a[1]) + rb(u1 * dv), y2 = rb(a[2]) + rb(u2 * dv), y3 = rb(a[3]) + rb(u3 * dv);
      u32x2 o = {pack_bf(y0, y1), pack_bf(y2, y3)};
      *(u32x2*)((u16*)(ws + WS_YT) + (size_t)ch * S + pos) = o;
    }
  }
}

constexpr int L_H = 0;
constexpr int L_ST = 147456;
constexpr int L_CS = L_ST + 4096;
constexpr int L_END = L_CS + 1024;
constexpr int L_RED = 0;

template <int NT>
__device__ __forceinline__ void ffn_front(const u16* __restrict__ X, char* __restrict__ ws, char* sm, int t, int lane, int w, int rt, int c,
                                          u32x4 (&w2)[22], u32x4& hres) {
  const int n = lane & 15, g = lane >> 4;
  const int row0 = 64 * rt;
  u32x2 yl[2][8];
  u32x4 xf[2][4];
#pragma unroll
  for (int kk = 0; kk < 2; kk++)
#pragma unroll
    for (int j = 0; j < 8; j++)
      yl[kk][j] = *(const u32x2*)((const u16*)(ws + WS_YT) + (size_t)(32 * (2 * w + kk) + 8 * g + j) * S + row0 + 4 * n);
#pragma unroll
  for (int kk = 0; kk < 2; kk++)
#pragma unroll
    for (int i = 0; i < 4; i++) xf[kk][i] = *(const u32x4*)(X + (size_t)(row0 + 4 * n + i) * D + 32 * (2 * w + kk) + 8 * g);
  f32x4 st01 = *(const f32x4*)(ws + WS_ST1 + (size_t)(row0 + 4 * n) * 8);
  f32x4 st23 = *(const f32x4*)(ws + WS_ST1 + (size_t)(row0 + 4 * n) * 8 + 16);

  u32x4 w1[NT > 0 ? NT : 1][16];
  const char* w1src = ws + WS_W1T + (size_t)c * 11 * 16 * 1024 + lane * 16;
  const char* w2src = ws + WS_W2T + (size_t)((c * 4 + (w & 3)) * 44 + 22 * (w >> 2)) * 1024 + lane * 16;
  const int blk0 = NT == 2 ? 2 * w : 4 + w;
  float csum = 0.f;
#pragma unroll
  for (int ks = 0; ks < 16; ks++)
#pragma unroll
    for (int i = 0; i < NT; i++) w1[i][ks] = *(const u32x4*)(w1src + ((blk0 + i) * 16 + ks) * 1024);
  if (NT < 2) {
    const int j = t - 256;
    if (j < 176) {
#pragma unroll
      for (int ks = 0; ks < 16; ks++) csum += *(const float*)(ws + WS_CSP + ((size_t)(ks * 8 + c) * 176 + j) * 4);
    }
#pragma unroll
    for (int i = 0; i < 22; i++) w2[i] = *(const u32x4*)(w2src + i * 1024);
  }

  {
    float s1[4] = {0.f, 0.f, 0.f, 0.f}, s2[4] = {0.f, 0.f, 0.f, 0.f};
    const float r1[4] = {st01[1], st01[3], st23[1], st23[3]};
    const float c1[4] = {-st01[0] * st01[1], -st01[2] * st01[3], -st23[0] * st23[1], -st23[2] * st23[3]};
#pragma unroll
    for (int kk = 0; kk < 2; kk++) {
#pragma unroll
      for (int i = 0; i < 4; i++) {
        u32x4 hf;
#pragma unroll
        for (int a = 0; a < 4; a++) {
          const u32 yp = (i & 1) ? perm_hi(yl[kk][2 * a][i >> 1], yl[kk][2 * a + 1][i >> 1]) : perm_lo(yl[kk][2 * a][i >> 1], yl[kk][2 * a + 1][i >> 1]);
          const u32 xp = xf[kk][i][a];
          const float x0 = bf_lo(xp), x1 = bf_hi(xp);
          const float h0 = fmaf(fmaf(x0, r1[i], c1[i]), bf_lo(yp), x0);
          const float h1 = fmaf(fmaf(x1, r1[i], c1[i]), bf_hi(yp), x1);
          const u32 hp = pack_bf(h0, h1);
          hf[a] = hp;
          s1[i] = dot2(hp, ONES, s1[i]);
          s2[i] = dot2(hp, hp, s2[i]);
        }
        *(u32x4*)(sm + L_H + ((i * 16 + 2 * w + kk) * 64 + lane) * 16) = hf;
      }
    }
#pragma unroll
    for (int i = 0; i < 4; i++) {
      s1[i] += __shfl_xor(s1[i], 16); s1[i] += __shfl_xor(s1[i], 32);
      s2[i] += __shfl_xor(s2[i], 16); s2[i] += __shfl_xor(s2[i], 32);
    }
    if (g == 0) {
#pragma unroll
      for (int i = 0; i < 4; i++) { f32x2 o = {s1[i], s2[i]}; *(f32x2*)(sm + L_ST + ((w * 4 + i) * 16 + n) * 8) = o; }
    }
    if (NT < 2 && t - 256 < 176) *(float*)(sm + L_CS + (t - 256) * 4) = csum;
  }
  __syncthreads();

  f32x4 acc[NT > 0 ? NT : 1][4];
  if (NT > 0) {
#pragma unroll
    for (int i = 0; i < NT; i++)
#pragma unroll
      for (int m = 0; m < 4; m++) acc[i][m] = f32x4{0.f, 0.f, 0.f, 0.f};
#pragma unroll
    for (int ks = 0; ks < 16; ks++) {
      u32x4 hb[4];
#pragma unroll
      for (int m = 0; m < 4; m++) hb[m] = *(const u32x4*)(sm + L_H + ((m * 16 + ks) * 64 + lane) * 16);
#pragma unroll
      for (int i = 0; i < NT; i++)
#pragma unroll
        for (int m = 0; m < 4; m++) acc[i][m] = mfma(w1[i][ks], hb[m], acc[i][m]);
      if (NT == 2 && ks >= 2 && ks < 13) {
        __builtin_amdgcn_sched_barrier(0);
        w2[2 * (ks - 2)] = *(const u32x4*)(w2src + (2 * (ks - 2)) * 1024);
        w2[2 * (ks - 2) + 1] = *(const u32x4*)(w2src + (2 * (ks - 2) + 1) * 1024);
        __builtin_amdgcn_sched_barrier(0);
      }
    }
  }

  hres = *(const u32x4*)(sm + L_H + (((w & 3) * 16 + 2 * c + (g >> 1)) * 64 + n + 16 * (2 * (g & 1) + (w >> 2))) * 16);

  if (NT > 0) {
    const __amdgpu_buffer_rsrc_t hrs = __builtin_amdgcn_make_buffer_rsrc(ws + WS_HT + (size_t)rt * 44 * 4096, 0, 44 * 4096, 0x00020000);
    float mean[4], rstd[4];
#pragma unroll
    for (int m = 0; m < 4; m++) {
      float a = 0.f, q = 0.f;
#pragma unroll
      for (int ww = 0; ww < 8; ww++) { f32x2 v = *(const f32x2*)(sm + L_ST + ((ww * 4 + m) * 16 + n) * 8); a += v[0]; q += v[1]; }
      const float mu = a * (1.f / D);
      const float var = fmaxf(q * (1.f / D) - mu * mu, 0.f);
      mean[m] = rb(mu);
      rstd[m] = 1.f / rb(sqrtf(rb(rb(var) + 1e-5f)));
    }
    u32x2 pk[NT > 0 ? NT : 1][4];
#pragma unroll
    for (int i = 0; i < NT; i++) {
      const f32x4 cs = *(const f32x4*)(sm + L_CS + ((blk0 + i) * 16 + 4 * g) * 4);
#pragma unroll
      for (int m = 0; m < 4; m++) {
        float hv[4];
#pragma unroll
        for (int r = 0; r < 4; r++) {
          const float z = rb((acc[i][m][r] - mean[m] * cs[r]) * rstd[m]);
          hv[r] = z * __builtin_amdgcn_rcpf(1.f + __expf(-z));
        }
        pk[i][m] = u32x2{pack_bf(hv[0], hv[1]), pack_bf(hv[2], hv[3])};
      }
    }
    if (NT == 2) {
      const int ks = 5 * c + w;
#pragma unroll
      for (int m = 0; m < 4; m++) {
        u32x4 o = {pk[0][m][0], pk[0][m][1], pk[NT - 1][m][0], pk[NT - 1][m][1]};
        __builtin_amdgcn_raw_buffer_store_b128(o, hrs, ((ks * 4 + m) * 64 + lane) * 16, 0, 16);
      }
    } else {
      const int ks = w < 6 ? 5 * c + 4 : 40 + (c >> 1);
      const int half = w < 6 ? (w - 4) : (c & 1);
#pragma unroll
      for (int m = 0; m < 4; m++) __builtin_amdgcn_raw_buffer_store_b64(pk[0][m], hrs, ((ks * 4 + m) * 64 + lane) * 16 + 8 * half, 0, 16);
    }
  }
  __builtin_amdgcn_s_waitcnt(0x0f70);
  asm volatile("s_waitcnt vmcnt(0)" ::: "memory");
#pragma unroll
  for (int i = 0; i < 22; i++) asm volatile("" : "+v"(w2[i]));
}

__global__ __launch_bounds__(512, 2) void k34_ffn(const u16* __restrict__ X, char* __restrict__ ws, u16* __restrict__ OUT) {
  __shared__ __attribute__((aligned(16))) char sm[L_END];
  const int b = blockIdx.x, t = threadIdx.x, lane = t & 63;
  const int w = __builtin_amdgcn_readfirstlane(t >> 6);
  const int rt = 4 * (b & 7) + ((b >> 3) & 3), c = b >> 5;
  const int n = lane & 15, g = lane >> 4;
  const int row0 = 64 * rt;
  u32x4 w2[22];
  u32x4 hres;
  if (w < 4) ffn_front<2>(X, ws, sm, t, lane, w, rt, c, w2, hres);
  else if (w < 7) ffn_front<1>(X, ws, sm, t, lane, w, rt, c, w2, hres);
  else ffn_front<0>(X, ws, sm, t, lane, w, rt, c, w2, hres);
  __syncthreads();
  GAS u32* flags = (GAS u32*)(ws + WS_FLAG);
  if (t == 0) __hip_atomic_store(flags + b * 64, 1u, RLX_AGENT);
  if (w == 0) {
    const int pb = (b & 31) + 32 * (lane & 7);
    for (u32 spins = 0; spins < (1u << 20); spins++) {
      const u32 v = __hip_atomic_load(flags + pb * 64, RLX_AGENT);
      if (__all(v == 1u)) break;
      __builtin_amdgcn_s_sleep(1);
    }
  }
  __syncthreads();

  const char* hsrc = ws + WS_HT + (size_t)rt * 44 * 4096;
  const int kh = w >> 2;
  auto stage = [&](int q, int ldsq, int nsteps) {
#pragma unroll
    for (int i = 0; i < 6; i++) {
      const int fidx = w + 8 * i;
      if (fidx < nsteps * 8) {
        const int khh = fidx / (nsteps * 4), rem = fidx - khh * nsteps * 4, s2 = rem >> 2, m = rem & 3;
        const int ks = 22 * khh + 6 * q + s2;
        __builtin_amdgcn_global_load_lds((const GAS void*)(hsrc + ((ks * 4 + m) * 64 + lane) * 16),
                                         (LAS void*)(sm + ((ldsq * 2 + khh) * 6 + s2) * 4096 + m * 1024), 16, 0, 16  );
      }
    }
  };
  stage(0, 0, 6); stage(1, 1, 6); stage(2, 2, 6);
  f32x4 acc2[4];
#pragma unroll
  for (int m = 0; m < 4; m++) acc2[m] = f32x4{0.f, 0.f, 0.f, 0.f};
  auto compute = [&](int q, int ldsq, int nsteps) {
#pragma unroll
    for (int s2 = 0; s2 < 6; s2++) {
      if (s2 < nsteps) {
        u32x4 hb[4];
#pragma unroll
        for (int m = 0; m < 4; m++) hb[m] = *(const u32x4*)(sm + ((ldsq * 2 + kh) * 6 + s2) * 4096 + m * 1024 + lane * 16);
#pragma unroll
        for (int m = 0; m < 4; m++) acc2[m] = mfma(w2[6 * q + s2], hb[m], acc2[m]);
      }
    }
  };
  asm volatile("s_waitcnt vmcnt(12)" ::: "memory");
  __builtin_amdgcn_s_barrier();
  compute(0, 0, 6);
  asm volatile("s_waitcnt vmcnt(6) lgkmcnt(0)" ::: "memory");
  __builtin_amdgcn_s_barrier();
  stage(3, 0, 4);
  compute(1, 1, 6);
  asm volatile("s_waitcnt vmcnt(4) lgkmcnt(0)" ::: "memory");
  __builtin_amdgcn_s_barrier();
  compute(2, 2, 6);
  asm volatile("s_waitcnt vmcnt(0) lgkmcnt(0)" ::: "memory");
  __builtin_amdgcn_s_barrier();
  compute(3, 0, 4);
  __syncthreads();
#pragma unroll
  for (int m = 0; m < 4; m++) *(f32x4*)(sm + L_RED + ((w * 4 + m) * 64 + lane) * 16) = acc2[m];
  __syncthreads();
  {
    const int mt = w & 3, hh = w >> 2;
    float o[8];
#pragma unroll
    for (int i = 0; i < 4; i++) {
      const f32x2 a0 = *(const f32x2*)(sm + L_RED + ((i * 4 + mt) * 64 + lane) * 16 + 8 * hh);
      const f32x2 a1 = *(const f32x2*)(sm + L_RED + (((4 + i) * 4 + mt) * 64 + lane) * 16 + 8 * hh);
      o[i] = a0[0] + a1[0];
      o[4 + i] = a0[1] + a1[1];
    }
    u32x4 res;
#pragma unroll
    for (int a = 0; a < 4; a++) res[a] = pack_bf(bf_lo(hres[a]) + rb(o[2 * a]), bf_hi(hres[a]) + rb(o[2 * a + 1]));
    *(u32x4*)(OUT + (size_t)(row0 + 4 * n + mt) * D + 64 * c + 16 * g + 8 * hh) = res;
  }
}

extern "C" void kernel_launch(void* const* d_in, const int* in_sizes, int n_in, void* d_out, int out_size, void* d_ws,
                              size_t ws_size, hipStream_t stream) {
  const u16* X = (const u16*)d_in[0];
  const u16* K = (const u16*)d_in[1];
  const u16* V = (const u16*)d_in[2];
  const u16* SK = (const u16*)d_in[3];
  const u16* HK = (const u16*)d_in[4];
  const u16* DV = (const u16*)d_in[5];
  const u16* W1 = (const u16*)d_in[7];
  const u16* W2 = (const u16*)d_in[8];
  char* ws = (char*)d_ws;
  k1_prep<<<256, 512, 0, stream>>>(X, K, V, SK, W1, W2, ws);
  k2_conv<<<256, 512, 0, stream>>>(HK, DV, ws);
  k34_ffn<<<256, 512, 0, stream>>>(X, ws, (u16*)d_out);
}
```

```cpp
#include <hip/hip_runtime.h>
#include <stdint.h>

typedef unsigned short u16;
typedef unsigned int u32;
typedef unsigned long long u64;
typedef __bf16 bf16x8 __attribute__((ext_vector_type(8)));
typedef __bf16 bf16x2 __attribute__((ext_vector_type(2)));
typedef short s16x4 __attribute__((ext_vector_type(4)));
typedef float f32x4 __attribute__((ext_vector_type(4)));
typedef float f32x2 __attribute__((ext_vector_type(2)));
typedef u32 u32x4 __attribute__((ext_vector_type(4)));
typedef u32 u32x2 __attribute__((ext_vector_type(2)));
#define GAS __attribute__((address_space(1)))
#define LAS __attribute__((address_space(3)))
#define RLX_AGENT __ATOMIC_RELAXED, __HIP_MEMORY_SCOPE_AGENT
#define SCHED_FENCE() __builtin_amdgcn_sched_barrier(0)

constexpr int S = 2048, D = 512, F = 1376;

constexpr size_t WS_FLAG = 0;
constexpr size_t WS_KVT  = 1 << 16;
constexpr size_t WS_XT   = WS_KVT + (size_t)D * S * 2;
constexpr size_t WS_HT   = WS_XT + (size_t)D * S * 2;
constexpr size_t WS_W1T  = WS_HT + (size_t)D * S * 2;
constexpr size_t WS_W2T  = WS_W1T + 8 * 11 * 16 * 1024;
constexpr size_t WS_CSP  = WS_W2T + 8 * 4 * 44 * 1024;
constexpr size_t WS_CS   = WS_CSP + 16 * 8 * 11 * 16 * 4;
constexpr size_t WS_ST1  = WS_CS + 8 * 176 * 4;
constexpr size_t WS_HF   = WS_ST1 + S * 8;
constexpr size_t WS_END  = WS_HF + (size_t)32 * 44 * 4 * 1024;

__device__ __forceinline__ float bf_lo(u32 v) { return __uint_as_float(v << 16); }
__device__ __forceinline__ float bf_hi(u32 v) { return __uint_as_float(v & 0xffff0000u); }
__device__ __forceinline__ u32 pack_bf(float a, float b) {
  f32x2 z = {a, b};
  return __builtin_bit_cast(u32, __builtin_convertvector(z, bf16x2));
}
__device__ __forceinline__ float rb(float a) { return bf_lo(pack_bf(a, 0.f)); }
__device__ __forceinline__ float dot2(u32 a, u32 b, float c) {
  return __builtin_amdgcn_fdot2_f32_bf16(__builtin_bit_cast(bf16x2, a), __builtin_bit_cast(bf16x2, b), c, false);
}
__device__ __forceinline__ u32 perm_lo(u32 a, u32 b) { return __builtin_amdgcn_perm(b, a, 0x05040100u); }
__device__ __forceinline__ u32 perm_hi(u32 a, u32 b) { return __builtin_amdgcn_perm(b, a, 0x07060302u); }
__device__ __forceinline__ f32x4 mfma(u32x4 a, u32x4 b, f32x4 c) {
  return __builtin_amdgcn_mfma_f32_16x16x32_bf16(__builtin_bit_cast(bf16x8, a), __builtin_bit_cast(bf16x8, b), c, 0, 0, 0);
}
constexpr u32 ONES = 0x3F803F80u;
template <int V> struct IntC { static constexpr int value = V; };
__device__ __forceinline__ float sum_g(float x) {
  auto s = __builtin_amdgcn_permlane16_swap(__float_as_uint(x), __float_as_uint(x), false, false);
  x = __uint_as_float(s[0]) + __uint_as_float(s[1]);
  auto t = __builtin_amdgcn_permlane32_swap(__float_as_uint(x), __float_as_uint(x), false, false);
  return __uint_as_float(t[0]) + __uint_as_float(t[1]);
}
template <int CTRL>
__device__ __forceinline__ float dpp_add(float x) {
  return x + __uint_as_float(__builtin_amdgcn_update_dpp(0u, __float_as_uint(x), CTRL, 0xf, 0xf, true));
}
__device__ __forceinline__ float wave_sum(float x) {
  x = dpp_add<0xB1>(x);
  x = dpp_add<0x4E>(x);
  x = dpp_add<0x141>(x);
  x = dpp_add<0x140>(x);
  return sum_g(x);
}
__device__ __forceinline__ __amdgpu_buffer_rsrc_t mk_rsrc(const void* p, u32 bytes) {
  return __builtin_amdgcn_make_buffer_rsrc((void*)p, 0, bytes, 0x00020000);
}
__device__ __forceinline__ u32x4 bload16(__amdgpu_buffer_rsrc_t r, u32 off) { return __builtin_amdgcn_raw_buffer_load_b128(r, off, 0, 0); }
__device__ __forceinline__ u32x2 bload8(__amdgpu_buffer_rsrc_t r, u32 off) { return __builtin_amdgcn_raw_buffer_load_b64(r, off, 0, 0); }
template <bool SC1>
__device__ __forceinline__ void glds16(__amdgpu_buffer_rsrc_t r, u32 voff, u32 soff, u32 lds_dst) {
  if (SC1)
    asm volatile("s_mov_b32 m0, %0\n\ts_nop 0\n\tbuffer_load_dwordx4 %1, %2, %3 offen sc1 lds" ::"s"(lds_dst), "v"(voff), "s"(r), "s"(soff) : "memory");
  else
    asm volatile("s_mov_b32 m0, %0\n\ts_nop 0\n\tbuffer_load_dwordx4 %1, %2, %3 offen lds" ::"s"(lds_dst), "v"(voff), "s"(r), "s"(soff) : "memory");
}
__device__ __forceinline__ void bload16_asm(u32x4& d, __amdgpu_buffer_rsrc_t r, u32 voff, u32 soff) {
  asm volatile("buffer_load_dwordx4 %0, %1, %2, %3 offen" : "=v"(d) : "v"(voff), "s"(r), "s"(soff) : "memory");
}
__device__ __forceinline__ u32x4 bload16s(__amdgpu_buffer_rsrc_t r, u32 voff, u32 soff) { return __builtin_amdgcn_raw_buffer_load_b128(r, voff, soff, 0); }
__device__ __forceinline__ u32x2 tr_read(const char* p) {
  s16x4 r = __builtin_amdgcn_ds_read_tr16_b64_v4i16((LAS s16x4*)p);
  return __builtin_bit_cast(u32x2, r);
}

__device__ __forceinline__ int fcol(int ks, int g, int j) {
  if (ks < 40) { int ct = ks / 5, p = ks - 5 * ct; return 128 * (2 * p + (j >> 2)) + 8 * (4 * g + (j & 3)) + ct; }
  return 1280 + 8 * (4 * g + (j & 3)) + 2 * (ks - 40) + (j >> 2);
}

__global__ __launch_bounds__(512, 2) void k1_prep(const u16* __restrict__ X, const u16* __restrict__ K, const u16* __restrict__ V,
                                                  const u16* __restrict__ SK, const u16* __restrict__ W1, const u16* __restrict__ W2,
                                                  char* __restrict__ ws) {
  __shared__ __attribute__((aligned(16))) char sm[16384];
  const int b = blockIdx.x, t = threadIdx.x, lane = t & 63;
  const int w = __builtin_amdgcn_readfirstlane(t >> 6);
  const int dt = b & 7, st = b >> 3;
  if (w < 4) {
    const int rp = t >> 3, c = t & 7;
    const int s = st * 64 + 2 * rp, ch0 = dt * 64 + c * 8;
    const __amdgpu_buffer_rsrc_t rk = mk_rsrc(K, S * D * 2);
    const u32 off = (u32)(s * D + ch0) * 2;
    u32x4 kk[5];
#pragma unroll
    for (int i = 0; i < 5; i++) kk[i] = bload16(rk, off + (1 - i) * 1024);
    const u32x4 v0 = *(const u32x4*)((const char*)V + off), v1 = *(const u32x4*)((const char*)V + off + 1024);
    const u32x4 x0 = *(const u32x4*)((const char*)X + off), x1 = *(const u32x4*)((const char*)X + off + 1024);
    u32x4 wa[4];
#pragma unroll
    for (int i = 0; i < 4; i++) wa[i] = *(const u32x4*)(SK + ch0 * 4 + i * 8);
    SCHED_FENCE();
    const int swz = ((rp >> 2) ^ c) << 4;
    char* pk = sm + (8 * c) * 128 + swz + ((3 - (rp & 3)) << 2);
    char* px = sm + 8192 + (8 * c) * 128 + swz + ((rp & 3) << 2);
#pragma unroll
    for (int i = 0; i < 4; i++) {
      const u32 l01 = perm_lo(kk[0][i], kk[1][i]), l12 = perm_lo(kk[1][i], kk[2][i]), l23 = perm_lo(kk[2][i], kk[3][i]), l34 = perm_lo(kk[3][i], kk[4][i]);
      const u32 h01 = perm_hi(kk[0][i], kk[1][i]), h12 = perm_hi(kk[1][i], kk[2][i]), h23 = perm_hi(kk[2][i], kk[3][i]), h34 = perm_hi(kk[3][i], kk[4][i]);
      const float a1 = dot2(l01, wa[i][0], dot2(l23, wa[i][1], 0.f)) * bf_lo(v1[i]);
      const float a0 = dot2(l12, wa[i][0], dot2(l34, wa[i][1], 0.f)) * bf_lo(v0[i]);
      const float b1 = dot2(h01, wa[i][2], dot2(h23, wa[i][3], 0.f)) * bf_hi(v1[i]);
      const float b0 = dot2(h12, wa[i][2], dot2(h34, wa[i][3], 0.f)) * bf_hi(v0[i]);
      *(u32*)(pk + (2 * i) * 128) = pack_bf(a1, a0);
      *(u32*)(pk + (2 * i + 1) * 128) = pack_bf(b1, b0);
      *(u32*)(px + (2 * i) * 128) = perm_lo(x0[i], x1[i]);
      *(u32*)(px + (2 * i + 1) * 128) = perm_hi(x0[i], x1[i]);
    }
    __syncthreads();
#pragma unroll
    for (int i = 0; i < 2; i++) {
      const int u = t + 256 * i, ch = u >> 3, cc = u & 7;
      const u32x4 o = *(const u32x4*)(sm + ch * 128 + cc * 16);
      const u32x4 o2 = *(const u32x4*)(sm + 8192 + ch * 128 + cc * 16);
      const int grp = cc ^ ((ch >> 3) & 7);
      const size_t go = (size_t)(dt * 64 + ch) * S + st * 64 + grp * 8;
      *(u32x4*)((u16*)(ws + WS_KVT) + go) = o;
      *(u32x4*)((u16*)(ws + WS_XT) + go) = o2;
    }
  } else {
    const int lrow = 256 * (b & 7) + 8 * (b >> 3) + 2 * (w - 4);
    u32x4 xr[2];
#pragma unroll
    for (int i = 0; i < 2; i++) xr[i] = *(const u32x4*)(X + (size_t)(lrow + i) * D + lane * 8);
    const int unit = b + 256 * (w - 4);
    const int lc = lane & 15, lg = lane >> 4;
    u32x4 wl[8];
    if (unit < 176) {
      const __amdgpu_buffer_rsrc_t rw = mk_rsrc(W1, D * F * 2);
      const int blk = unit >> 4, ks = unit & 15;
      const int col = 128 * blk + 8 * lc;
      const u32 o0 = col < F ? (u32)((32 * ks + 8 * lg) * F + col) * 2 : 0x80000000u;
#pragma unroll
      for (int j = 0; j < 8; j++) wl[j] = bload16(rw, o0 + j * F * 2);
    } else if (unit < 528) {
      const __amdgpu_buffer_rsrc_t rw = mk_rsrc(W2, D * F * 2);
      const int u2 = unit - 176, ks = u2 >> 3, c2 = u2 & 7;
#pragma unroll
      for (int j = 0; j < 8; j++) {
        const u32 o = (u32)(fcol(ks, lg, j) * D + 64 * c2 + 8 * (lc >> 2) + 2 * (lc & 3)) * 2;
        wl[j].x = __builtin_amdgcn_raw_buffer_load_b32(rw, o, 0, 0);
        wl[j].y = __builtin_amdgcn_raw_buffer_load_b32(rw, o + 64, 0, 0);
        wl[j].z = 0; wl[j].w = 0;
      }
    }
    if (t == 256) __hip_atomic_store((GAS u32*)(ws + WS_FLAG) + b * 64, 0u, RLX_AGENT);
    __builtin_amdgcn_s_barrier();
#pragma unroll
    for (int q = 0; q < 2; q++) {
      float sum = 0.f;
#pragma unroll
      for (int i = 0; i < 4; i++) sum = dot2(xr[q][i], ONES, sum);
      const float mean = wave_sum(sum) * (1.f / D);
      float sq = 0.f;
#pragma unroll
      for (int i = 0; i < 4; i++) { const float d0 = bf_lo(xr[q][i]) - mean, d1 = bf_hi(xr[q][i]) - mean; sq += d0 * d0 + d1 * d1; }
      sq = wave_sum(sq);
      const float sd = rb(__builtin_amdgcn_sqrtf(rb(rb(sq * (1.f / D)) + 1e-5f)));
      const float r1 = __builtin_amdgcn_rcpf(sd);
      if (lane == 0) { f32x2 o2 = {r1, -rb(mean) * r1}; *(f32x2*)(ws + WS_ST1 + (size_t)(lrow + q) * 8) = o2; }
    }
    if (unit < 176) {
      const int blk = unit >> 4, ks = unit & 15;
#pragma unroll
      for (int i = 0; i < 8; i++) {
        u32x4 o;
#pragma unroll
        for (int a = 0; a < 4; a++) o[a] = (i & 1) ? perm_hi(wl[2 * a][i >> 1], wl[2 * a + 1][i >> 1]) : perm_lo(wl[2 * a][i >> 1], wl[2 * a + 1][i >> 1]);
        *(u32x4*)(ws + WS_W1T + ((size_t)((i * 11 + blk) * 16 + ks) * 64 + lane) * 16) = o;
        const float cs = sum_g(dot2(o[0], ONES, dot2(o[1], ONES, dot2(o[2], ONES, dot2(o[3], ONES, 0.f)))));
        if (lane < 16) *(float*)(ws + WS_CSP + ((size_t)((ks * 8 + i) * 11 + blk) * 16 + lane) * 4) = cs;
      }
    } else if (unit < 528) {
      const int u2 = unit - 176, ks = u2 >> 3, c2 = u2 & 7;
#pragma unroll
      for (int i = 0; i < 4; i++) {
        u32x4 o;
#pragma unroll
        for (int a = 0; a < 4; a++) o[a] = (i & 1) ? perm_hi(wl[2 * a][i >> 1], wl[2 * a + 1][i >> 1]) : perm_lo(wl[2 * a][i >> 1], wl[2 * a + 1][i >> 1]);
        *(u32x4*)(ws + WS_W2T + ((size_t)(((c2 * 2 + (i >> 1)) * 44 + ks) * 2 + (i & 1)) * 64 + lane) * 16) = o;
      }
    }
  }
}

constexpr int HC_STRIDE = 4608;
constexpr int HC_CH = 8 * HC_STRIDE;
constexpr int U_CH = (240 + 2048) * 2;
constexpr int K2_H = 0, K2_U = 2 * HC_CH, K2_R = K2_U + 2 * U_CH + 32, K2_PF = K2_R + 4 * 4096, K2_END = K2_PF + 4096;
__device__ __forceinline__ constexpr int hoff(int c) {
  return c == 0 ? 4 : c == 1 ? 2 : c == 2 ? 8 : c == 3 ? 3 : c == 4 ? 7 : c == 5 ? 0 : c == 6 ? 15 : 12;
}
__device__ __forceinline__ int hoff_rt(int c) { return (0xCF073824u >> (4 * c)) & 15; }

__global__ __launch_bounds__(512, 2) void k2_conv(const u16* __restrict__ HK, const u16* __restrict__ DV, char* __restrict__ ws) {
  __shared__ __attribute__((aligned(16))) char sm[K2_END];
  const int b = blockIdx.x, t = threadIdx.x, lane = t & 63;
  const int w = __builtin_amdgcn_readfirstlane(t >> 6);
  const int chp = 64 * (b & 7) + 2 * (b >> 3);
  const int wcl = (w >> 1) & 1, rh = w & 1;
  const int n = lane & 15, g = lane >> 4;
  {
    const int cl = t >> 8, j = t & 255, ch = chp + cl;
    const __amdgpu_buffer_rsrc_t rh_ = mk_rsrc(HK + (size_t)chp * S, 2 * S * 2);
    const u32 ho = (u32)(cl * S + 8 * j) * 2;
    u32x4 ha = bload16(rh_, ho - 16);
    const u32x4 hb = bload16(rh_, ho);
    const u32x4 uu = *(const u32x4*)((const u16*)(ws + WS_KVT) + (size_t)ch * S + 8 * j);
    SCHED_FENCE();
    if (j == 0) ha = u32x4{0, 0, 0, 0};
    char* hbase = sm + K2_H + cl * HC_CH;
    const u32 W[8] = {ha[0], ha[1], ha[2], ha[3], hb[0], hb[1], hb[2], hb[3]};
#pragma unroll
    for (int c = 0; c < 8; c++) {
      u32x4 o;
#pragma unroll
      for (int a = 0; a < 4; a++) o[a] = (c & 1) ? __builtin_amdgcn_alignbit(W[(c >> 1) + a + 1], W[(c >> 1) + a], 16) : W[(c >> 1) + a];
      *(u32x4*)(hbase + c * HC_STRIDE + 16 * (j + 1 + hoff(c))) = o;
    }
    const u32x4 z4 = {0, 0, 0, 0};
    if (j < 8) *(u32x4*)(hbase + j * HC_STRIDE + 16 * hoff_rt(j)) = z4;
    if (j == 255) *(u32x4*)(hbase + 16 * (257 + hoff(0))) = hb;
    char* ub = sm + K2_U + cl * U_CH;
    *(u32x4*)(ub + 480 + 16 * j) = uu;
    if (j < 30) *(u32x4*)(ub + 16 * j) = z4;
  }
  u32x2 xq[4];
  f32x4 sa[4], sb[4];
  u32 dvr = 0;
  if (w < 4) {
    const __amdgpu_buffer_rsrc_t rx = mk_rsrc(ws + WS_XT + (size_t)(chp + wcl) * S * 2, S * 2);
    const __amdgpu_buffer_rsrc_t rs1 = mk_rsrc(ws + WS_ST1, S * 8);
    const u32 p0 = 1024 * rh + 16 * n + 4 * g;
#pragma unroll
    for (int i = 0; i < 4; i++) {
      asm volatile("buffer_load_dwordx2 %0, %1, %2, %3 offen" : "=v"(xq[i]) : "v"(p0 * 2), "s"(rx), "s"(i * 512) : "memory");
      asm volatile("buffer_load_dwordx4 %0, %1, %2, %3 offen" : "=v"(sa[i]) : "v"(p0 * 8), "s"(rs1), "s"(i * 2048) : "memory");
      asm volatile("buffer_load_dwordx4 %0, %1, %2, %3 offen offset:16" : "=v"(sb[i]) : "v"(p0 * 8), "s"(rs1), "s"(i * 2048) : "memory");
    }
    dvr = *(const u32*)(DV + chp) >> (16 * wcl);
  }
  __syncthreads();
  f32x4 acc[8];
  if (w < 4) {
#pragma unroll
    for (int i = 0; i < 8; i++) acc[i] = f32x4{0.f, 0.f, 0.f, 0.f};
    const int idx0 = 25 + n - 8 * g;
    const int c = idx0 & 7;
    const char* fbase = sm + K2_H + wcl * HC_CH + c * HC_STRIDE + 16 * ((idx0 >> 3) + hoff_rt(c)) + 64 * 4 * rh;
    const char* gbase = sm + K2_U + wcl * U_CH + 2 * (16 * n + 8 * g + 224) - 64 * 4 * rh;
    u32x4 Ff[2][8], Gf[2][8];
#pragma unroll
    for (int a = 0; a < 8; a++) { Gf[0][a] = *(const u32x4*)(gbase + 64 * (8 * a)); Ff[0][a] = *(const u32x4*)(fbase + 64 * (8 * a)); }
    SCHED_FENCE();
#pragma unroll
    for (int rr = 0; rr < 4; rr++) {
      const int cur = rr & 1, nxt = cur ^ 1;
      int m = 0;
#pragma unroll
      for (int bb = 0; bb < 8; bb++) {
#pragma unroll
        for (int a = 0; a + bb < 8; a++) {
          acc[a + bb] = mfma(Ff[cur][a], Gf[cur][bb], acc[a + bb]);
          if (rr < 3 && (m & 1) == 1 && m < 32) {
            const int q = m >> 2;
            SCHED_FENCE();
            if (m & 2) Ff[nxt][q] = *(const u32x4*)(fbase + 64 * (8 * q + rr + 1));
            else Gf[nxt][q] = *(const u32x4*)(gbase + 64 * (8 * q - rr - 1));
            SCHED_FENCE();
          }
          m++;
        }
      }
      SCHED_FENCE();
    }
    f32x4* rbuf = (f32x4*)(sm + K2_R) + w * 256;
    if (rh) {
#pragma unroll
      for (int i = 0; i < 4; i++) rbuf[i * 64 + lane] = acc[i];
#pragma unroll
      for (int i = 0; i < 4; i++) acc[i] = acc[4 + i];
    } else {
#pragma unroll
      for (int i = 0; i < 4; i++) rbuf[i * 64 + lane] = acc[4 + i];
    }
  }
  else {
    const int half = (b >> 2) & 1, u = (b >> 3) * 4 + (w - 4);
    const size_t base = (u < 64 ? WS_W1T : WS_W2T) + (size_t)half * (4 * 176 * 1024) + (size_t)(u & 63) * 11 * 1024;
    const __amdgpu_buffer_rsrc_t rp = mk_rsrc(ws + base, 11 * 1024);
#pragma unroll
    for (int i = 0; i < 11; i++) glds16<false>(rp, lane * 16, i * 1024, (u32)(size_t)(sm + K2_PF) + (w - 4) * 1024);
  }
  if (w >= 4 && b < 8 && t - 256 < 176) {
    float s = 0.f;
#pragma unroll
    for (int ks = 0; ks < 16; ks++) s += *(const float*)(ws + WS_CSP + ((size_t)(ks * 8 + b) * 176 + (t - 256)) * 4);
    *(float*)(ws + WS_CS + (size_t)(b * 176 + (t - 256)) * 4) = s;
  }
  __syncthreads();
  if (w < 4) {
    asm volatile("s_waitcnt vmcnt(0)" : "+v"(xq[0]), "+v"(xq[1]), "+v"(xq[2]), "+v"(xq[3]), "+v"(sa[0]), "+v"(sa[1]), "+v"(sa[2]), "+v"(sa[3]),
                 "+v"(sb[0]), "+v"(sb[1]), "+v"(sb[2]), "+v"(sb[3])::"memory");
    const float dv = bf_lo(dvr);
    const f32x4* pbuf = (const f32x4*)(sm + K2_R) + (w ^ 1) * 256;
    const int ch = chp + wcl;
    const char* ub = sm + K2_U + wcl * U_CH;
#pragma unroll
    for (int i = 0; i < 4; i++) {
      const int T = 4 * rh + i;
      const f32x4 a = acc[i] + pbuf[i * 64 + lane];
      const int pos = 256 * T + 16 * n + 4 * g;
      const u32x2 uq = *(const u32x2*)(ub + 2 * (240 + 256 * T + 16 * n + 8 * (g >> 1) + ((g & 1) ? 0 : 4)));
      const float y0 = fmaf(bf_hi(uq[1]), dv, a[0]), y1 = fmaf(bf_lo(uq[1]), dv, a[1]), y2 = fmaf(bf_hi(uq[0]), dv, a[2]), y3 = fmaf(bf_lo(uq[0]), dv, a[3]);
      const float x0 = bf_lo(xq[i][0]), x1 = bf_hi(xq[i][0]), x2 = bf_lo(xq[i][1]), x3 = bf_hi(xq[i][1]);
      const float h0 = fmaf(fmaf(x0, sa[i][0], sa[i][1]), y0, x0), h1 = fmaf(fmaf(x1, sa[i][2], sa[i][3]), y1, x1);
      const float h2 = fmaf(fmaf(x2, sb[i][0], sb[i][1]), y2, x2), h3 = fmaf(fmaf(x3, sb[i][2], sb[i][3]), y3, x3);
      const u32x2 o = {pack_bf(h0, h1), pack_bf(h2, h3)};
      *(u32x2*)((u16*)(ws + WS_HT) + (size_t)ch * S + pos) = o;
    }
  }
  asm volatile("s_waitcnt vmcnt(0)" ::: "memory");
}

constexpr int L_HT = 0;
constexpr int L_ST = 147456;
constexpr int L_RES = L_ST + 512;
constexpr int L_END = L_RES + 8192;
constexpr int L_RED = 0;
constexpr int L_FLG = L_END;
constexpr int L_TOT = L_END + 16;
template <int LO, int HI>
__device__ __forceinline__ void load_w2(u32x4 (&w2)[22], __amdgpu_buffer_rsrc_t r, u32 voff) {
  if constexpr (LO < HI) {
    bload16_asm(w2[LO], r, voff, LO * 1024);
    load_w2<LO + 1, HI>(w2, r, voff);
  }
}
__device__ __forceinline__ float silu_val(float acc, float rs, float bias) {
  const float tt = fmaf(acc, rs, bias);
  const float e = __builtin_amdgcn_exp2f(tt);
  return tt * __builtin_amdgcn_rcpf(fmaf(e, -1.44269504089f, -1.44269504089f));
}

template <int ROLE>
__device__ __forceinline__ void ffn_wave(char* __restrict__ ws, u16* __restrict__ OUT, char* sm, int lane, int w, int rt, int c) {
  const int n = lane & 15, g = lane >> 4;
  const int row0 = 64 * rt;
  constexpr int NT = ROLE == 4 ? 0 : ROLE == 3 ? 2 : 3;
  u32x4 w2[22];
  {
    const __amdgpu_buffer_rsrc_t rh = mk_rsrc(ws + WS_HT + ((size_t)(64 * w) * S + row0) * 2, 64 * S * 2);
    const int f0 = 2 * ((lane >> 4) & 1);
    const u32 v0 = (u32)(lane >> 3) * S * 2 + (((lane & 7) ^ f0) << 4), v1 = (u32)(lane >> 3) * S * 2 + (((lane & 7) ^ (f0 + 4)) << 4);
#pragma unroll
    for (int i = 0; i < 8; i++) glds16<false>(rh, (i & 1) ? v1 : v0, i * 8 * S * 2, (u32)(size_t)(sm + L_HT) + (64 * w + 8 * i) * 128);
  }
  if (ROLE == 4 && w == 7 && lane < 4) *(volatile LAS u32*)(sm + L_FLG + lane * 4) = 0u;
  __builtin_amdgcn_s_barrier();
  constexpr int NW1 = 16 * NT, NTT = NT > 0 ? NT : 1;
  constexpr int PRE = 3 * NTT;
  u32x4 w1[NW1 > 0 ? NW1 : 1];
  f32x4 cs[NTT];
  const int blk0 = 3 * ROLE;
  const __amdgpu_buffer_rsrc_t rw1 = mk_rsrc(ws + WS_W1T + (size_t)(c * 11 + (NT > 0 ? blk0 : 0)) * 16 * 1024, 48 * 1024);
  const __amdgpu_buffer_rsrc_t rw2 = mk_rsrc(ws + WS_W2T + (size_t)((c * 2 + (w & 1)) * 44 + 11 * (w >> 1)) * 2048, 22 * 1024);
  auto w1load = [&](int k) { w1[k] = bload16s(rw1, lane * 16, (k % NTT) * 16384 + (k / NTT) * 1024); };
  auto issued = [&](int ks) constexpr { const int v = PRE + NTT * (ks + 1) + (ks < 0 ? 0 : ks < 2 ? ks + 1 : 2) * NTT; return v < NW1 ? v : NW1; };
  if (NT > 0) {
#pragma unroll
    for (int i = 0; i < PRE; i++) w1load(i);
#pragma unroll
    for (int i = 0; i < NT; i++) cs[i] = *(const f32x4*)(ws + WS_CS + (size_t)(c * 176 + (blk0 + i) * 16 + 4 * g) * 4);
  }
  asm volatile("s_waitcnt vmcnt(%0) lgkmcnt(0)" ::"n"(NT == 0 ? 0 : PRE + NT) : "memory");
  __builtin_amdgcn_s_barrier();
  const int q4 = (lane & 15) >> 2, p4 = lane & 3;
  const int fsw = 2 * ((q4 >> 1) & 1) + 4 * (g & 1);
  const char* tb[4];
#pragma unroll
  for (int m = 0; m < 4; m++) tb[m] = sm + L_HT + (8 * g + q4) * 128 + (((2 * m + (p4 >> 1)) ^ fsw) << 4) + 8 * (p4 & 1);
  auto hfrag = [&](int m, int ks) -> u32x4 {
    const u32x2 a = tr_read(tb[m] + ks * 4096), bq = tr_read(tb[m] + ks * 4096 + 512);
    return u32x4{a[0], a[1], bq[0], bq[1]};
  };
  auto save_residual = [&]() {
    const char* p = sm + L_HT + (8 * g + q4) * 128 + (((2 * (w >> 1) + (p4 >> 1)) ^ fsw) << 4) + 8 * (p4 & 1) + (2 * c + (w & 1)) * 4096;
    const u32x2 a = tr_read(p), bq = tr_read(p + 512);
    *(u32x4*)(sm + L_RES + (w * 64 + lane) * 16) = u32x4{a[0], a[1], bq[0], bq[1]};
  };
  if (ROLE == 4) {
    __builtin_amdgcn_s_setprio(3);
    const char* p = sm + L_HT + (8 * g + q4) * 128 + (((2 * (w - 4) + (p4 >> 1)) ^ fsw) << 4) + 8 * (p4 & 1);
    u32x2 fa[16][2];
#pragma unroll
    for (int ks = 0; ks < 16; ks++) { fa[ks][0] = tr_read(p + ks * 4096); fa[ks][1] = tr_read(p + ks * 4096 + 512); }
    f32x4 q0 = {0.f, 0.f, 0.f, 0.f}, q1 = {0.f, 0.f, 0.f, 0.f};
    const u32x4 ones = {ONES, ONES, ONES, ONES};
#pragma unroll
    for (int ks = 0; ks < 16; ks++) {
      const u32x4 f0 = {fa[ks][0][0], fa[ks][0][1], fa[ks][1][0], fa[ks][1][1]};
      q0 = mfma(f0, f0, q0);
      q1 = mfma(ones, f0, q1);
    }
    {
      const float lo = (n & 1) ? q0[1] : q0[0], hi = (n & 1) ? q0[3] : q0[2];
      const float q = (n & 2) ? hi : lo;
      const float mu = q1[0] * (1.f / D);
      const float var = fmaxf(q * (1.f / D) - mu * mu, 0.f);
      const float rstd = __builtin_amdgcn_rcpf(rb(__builtin_amdgcn_sqrtf(rb(rb(var) + 1e-5f))));
      if (g == (n >> 2)) *(f32x2*)(sm + L_ST + ((w - 4) * 16 + n) * 8) = f32x2{-1.44269504089f * rstd, rb(mu) * rstd * 1.44269504089f};
    }
    save_residual();
    asm volatile("s_waitcnt lgkmcnt(0)" ::: "memory");
    if (lane == 0) __hip_atomic_fetch_add((LAS u32*)(sm + L_FLG + 4), 1u, __ATOMIC_RELAXED, __HIP_MEMORY_SCOPE_WORKGROUP);
    __builtin_amdgcn_s_setprio(0);
    for (u32 spins = 0; spins < (1u << 22); spins++) {
      if (__builtin_amdgcn_readfirstlane(*(volatile LAS u32*)(sm + L_FLG + 8))) break;
      __builtin_amdgcn_s_sleep(2);
    }
    load_w2<0, 22>(w2, rw2, lane * 16);
  } else {
    f32x4 acc[NTT][4];
    u32x4 hb[3][4];
#pragma unroll
    for (int i = 0; i < NTT; i++)
#pragma unroll
      for (int m = 0; m < 4; m++) acc[i][m] = f32x4{0.f, 0.f, 0.f, 0.f};
#pragma unroll
    for (int m = 0; m < 4; m++) { hb[0][m] = hfrag(m, 0); hb[1][m] = hfrag(m, 1); }
#pragma unroll
    for (int ks = 0; ks < 16; ks++) {
#pragma unroll
      for (int m = 0; m < 4; m++) {
        acc[0][m] = mfma(w1[ks * NTT], hb[ks % 3][m], acc[0][m]);
        SCHED_FENCE();
        if (ks + 2 < 16) { const u32x2 a = tr_read(tb[m] + (ks + 2) * 4096); hb[(ks + 2) % 3][m][0] = a[0]; hb[(ks + 2) % 3][m][1] = a[1]; }
        SCHED_FENCE();
        acc[1][m] = mfma(w1[ks * NTT + 1], hb[ks % 3][m], acc[1][m]);
        SCHED_FENCE();
        if (ks + 2 < 16) { const u32x2 a = tr_read(tb[m] + (ks + 2) * 4096 + 512); hb[(ks + 2) % 3][m][2] = a[0]; hb[(ks + 2) % 3][m][3] = a[1]; }
        SCHED_FENCE();
        if (NT == 3) acc[NTT - 1][m] = mfma(w1[ks * NTT + NTT - 1], hb[ks % 3][m], acc[NTT - 1][m]);
        SCHED_FENCE();
        {
          const int lo = issued(ks - 1), cnt = issued(ks) - lo;
#pragma unroll
          for (int k = (cnt * m) / 4; k < (cnt * (m + 1)) / 4; k++) w1load(lo + k);
        }
        SCHED_FENCE();
      }
      if (ROLE == 0 && ks == 10 && lane == 0) *(volatile LAS u32*)(sm + L_FLG + 8) = 1u;
    }
    save_residual();
    for (u32 spins = 0; spins < (1u << 22); spins++) {
      if (__builtin_amdgcn_readfirstlane(*(volatile LAS u32*)(sm + L_FLG + 4)) == 4u) break;
      __builtin_amdgcn_s_sleep(1);
    }
    asm volatile("" ::: "memory");
    float rs[4], mr[4];
#pragma unroll
    for (int m = 0; m < 4; m++) { const f32x2 v = *(const f32x2*)(sm + L_ST + (m * 16 + n) * 8); rs[m] = v[0]; mr[m] = v[1]; }
    const __amdgpu_buffer_rsrc_t hrs = mk_rsrc(ws + WS_HF + (size_t)rt * 44 * 4096, 44 * 4096);
    constexpr int FULL = (ROLE & 1) ? 1 : 0;
    constexpr int SOLO = (ROLE & 1) ? 0 : 2;
#pragma unroll
    for (int m = 0; m < 4; m++) {
      u32x2 pk[NTT];
#pragma unroll
      for (int i = 0; i < NT; i++) {
        float h1[4];
#pragma unroll
        for (int r = 0; r < 4; r++) h1[r] = silu_val(acc[i][m][r], rs[m], mr[m] * cs[i][r]);
        pk[i] = u32x2{pack_bf(h1[0], h1[1]), pack_bf(h1[2], h1[3])};
      }
      if (ROLE < 3) {
        const int ksf = 5 * c + (blk0 + FULL) / 2, kss = 5 * c + (blk0 + SOLO) / 2;
        const u32x4 o = {pk[FULL][0], pk[FULL][1], pk[FULL + 1][0], pk[FULL + 1][1]};
        __builtin_amdgcn_raw_buffer_store_b128(o, hrs, ((ksf * 4 + m) * 64 + lane) * 16, 0, 16);
        __builtin_amdgcn_raw_buffer_store_b64(pk[SOLO < NTT ? SOLO : 0], hrs, ((kss * 4 + m) * 64 + lane) * 16 + 8 * ((blk0 + SOLO) & 1), 0, 16);
      } else {
        __builtin_amdgcn_raw_buffer_store_b64(pk[0], hrs, (((5 * c + 4) * 4 + m) * 64 + lane) * 16 + 8, 0, 16);
        __builtin_amdgcn_raw_buffer_store_b64(pk[1], hrs, (((40 + (c >> 1)) * 4 + m) * 64 + lane) * 16 + 8 * (c & 1), 0, 16);
      }
      SCHED_FENCE();
    }
    load_w2<0, 8>(w2, rw2, lane * 16);
    asm volatile("s_waitcnt vmcnt(8)" ::: "memory");
  }
  asm volatile("s_waitcnt lgkmcnt(0)" ::: "memory");
  __builtin_amdgcn_s_barrier();
  if (ROLE < 4) {
    __builtin_amdgcn_s_sleep(2);
    load_w2<8, 22>(w2, rw2, lane * 16);
  } else if (w == 7) {
    const int b = blockIdx.x;
    GAS u32* flags = (GAS u32*)(ws + WS_FLAG);
    if (lane == 0) __hip_atomic_store(flags + b * 64, 1u, RLX_AGENT);
    const int pb = (b & 3) + (b & 56) + 4 * ((lane >> 2) & 1) + 64 * (lane & 3);
    for (u32 spins = 0; spins < (1u << 20); spins++) {
      const u32 v = __hip_atomic_load(flags + pb * 64, RLX_AGENT);
      if (__all(v == 1u)) break;
      __builtin_amdgcn_s_sleep(1);
    }
  }
  __builtin_amdgcn_s_barrier();

  const int dh = w & 1, kq = w >> 1;
  const __amdgpu_buffer_rsrc_t rhf = mk_rsrc(ws + WS_HF + (size_t)rt * 44 * 4096 + (size_t)(11 * kq * 4 + 2 * dh) * 1024, 11 * 4096);
  const u32 ldst = (u32)(size_t)sm + (kq * 4 + 2 * dh) * 1024;
  auto stage1 = [&](int s, int e) {
    const int slot = s < 9 ? s : s - 9;
    glds16<true>(rhf, lane * 16, s * 4096 + e * 1024, ldst + slot * 16384 + e * 1024);
  };
  auto stage = [&](int s) { stage1(s, 0); stage1(s, 1); };
  stage(0); stage(1); stage(2); stage(3);
  f32x4 acc2[2][4];
#pragma unroll
  for (int i = 0; i < 2; i++)
#pragma unroll
    for (int m = 0; m < 4; m++) acc2[i][m] = f32x4{0.f, 0.f, 0.f, 0.f};
  asm volatile("s_waitcnt vmcnt(4)"
               : "+v"(w2[0]), "+v"(w2[1]), "+v"(w2[2]), "+v"(w2[3]), "+v"(w2[4]), "+v"(w2[5]), "+v"(w2[6]), "+v"(w2[7]), "+v"(w2[8]), "+v"(w2[9]), "+v"(w2[10]),
                 "+v"(w2[11]), "+v"(w2[12]), "+v"(w2[13]), "+v"(w2[14]), "+v"(w2[15]), "+v"(w2[16]), "+v"(w2[17]), "+v"(w2[18]), "+v"(w2[19]), "+v"(w2[20]), "+v"(w2[21])
               ::"memory");
  __builtin_amdgcn_s_barrier();
  {
    const char* base = sm + kq * 4096 + lane * 16;
    u32x4 hb[2][4];
#pragma unroll
    for (int m = 0; m < 4; m++) hb[0][m] = *(const u32x4*)(base + m * 1024);
#pragma unroll
    for (int s = 0; s < 11; s++) {
      if (s == 1) { asm volatile("s_waitcnt vmcnt(4)" ::: "memory"); __builtin_amdgcn_s_barrier(); }
      else if (s == 3 || s == 5) { asm volatile("s_waitcnt vmcnt(6)" ::: "memory"); __builtin_amdgcn_s_barrier(); }
      else if (s == 7) { asm volatile("s_waitcnt vmcnt(2)" ::: "memory"); __builtin_amdgcn_s_barrier(); }
      else if (s == 9) { asm volatile("s_waitcnt vmcnt(0)" ::: "memory"); __builtin_amdgcn_s_barrier(); }
      const int slot = s + 1 < 9 ? s + 1 : s + 1 - 9;
#pragma unroll
      for (int m = 0; m < 4; m++) {
        acc2[0][m] = mfma(w2[2 * s], hb[s & 1][m], acc2[0][m]);
        SCHED_FENCE();
        if (s < 10) hb[(s + 1) & 1][m] = *(const u32x4*)(base + slot * 16384 + m * 1024);
        SCHED_FENCE();
        acc2[1][m] = mfma(w2[2 * s + 1], hb[s & 1][m], acc2[1][m]);
        SCHED_FENCE();
        if (s < 2) stage1(4 + 2 * s + (m >> 1), m & 1);
        else if (s < 5 && (m & 1)) stage1(s + 6, m >> 1);
        SCHED_FENCE();
      }
    }
  }
  asm volatile("s_waitcnt lgkmcnt(0)" ::: "memory");
  __builtin_amdgcn_s_barrier();
  {
    char* red = sm + L_RED + dh * 32768;
    f32x4 o0, o1;
    auto scatter = [&](auto KQ) {
      constexpr int k = decltype(KQ)::value;
#pragma unroll
      for (int m = 0; m < 4; m++) {
        if (m != k) {
#pragma unroll
          for (int i = 0; i < 2; i++) *(f32x4*)(red + (((k * 4 + m) * 2 + i) * 64 + lane) * 16) = acc2[i][m];
        }
      }
      o0 = acc2[0][k]; o1 = acc2[1][k];
      asm volatile("" : "+v"(o0), "+v"(o1));
    };
    if (kq == 0) scatter(IntC<0>{}); else if (kq == 1) scatter(IntC<1>{}); else if (kq == 2) scatter(IntC<2>{}); else scatter(IntC<3>{});
    __syncthreads();
#pragma unroll
    for (int j = 1; j < 4; j++) {
      const int src = (kq + j) & 3;
      o0 += *(const f32x4*)(red + (((src * 4 + kq) * 2 + 0) * 64 + lane) * 16);
      o1 += *(const f32x4*)(red + (((src * 4 + kq) * 2 + 1) * 64 + lane) * 16);
    }
    const u32x4 hres = *(const u32x4*)(sm + L_RES + (w * 64 + lane) * 16);
    u32x4 res;
#pragma unroll
    for (int r = 0; r < 4; r++) res[r] = pack_bf(bf_lo(hres[r]) + rb(o0[r]), bf_hi(hres[r]) + rb(o1[r]));
    *(u32x4*)(OUT + (size_t)(row0 + 16 * kq + n) * D + 64 * c + 32 * dh + 8 * g) = res;
  }
}

__global__ __launch_bounds__(512, 2) void k34_ffn(char* __restrict__ ws, u16* __restrict__ OUT) {
  __shared__ __attribute__((aligned(16))) char sm[L_TOT];
  const int b = blockIdx.x, t = threadIdx.x, lane = t & 63;
  const int w = __builtin_amdgcn_readfirstlane(t >> 6);
  const int rt = 8 * (b & 3) + ((b >> 3) & 7), c = 4 * ((b >> 2) & 1) + (b >> 6);
  if (w == 0) ffn_wave<0>(ws, OUT, sm, lane, w, rt, c);
  else if (w == 1) ffn_wave<1>(ws, OUT, sm, lane, w, rt, c);
  else if (w == 2) ffn_wave<2>(ws, OUT, sm, lane, w, rt, c);
  else if (w == 3) ffn_wave<3>(ws, OUT, sm, lane, w, rt, c);
  else ffn_wave<4>(ws, OUT, sm, lane, w, rt, c);
}

extern "C" void kernel_launch(void* const* d_in, const int* in_sizes, int n_in, void* d_out, int out_size, void* d_ws,
                              size_t ws_size, hipStream_t stream) {
  const u16* X = (const u16*)d_in[0];
  const u16* K = (const u16*)d_in[1];
  const u16* V = (const u16*)d_in[2];
  const u16* SK = (const u16*)d_in[3];
  const u16* HK = (const u16*)d_in[4];
  const u16* DV = (const u16*)d_in[5];
  const u16* W1 = (const u16*)d_in[7];
  const u16* W2 = (const u16*)d_in[8];
  char* ws = (char*)d_ws;
  k1_prep<<<256, 512, 0, stream>>>(X, K, V, SK, W1, W2, ws);
  k2_conv<<<256, 512, 0, stream>>>(HK, DV, ws);
  k34_ffn<<<256, 512, 0, stream>>>(ws, (u16*)d_out);
}
```

```cpp
#include <hip/hip_runtime.h>
#include <stdint.h>

typedef unsigned short u16;
typedef unsigned int u32;
typedef unsigned long long u64;
typedef __bf16 bf16x8 __attribute__((ext_vector_type(8)));
typedef __bf16 bf16x2 __attribute__((ext_vector_type(2)));
typedef short s16x4 __attribute__((ext_vector_type(4)));
typedef float f32x4 __attribute__((ext_vector_type(4)));
typedef float f32x2 __attribute__((ext_vector_type(2)));
typedef u32 u32x4 __attribute__((ext_vector_type(4)));
typedef u32 u32x2 __attribute__((ext_vector_type(2)));
#define GAS __attribute__((address_space(1)))
#define LAS __attribute__((address_space(3)))
#define RLX_AGENT __ATOMIC_RELAXED, __HIP_MEMORY_SCOPE_AGENT
#define SCHED_FENCE() __builtin_amdgcn_sched_barrier(0)

constexpr int S = 2048, D = 512, F = 1376;

constexpr size_t WS_FLAG = 0;
constexpr size_t WS_KVT  = 1 << 16;
constexpr size_t WS_XT   = WS_KVT + (size_t)D * S * 2;
constexpr size_t WS_HT   = WS_XT + (size_t)D * S * 2;
constexpr size_t WS_W1T  = WS_HT + (size_t)D * S * 2;
constexpr size_t WS_W2T  = WS_W1T + 8 * 11 * 16 * 1024;
constexpr size_t WS_CSP  = WS_W2T + 8 * 4 * 44 * 1024;
constexpr size_t WS_ST1  = WS_CSP + 16 * 8 * 11 * 16 * 4;
constexpr size_t WS_HF   = WS_ST1 + S * 8;
constexpr size_t WS_END  = WS_HF + (size_t)32 * 44 * 4 * 1024;

__device__ __forceinline__ float bf_lo(u32 v) { return __uint_as_float(v << 16); }
__device__ __forceinline__ float bf_hi(u32 v) { return __uint_as_float(v & 0xffff0000u); }
__device__ __forceinline__ u32 pack_bf(float a, float b) {
  f32x2 z = {a, b};
  return __builtin_bit_cast(u32, __builtin_convertvector(z, bf16x2));
}
__device__ __forceinline__ float rb(float a) { return bf_lo(pack_bf(a, 0.f)); }
__device__ __forceinline__ float dot2(u32 a, u32 b, float c) {
  return __builtin_amdgcn_fdot2_f32_bf16(__builtin_bit_cast(bf16x2, a), __builtin_bit_cast(bf16x2, b), c, false);
}
__device__ __forceinline__ u32 perm_lo(u32 a, u32 b) { return __builtin_amdgcn_perm(b, a, 0x05040100u); }
__device__ __forceinline__ u32 perm_hi(u32 a, u32 b) { return __builtin_amdgcn_perm(b, a, 0x07060302u); }
__device__ __forceinline__ f32x4 mfma(u32x4 a, u32x4 b, f32x4 c) {
  return __builtin_amdgcn_mfma_f32_16x16x32_bf16(__builtin_bit_cast(bf16x8, a), __builtin_bit_cast(bf16x8, b), c, 0, 0, 0);
}
constexpr u32 ONES = 0x3F803F80u;
template <int V> struct IntC { static constexpr int value = V; };
__device__ __forceinline__ float sum_g(float x) {
  auto s = __builtin_amdgcn_permlane16_swap(__float_as_uint(x), __float_as_uint(x), false, false);
  x = __uint_as_float(s[0]) + __uint_as_float(s[1]);
  auto t = __builtin_amdgcn_permlane32_swap(__float_as_uint(x), __float_as_uint(x), false, false);
  return __uint_as_float(t[0]) + __uint_as_float(t[1]);
}
template <int CTRL>
__device__ __forceinline__ float dpp_add(float x) {
  return x + __uint_as_float(__builtin_amdgcn_update_dpp(0u, __float_as_uint(x), CTRL, 0xf, 0xf, true));
}
__device__ __forceinline__ float wave_sum(float x) {
  x = dpp_add<0xB1>(x);
  x = dpp_add<0x4E>(x);
  x = dpp_add<0x141>(x);
  x = dpp_add<0x140>(x);
  return sum_g(x);
}
__device__ __forceinline__ __amdgpu_buffer_rsrc_t mk_rsrc(const void* p, u32 bytes) {
  return __builtin_amdgcn_make_buffer_rsrc((void*)p, 0, bytes, 0x00020000);
}
__device__ __forceinline__ u32x4 bload16(__amdgpu_buffer_rsrc_t r, u32 off) { return __builtin_amdgcn_raw_buffer_load_b128(r, off, 0, 0); }
__device__ __forceinline__ u32x2 bload8(__amdgpu_buffer_rsrc_t r, u32 off) { return __builtin_amdgcn_raw_buffer_load_b64(r, off, 0, 0); }
template <bool SC1>
__device__ __forceinline__ void glds16(__amdgpu_buffer_rsrc_t r, u32 voff, u32 soff, u32 lds_dst) {
  if (SC1)
    asm volatile("s_mov_b32 m0, %0\n\ts_nop 0\n\tbuffer_load_dwordx4 %1, %2, %3 offen sc1 lds" ::"s"(lds_dst), "v"(voff), "s"(r), "s"(soff) : "memory");
  else
    asm volatile("s_mov_b32 m0, %0\n\ts_nop 0\n\tbuffer_load_dwordx4 %1, %2, %3 offen lds" ::"s"(lds_dst), "v"(voff), "s"(r), "s"(soff) : "memory");
}
__device__ __forceinline__ void bload16_asm(u32x4& d, __amdgpu_buffer_rsrc_t r, u32 voff, u32 soff) {
  asm volatile("buffer_load_dwordx4 %0, %1, %2, %3 offen" : "=v"(d) : "v"(voff), "s"(r), "s"(soff) : "memory");
}
#define K1_AUX 0
#define WT_AUX 16
#define K2_AUX 0
template <int AUX>
__device__ __forceinline__ void gstore16(char* base, size_t off, u32x4 v) {
  const __amdgpu_buffer_rsrc_t r = mk_rsrc(base, 0x7fffffffu);
  __builtin_amdgcn_raw_buffer_store_b128(v, r, (u32)off, 0, AUX);
}
template <int AUX>
__device__ __forceinline__ void gstore8(char* base, size_t off, u32x2 v) {
  const __amdgpu_buffer_rsrc_t r = mk_rsrc(base, 0x7fffffffu);
  __builtin_amdgcn_raw_buffer_store_b64(v, r, (u32)off, 0, AUX);
}
__device__ __forceinline__ u32x4 bload16s(__amdgpu_buffer_rsrc_t r, u32 voff, u32 soff) { return __builtin_amdgcn_raw_buffer_load_b128(r, voff, soff, 0); }
__device__ __forceinline__ u32x2 tr_read(const char* p) {
  s16x4 r = __builtin_amdgcn_ds_read_tr16_b64_v4i16((LAS s16x4*)p);
  return __builtin_bit_cast(u32x2, r);
}

__device__ __forceinline__ int fcol(int ks, int g, int j) {
  if (ks < 40) { int ct = ks / 5, p = ks - 5 * ct; return 128 * (2 * p + (j >> 2)) + 8 * (4 * g + (j & 3)) + ct; }
  return 1280 + 8 * (4 * g + (j & 3)) + 2 * (ks - 40) + (j >> 2);
}

__global__ __launch_bounds__(512, 2) void k1_prep(const u16* __restrict__ X, const u16* __restrict__ K, const u16* __restrict__ V,
                                                  const u16* __restrict__ SK, char* __restrict__ ws) {
  __shared__ __attribute__((aligned(16))) char sm[16384];
  const int b = blockIdx.x, t = threadIdx.x, lane = t & 63;
  const int w = __builtin_amdgcn_readfirstlane(t >> 6);
  const int dt = b & 7, st = b >> 3;
  if (w < 4) {
    const int rp = t >> 3, c = t & 7;
    const int s = st * 64 + 2 * rp, ch0 = dt * 64 + c * 8;
    const __amdgpu_buffer_rsrc_t rk = mk_rsrc(K, S * D * 2);
    const u32 off = (u32)(s * D + ch0) * 2;
    u32x4 kk[5];
#pragma unroll
    for (int i = 0; i < 5; i++) kk[i] = bload16(rk, off + (1 - i) * 1024);
    const u32x4 v0 = *(const u32x4*)((const char*)V + off), v1 = *(const u32x4*)((const char*)V + off + 1024);
    const u32x4 x0 = *(const u32x4*)((const char*)X + off), x1 = *(const u32x4*)((const char*)X + off + 1024);
    u32x4 wa[4];
#pragma unroll
    for (int i = 0; i < 4; i++) wa[i] = *(const u32x4*)(SK + ch0 * 4 + i * 8);
    SCHED_FENCE();
    const int swz = ((rp >> 2) ^ c) << 4;
    char* pk = sm + (8 * c) * 128 + swz + ((3 - (rp & 3)) << 2);
    char* px = sm + 8192 + (8 * c) * 128 + swz + ((rp & 3) << 2);
#pragma unroll
    for (int i = 0; i < 4; i++) {
      const u32 l01 = perm_lo(kk[0][i], kk[1][i]), l12 = perm_lo(kk[1][i], kk[2][i]), l23 = perm_lo(kk[2][i], kk[3][i]), l34 = perm_lo(kk[3][i], kk[4][i]);
      const u32 h01 = perm_hi(kk[0][i], kk[1][i]), h12 = perm_hi(kk[1][i], kk[2][i]), h23 = perm_hi(kk[2][i], kk[3][i]), h34 = perm_hi(kk[3][i], kk[4][i]);
      const float a1 = dot2(l01, wa[i][0], dot2(l23, wa[i][1], 0.f)) * bf_lo(v1[i]);
      const float a0 = dot2(l12, wa[i][0], dot2(l34, wa[i][1], 0.f)) * bf_lo(v0[i]);
      const float b1 = dot2(h01, wa[i][2], dot2(h23, wa[i][3], 0.f)) * bf_hi(v1[i]);
      const float b0 = dot2(h12, wa[i][2], dot2(h34, wa[i][3], 0.f)) * bf_hi(v0[i]);
      *(u32*)(pk + (2 * i) * 128) = pack_bf(a1, a0);
      *(u32*)(pk + (2 * i + 1) * 128) = pack_bf(b1, b0);
      *(u32*)(px + (2 * i) * 128) = perm_lo(x0[i], x1[i]);
      *(u32*)(px + (2 * i + 1) * 128) = perm_hi(x0[i], x1[i]);
    }
    __syncthreads();
#pragma unroll
    for (int i = 0; i < 2; i++) {
      const int u = t + 256 * i, ch = u >> 3, cc = u & 7;
      const u32x4 o = *(const u32x4*)(sm + ch * 128 + cc * 16);
      const int grp = cc ^ ((ch >> 3) & 7);
      const size_t go = (size_t)(dt * 64 + ch) * S + st * 64 + grp * 8;
      gstore16<K1_AUX>(ws, WS_KVT + go * 2, o);
      gstore16<K1_AUX>(ws, WS_XT + go * 2, *(const u32x4*)(sm + 8192 + ch * 128 + cc * 16));
    }
  } else {
    const int lrow = 256 * (b & 7) + 8 * (b >> 3) + 2 * (w - 4);
    u32x4 xr[2];
#pragma unroll
    for (int i = 0; i < 2; i++) xr[i] = *(const u32x4*)(X + (size_t)(lrow + i) * D + lane * 8);
    if (t == 256) __hip_atomic_store((GAS u32*)(ws + WS_FLAG) + b * 64, 0u, RLX_AGENT);
    __builtin_amdgcn_s_barrier();
#pragma unroll
    for (int q = 0; q < 2; q++) {
      float sum = 0.f;
#pragma unroll
      for (int i = 0; i < 4; i++) sum = dot2(xr[q][i], ONES, sum);
      const float mean = wave_sum(sum) * (1.f / D);
      float sq = 0.f;
#pragma unroll
      for (int i = 0; i < 4; i++) { const float d0 = bf_lo(xr[q][i]) - mean, d1 = bf_hi(xr[q][i]) - mean; sq += d0 * d0 + d1 * d1; }
      sq = wave_sum(sq);
      const float sd = rb(__builtin_amdgcn_sqrtf(rb(rb(sq * (1.f / D)) + 1e-5f)));
      const float r1 = __builtin_amdgcn_rcpf(sd);
      if (lane == 0) { f32x2 o2 = {r1, -rb(mean) * r1}; *(f32x2*)(ws + WS_ST1 + (size_t)(lrow + q) * 8) = o2; }
    }
  }
}

constexpr int HC_STRIDE = 4608;
constexpr int HC_CH = 8 * HC_STRIDE;
constexpr int U_CH = (240 + 2048) * 2;
constexpr int K2_H = 0, K2_U = 2 * HC_CH, K2_R = K2_U + 2 * U_CH + 32, K2_F = K2_R + 4 * 4096, K2_END = K2_F + 32;
__device__ __forceinline__ constexpr int hoff(int c) {
  return c == 0 ? 4 : c == 1 ? 2 : c == 2 ? 8 : c == 3 ? 3 : c == 4 ? 7 : c == 5 ? 0 : c == 6 ? 15 : 12;
}
__device__ __forceinline__ int hoff_rt(int c) { return (0xCF073824u >> (4 * c)) & 15; }
__device__ __forceinline__ void lds_wait(const char* p, u32 want) {
  for (u32 spins = 0; spins < (1u << 22); spins++) {
    if (__builtin_amdgcn_readfirstlane(*(volatile LAS u32*)p) == want) break;
    __builtin_amdgcn_s_sleep(1);
  }
  asm volatile("" ::: "memory");
}

__global__ __launch_bounds__(512, 2) void k2_conv(const u16* __restrict__ HK, const u16* __restrict__ DV,
                                                  const u16* __restrict__ W1, const u16* __restrict__ W2, char* __restrict__ ws) {
  __shared__ __attribute__((aligned(16))) char sm[K2_END];
  const int b = blockIdx.x, t = threadIdx.x, lane = t & 63;
  const int w = __builtin_amdgcn_readfirstlane(t >> 6);
  const int chp = 64 * (b & 7) + 2 * (b >> 3);
  const int wcl = (w >> 1) & 1, rh = w & 1;
  const int n = lane & 15, g = lane >> 4;
  {
    const int cl = t >> 8, j = t & 255;
    const __amdgpu_buffer_rsrc_t rh_ = mk_rsrc(HK + (size_t)chp * S, 2 * S * 2);
    const u32 ho = (u32)(cl * S + 8 * j) * 2;
    u32x4 ha, hb, uu;
    const __amdgpu_buffer_rsrc_t ru = mk_rsrc(ws + WS_KVT + (size_t)chp * S * 2, 2 * S * 2);
    bload16_asm(ha, rh_, ho - 16, 0);
    bload16_asm(hb, rh_, ho, 0);
    bload16_asm(uu, ru, ho, 0);
    asm volatile("s_waitcnt vmcnt(0)" : "+v"(ha), "+v"(hb), "+v"(uu)::"memory");
    if (t < 8) *(volatile LAS u32*)(sm + K2_F + t * 4) = 0u;
    SCHED_FENCE();
    if (j == 0) ha = u32x4{0, 0, 0, 0};
    char* hbase = sm + K2_H + cl * HC_CH;
    const u32 W[8] = {ha[0], ha[1], ha[2], ha[3], hb[0], hb[1], hb[2], hb[3]};
#pragma unroll
    for (int c = 0; c < 8; c++) {
      u32x4 o;
#pragma unroll
      for (int a = 0; a < 4; a++) o[a] = (c & 1) ? __builtin_amdgcn_alignbit(W[(c >> 1) + a + 1], W[(c >> 1) + a], 16) : W[(c >> 1) + a];
      *(u32x4*)(hbase + c * HC_STRIDE + 16 * (j + 1 + hoff(c))) = o;
    }
    const u32x4 z4 = {0, 0, 0, 0};
    if (j < 8) *(u32x4*)(hbase + j * HC_STRIDE + 16 * hoff_rt(j)) = z4;
    if (j == 255) *(u32x4*)(hbase + 16 * (257 + hoff(0))) = hb;
    char* ub = sm + K2_U + cl * U_CH;
    *(u32x4*)(ub + 480 + 16 * j) = uu;
    if (j < 30) *(u32x4*)(ub + 16 * j) = z4;
  }
  u32x2 xq[4];
  f32x4 sa[4], sb[4];
  u32 dvr = 0;
  if (w < 4) {
    const __amdgpu_buffer_rsrc_t rs1 = mk_rsrc(ws + WS_ST1, S * 8);
    const __amdgpu_buffer_rsrc_t rx = mk_rsrc(ws + WS_XT + (size_t)(chp + wcl) * S * 2, S * 2);
    const u32 p0 = 1024 * rh + 16 * n + 4 * g;
#pragma unroll
    for (int i = 0; i < 4; i++) {
      asm volatile("buffer_load_dwordx2 %0, %1, %2, %3 offen" : "=v"(xq[i]) : "v"(p0 * 2), "s"(rx), "s"(i * 512) : "memory");
      asm volatile("buffer_load_dwordx4 %0, %1, %2, %3 offen" : "=v"(sa[i]) : "v"(p0 * 8), "s"(rs1), "s"(i * 2048) : "memory");
      asm volatile("buffer_load_dwordx4 %0, %1, %2, %3 offen offset:16" : "=v"(sb[i]) : "v"(p0 * 8), "s"(rs1), "s"(i * 2048) : "memory");
    }
    dvr = *(const u32*)(DV + chp) >> (16 * wcl);
  }
  __syncthreads();
  if (w < 4) {
    f32x4 acc[8];
#pragma unroll
    for (int i = 0; i < 8; i++) acc[i] = f32x4{0.f, 0.f, 0.f, 0.f};
    const int idx0 = 25 + n - 8 * g;
    const int c = idx0 & 7;
    const char* fbase = sm + K2_H + wcl * HC_CH + c * HC_STRIDE + 16 * ((idx0 >> 3) + hoff_rt(c)) + 64 * 4 * rh;
    const char* gbase = sm + K2_U + wcl * U_CH + 2 * (16 * n + 8 * g + 224) - 64 * 4 * rh;
    u32x4 Ff[2][8], Gf[2][8];
#pragma unroll
    for (int a = 0; a < 8; a++) { Gf[0][a] = *(const u32x4*)(gbase + 64 * (8 * a)); Ff[0][a] = *(const u32x4*)(fbase + 64 * (8 * a)); }
    SCHED_FENCE();
#pragma unroll
    for (int rr = 0; rr < 4; rr++) {
      const int cur = rr & 1, nxt = cur ^ 1;
      int m = 0;
#pragma unroll
      for (int bb = 0; bb < 8; bb++) {
#pragma unroll
        for (int a = 0; a + bb < 8; a++) {
          acc[a + bb] = mfma(Ff[cur][a], Gf[cur][bb], acc[a + bb]);
          if (rr < 3 && (m & 1) == 1 && m < 32) {
            const int q = m >> 2;
            SCHED_FENCE();
            if (m & 2) Ff[nxt][q] = *(const u32x4*)(fbase + 64 * (8 * q + rr + 1));
            else Gf[nxt][q] = *(const u32x4*)(gbase + 64 * (8 * q - rr - 1));
            SCHED_FENCE();
          }
          m++;
        }
      }
      SCHED_FENCE();
    }
    f32x4* rbuf = (f32x4*)(sm + K2_R) + w * 256;
    if (rh) {
#pragma unroll
      for (int i = 0; i < 4; i++) rbuf[i * 64 + lane] = acc[i];
#pragma unroll
      for (int i = 0; i < 4; i++) acc[i] = acc[4 + i];
    } else {
#pragma unroll
      for (int i = 0; i < 4; i++) rbuf[i * 64 + lane] = acc[4 + i];
    }
    asm volatile("s_waitcnt lgkmcnt(0)" ::: "memory");
    if (lane == 0) *(volatile LAS u32*)(sm + K2_F + w * 4) = 1u;
    const int ch = chp + wcl;
    const char* ub = sm + K2_U + wcl * U_CH;
    u32x2 uq[4];
#pragma unroll
    for (int i = 0; i < 4; i++) uq[i] = *(const u32x2*)(ub + 2 * (240 + 256 * (4 * rh + i) + 16 * n + 8 * (g >> 1) + ((g & 1) ? 0 : 4)));
    asm volatile("s_waitcnt vmcnt(0)" : "+v"(xq[0]), "+v"(xq[1]), "+v"(xq[2]), "+v"(xq[3]), "+v"(sa[0]), "+v"(sa[1]), "+v"(sa[2]), "+v"(sa[3]),
                 "+v"(sb[0]), "+v"(sb[1]), "+v"(sb[2]), "+v"(sb[3])::"memory");
    lds_wait(sm + K2_F + (w ^ 1) * 4, 1u);
    const float dv = bf_lo(dvr);
    const f32x4* pbuf = (const f32x4*)(sm + K2_R) + (w ^ 1) * 256;
#pragma unroll
    for (int i = 0; i < 4; i++) {
      const int T = 4 * rh + i;
      const f32x4 a = acc[i] + pbuf[i * 64 + lane];
      const int pos = 256 * T + 16 * n + 4 * g;
      const float y0 = fmaf(bf_hi(uq[i][1]), dv, a[0]), y1 = fmaf(bf_lo(uq[i][1]), dv, a[1]), y2 = fmaf(bf_hi(uq[i][0]), dv, a[2]), y3 = fmaf(bf_lo(uq[i][0]), dv, a[3]);
      const float x0 = bf_lo(xq[i][0]), x1 = bf_hi(xq[i][0]), x2 = bf_lo(xq[i][1]), x3 = bf_hi(xq[i][1]);
      const float h0 = fmaf(fmaf(x0, sa[i][0], sa[i][1]), y0, x0), h1 = fmaf(fmaf(x1, sa[i][2], sa[i][3]), y1, x1);
      const float h2 = fmaf(fmaf(x2, sb[i][0], sb[i][1]), y2, x2), h3 = fmaf(fmaf(x3, sb[i][2], sb[i][3]), y3, x3);
      const u32x2 o = {pack_bf(h0, h1), pack_bf(h2, h3)};
      gstore8<K2_AUX>(ws, WS_HT + ((size_t)ch * S + pos) * 2, o);
    }
  } else {
    const int unit = b + 256 * (w - 4);
    const int lc = lane & 15, lg = lane >> 4;
    if (unit < 176) {
      const __amdgpu_buffer_rsrc_t rw = mk_rsrc(W1, D * F * 2);
      const int blk = unit >> 4, ks = unit & 15;
      const int col = 128 * blk + 8 * lc;
      const u32 o0 = col < F ? (u32)((32 * ks + 8 * lg) * F + col) * 2 : 0x80000000u;
      u32x4 wl[8];
#pragma unroll
      for (int j = 0; j < 8; j++) wl[j] = bload16(rw, o0 + j * F * 2);
#pragma unroll
      for (int i = 0; i < 8; i++) {
        u32x4 o;
#pragma unroll
        for (int a = 0; a < 4; a++) o[a] = (i & 1) ? perm_hi(wl[2 * a][i >> 1], wl[2 * a + 1][i >> 1]) : perm_lo(wl[2 * a][i >> 1], wl[2 * a + 1][i >> 1]);
        gstore16<WT_AUX>(ws, WS_W1T + ((size_t)((i * 11 + blk) * 16 + ks) * 64 + lane) * 16, o);
        const float cs = sum_g(dot2(o[0], ONES, dot2(o[1], ONES, dot2(o[2], ONES, dot2(o[3], ONES, 0.f)))));
        if (lane < 16) *(float*)(ws + WS_CSP + ((size_t)(i * 176 + blk * 16 + lane) * 16 + ks) * 4) = cs;
      }
    } else if (unit < 528) {
      const __amdgpu_buffer_rsrc_t rw = mk_rsrc(W2, D * F * 2);
      const int u2 = unit - 176, ks = u2 >> 3, c2 = u2 & 7;
      u32 wx[8], wy[8];
#pragma unroll
      for (int j = 0; j < 8; j++) {
        const u32 o = (u32)(fcol(ks, lg, j) * D + 64 * c2 + 8 * (lc >> 2) + 2 * (lc & 3)) * 2;
        wx[j] = __builtin_amdgcn_raw_buffer_load_b32(rw, o, 0, 0);
        wy[j] = __builtin_amdgcn_raw_buffer_load_b32(rw, o + 64, 0, 0);
      }
#pragma unroll
      for (int i = 0; i < 4; i++) {
        u32x4 o;
#pragma unroll
        for (int a = 0; a < 4; a++) {
          const u32 e0 = (i >> 1) ? wy[2 * a] : wx[2 * a], e1 = (i >> 1) ? wy[2 * a + 1] : wx[2 * a + 1];
          o[a] = (i & 1) ? perm_hi(e0, e1) : perm_lo(e0, e1);
        }
        gstore16<WT_AUX>(ws, WS_W2T + ((size_t)(((c2 * 2 + (i >> 1)) * 44 + ks) * 2 + (i & 1)) * 64 + lane) * 16, o);
      }
    }
  }
}

constexpr int L_HT = 0;
constexpr int L_ST = 147456;
constexpr int L_CS = L_ST + 512;
constexpr int L_RES = L_CS + 768;
constexpr int L_END = L_RES + 8192;
constexpr int L_RED = 0;
constexpr int L_FLG = L_END;
constexpr int L_TOT = L_END + 16;
template <int LO, int HI>
__device__ __forceinline__ void load_w2(u32x4 (&w2)[22], __amdgpu_buffer_rsrc_t r, u32 voff) {
  if constexpr (LO < HI) {
    bload16_asm(w2[LO], r, voff, LO * 1024);
    load_w2<LO + 1, HI>(w2, r, voff);
  }
}
__device__ __forceinline__ float silu_val(float acc, float rs, float bias) {
  const float tt = fmaf(acc, rs, bias);
  const float e = __builtin_amdgcn_exp2f(tt);
  return tt * __builtin_amdgcn_rcpf(fmaf(e, -1.44269504089f, -1.44269504089f));
}

template <int ROLE>
__device__ __forceinline__ void ffn_wave(char* __restrict__ ws, u16* __restrict__ OUT, char* sm, int lane, int w, int rt, int c) {
  const int n = lane & 15, g = lane >> 4;
  const int row0 = 64 * rt;
  constexpr int NT = ROLE == 4 ? 0 : ROLE == 3 ? 2 : 3;
  u32x4 w2[22];
  {
    const __amdgpu_buffer_rsrc_t rh = mk_rsrc(ws + WS_HT + ((size_t)(64 * w) * S + row0) * 2, 64 * S * 2);
    const int f0 = 2 * ((lane >> 4) & 1);
    const u32 v0 = (u32)(lane >> 3) * S * 2 + (((lane & 7) ^ f0) << 4), v1 = (u32)(lane >> 3) * S * 2 + (((lane & 7) ^ (f0 + 4)) << 4);
#pragma unroll
    for (int i = 0; i < 8; i++) glds16<false>(rh, (i & 1) ? v1 : v0, i * 8 * S * 2, (u32)(size_t)(sm + L_HT) + (64 * w + 8 * i) * 128);
  }
  if (ROLE == 4 && w == 7 && lane < 4) *(volatile LAS u32*)(sm + L_FLG + lane * 4) = 0u;
  __builtin_amdgcn_s_barrier();
  constexpr int NW1 = 16 * NT, NTT = NT > 0 ? NT : 1;
  constexpr int PRE = 3 * NTT;
  u32x4 w1[NW1 > 0 ? NW1 : 1];
  f32x4 cs[NTT];
  const int blk0 = 3 * ROLE;
  const __amdgpu_buffer_rsrc_t rw1 = mk_rsrc(ws + WS_W1T + (size_t)(c * 11 + (NT > 0 ? blk0 : 0)) * 16 * 1024, 48 * 1024);
  const __amdgpu_buffer_rsrc_t rw2 = mk_rsrc(ws + WS_W2T + (size_t)((c * 2 + (w & 1)) * 44 + 11 * (w >> 1)) * 2048, 22 * 1024);
  auto w1load = [&](int k) { w1[k] = bload16s(rw1, lane * 16, (k % NTT) * 16384 + (k / NTT) * 1024); };
  auto issued = [&](int ks) constexpr { const int v = PRE + NTT * (ks + 1) + (ks < 0 ? 0 : ks < 2 ? ks + 1 : 2) * NTT; return v < NW1 ? v : NW1; };
  if (NT > 0) {
#pragma unroll
    for (int i = 0; i < PRE; i++) w1load(i);
  }
  f32x4 csp[4];
  if (ROLE == 4) {
    const __amdgpu_buffer_rsrc_t rc = mk_rsrc(ws + WS_CSP + (size_t)c * 176 * 64, 176 * 64);
#pragma unroll
    for (int i = 0; i < 4; i++) csp[i] = __builtin_bit_cast(f32x4, bload16s(rc, (u32)((w - 4) * 64 + lane) * 64, i * 16));
  }
  asm volatile("s_waitcnt vmcnt(%0) lgkmcnt(0)" ::"n"(NT == 0 ? 4 : PRE) : "memory");
  __builtin_amdgcn_s_barrier();
  const int q4 = (lane & 15) >> 2, p4 = lane & 3;
  const int fsw = 2 * ((q4 >> 1) & 1) + 4 * (g & 1);
  const char* tb[4];
#pragma unroll
  for (int m = 0; m < 4; m++) tb[m] = sm + L_HT + (8 * g + q4) * 128 + (((2 * m + (p4 >> 1)) ^ fsw) << 4) + 8 * (p4 & 1);
  auto hfrag = [&](int m, int ks) -> u32x4 {
    const u32x2 a = tr_read(tb[m] + ks * 4096), bq = tr_read(tb[m] + ks * 4096 + 512);
    return u32x4{a[0], a[1], bq[0], bq[1]};
  };
  auto save_residual = [&]() {
    const char* p = sm + L_HT + (8 * g + q4) * 128 + (((2 * (w >> 1) + (p4 >> 1)) ^ fsw) << 4) + 8 * (p4 & 1) + (2 * c + (w & 1)) * 4096;
    const u32x2 a = tr_read(p), bq = tr_read(p + 512);
    *(u32x4*)(sm + L_RES + (w * 64 + lane) * 16) = u32x4{a[0], a[1], bq[0], bq[1]};
  };
  if (ROLE == 4) {
    __builtin_amdgcn_s_setprio(3);
    const char* p = sm + L_HT + (8 * g + q4) * 128 + (((2 * (w - 4) + (p4 >> 1)) ^ fsw) << 4) + 8 * (p4 & 1);
    u32x2 fa[16][2];
#pragma unroll
    for (int ks = 0; ks < 16; ks++) { fa[ks][0] = tr_read(p + ks * 4096); fa[ks][1] = tr_read(p + ks * 4096 + 512); }
    f32x4 q0 = {0.f, 0.f, 0.f, 0.f}, q1 = {0.f, 0.f, 0.f, 0.f};
    const u32x4 ones = {ONES, ONES, ONES, ONES};
#pragma unroll
    for (int ks = 0; ks < 16; ks++) {
      const u32x4 f0 = {fa[ks][0][0], fa[ks][0][1], fa[ks][1][0], fa[ks][1][1]};
      q0 = mfma(f0, f0, q0);
      q1 = mfma(ones, f0, q1);
    }
    {
      const float lo = (n & 1) ? q0[1] : q0[0], hi = (n & 1) ? q0[3] : q0[2];
      const float q = (n & 2) ? hi : lo;
      const float mu = q1[0] * (1.f / D);
      const float var = fmaxf(q * (1.f / D) - mu * mu, 0.f);
      const float rstd = __builtin_amdgcn_rcpf(rb(__builtin_amdgcn_sqrtf(rb(rb(var) + 1e-5f))));
      if (g == (n >> 2)) *(f32x2*)(sm + L_ST + ((w - 4) * 16 + n) * 8) = f32x2{-1.44269504089f * rstd, rb(mu) * rstd * 1.44269504089f};
    }
    {
      const f32x4 s4 = (csp[0] + csp[1]) + (csp[2] + csp[3]);
      if ((w - 4) * 64 + lane < 176) *(float*)(sm + L_CS + ((w - 4) * 64 + lane) * 4) = (s4[0] + s4[1]) + (s4[2] + s4[3]);
    }
    save_residual();
    asm volatile("s_waitcnt lgkmcnt(0)" ::: "memory");
    if (lane == 0) __hip_atomic_fetch_add((LAS u32*)(sm + L_FLG + 4), 1u, __ATOMIC_RELAXED, __HIP_MEMORY_SCOPE_WORKGROUP);
    __builtin_amdgcn_s_setprio(0);
    for (u32 spins = 0; spins < (1u << 22); spins++) {
      if (__builtin_amdgcn_readfirstlane(*(volatile LAS u32*)(sm + L_FLG + 8))) break;
      __builtin_amdgcn_s_sleep(2);
    }
    load_w2<0, 22>(w2, rw2, lane * 16);
  } else {
    f32x4 acc[NTT][4];
    u32x4 hb[3][4];
#pragma unroll
    for (int i = 0; i < NTT; i++)
#pragma unroll
      for (int m = 0; m < 4; m++) acc[i][m] = f32x4{0.f, 0.f, 0.f, 0.f};
#pragma unroll
    for (int m = 0; m < 4; m++) { hb[0][m] = hfrag(m, 0); hb[1][m] = hfrag(m, 1); }
#pragma unroll
    for (int ks = 0; ks < 16; ks++) {
#pragma unroll
      for (int m = 0; m < 4; m++) {
        acc[0][m] = mfma(w1[ks * NTT], hb[ks % 3][m], acc[0][m]);
        SCHED_FENCE();
        if (ks + 2 < 16) { const u32x2 a = tr_read(tb[m] + (ks + 2) * 4096); hb[(ks + 2) % 3][m][0] = a[0]; hb[(ks + 2) % 3][m][1] = a[1]; }
        SCHED_FENCE();
        acc[1][m] = mfma(w1[ks * NTT + 1], hb[ks % 3][m], acc[1][m]);
        SCHED_FENCE();
        if (ks + 2 < 16) { const u32x2 a = tr_read(tb[m] + (ks + 2) * 4096 + 512); hb[(ks + 2) % 3][m][2] = a[0]; hb[(ks + 2) % 3][m][3] = a[1]; }
        SCHED_FENCE();
        if (NT == 3) acc[NTT - 1][m] = mfma(w1[ks * NTT + NTT - 1], hb[ks % 3][m], acc[NTT - 1][m]);
        SCHED_FENCE();
        {
          const int lo = issued(ks - 1), cnt = issued(ks) - lo;
#pragma unroll
          for (int k = (cnt * m) / 4; k < (cnt * (m + 1)) / 4; k++) w1load(lo + k);
        }
        SCHED_FENCE();
      }
      if (ROLE == 0 && ks == 10 && lane == 0) *(volatile LAS u32*)(sm + L_FLG + 8) = 1u;
    }
    save_residual();
    for (u32 spins = 0; spins < (1u << 22); spins++) {
      if (__builtin_amdgcn_readfirstlane(*(volatile LAS u32*)(sm + L_FLG + 4)) == 4u) break;
      __builtin_amdgcn_s_sleep(1);
    }
    asm volatile("" ::: "memory");
    float rs[4], mr[4];
#pragma unroll
    for (int m = 0; m < 4; m++) { const f32x2 v = *(const f32x2*)(sm + L_ST + (m * 16 + n) * 8); rs[m] = v[0]; mr[m] = v[1]; }
#pragma unroll
    for (int i = 0; i < NT; i++) cs[i] = *(const f32x4*)(sm + L_CS + ((blk0 + i) * 16 + 4 * g) * 4);
    const __amdgpu_buffer_rsrc_t hrs = mk_rsrc(ws + WS_HF + (size_t)rt * 44 * 4096, 44 * 4096);
    constexpr int FULL = (ROLE & 1) ? 1 : 0;
    constexpr int SOLO = (ROLE & 1) ? 0 : 2;
#pragma unroll
    for (int m = 0; m < 4; m++) {
      u32x2 pk[NTT];
#pragma unroll
      for (int i = 0; i < NT; i++) {
        float h1[4];
#pragma unroll
        for (int r = 0; r < 4; r++) h1[r] = silu_val(acc[i][m][r], rs[m], mr[m] * cs[i][r]);
        pk[i] = u32x2{pack_bf(h1[0], h1[1]), pack_bf(h1[2], h1[3])};
      }
      if (ROLE < 3) {
        const int ksf = 5 * c + (blk0 + FULL) / 2, kss = 5 * c + (blk0 + SOLO) / 2;
        const u32x4 o = {pk[FULL][0], pk[FULL][1], pk[FULL + 1][0], pk[FULL + 1][1]};
        __builtin_amdgcn_raw_buffer_store_b128(o, hrs, ((ksf * 4 + m) * 64 + lane) * 16, 0, 16);
        __builtin_amdgcn_raw_buffer_store_b64(pk[SOLO < NTT ? SOLO : 0], hrs, ((kss * 4 + m) * 64 + lane) * 16 + 8 * ((blk0 + SOLO) & 1), 0, 16);
      } else {
        __builtin_amdgcn_raw_buffer_store_b64(pk[0], hrs, (((5 * c + 4) * 4 + m) * 64 + lane) * 16 + 8, 0, 16);
        __builtin_amdgcn_raw_buffer_store_b64(pk[1], hrs, (((40 + (c >> 1)) * 4 + m) * 64 + lane) * 16 + 8 * (c & 1), 0, 16);
      }
      SCHED_FENCE();
    }
    load_w2<0, 8>(w2, rw2, lane * 16);
    asm volatile("s_waitcnt vmcnt(8)" ::: "memory");
  }
  asm volatile("s_waitcnt lgkmcnt(0)" ::: "memory");
  __builtin_amdgcn_s_barrier();
  if (ROLE < 4) {
    __builtin_amdgcn_s_sleep(2);
    load_w2<8, 22>(w2, rw2, lane * 16);
  } else if (w == 7) {
    const int b = blockIdx.x;
    GAS u32* flags = (GAS u32*)(ws + WS_FLAG);
    if (lane == 0) __hip_atomic_store(flags + b * 64, 1u, RLX_AGENT);
    const int pb = (b & 3) + (b & 56) + 4 * ((lane >> 2) & 1) + 64 * (lane & 3);
    for (u32 spins = 0; spins < (1u << 20); spins++) {
      const u32 v = __hip_atomic_load(flags + pb * 64, RLX_AGENT);
      if (__all(v == 1u)) break;
      __builtin_amdgcn_s_sleep(1);
    }
  }
  __builtin_amdgcn_s_barrier();

  const int dh = w & 1, kq = w >> 1;
  const __amdgpu_buffer_rsrc_t rhf = mk_rsrc(ws + WS_HF + (size_t)rt * 44 * 4096 + (size_t)(11 * kq * 4 + 2 * dh) * 1024, 11 * 4096);
  const u32 ldst = (u32)(size_t)sm + (kq * 4 + 2 * dh) * 1024;
  auto stage1 = [&](int s, int e) {
    const int slot = s < 9 ? s : s - 9;
    glds16<true>(rhf, lane * 16, s * 4096 + e * 1024, ldst + slot * 16384 + e * 1024);
  };
  auto stage = [&](int s) { stage1(s, 0); stage1(s, 1); };
  stage(0); stage(1); stage(2); stage(3);
  f32x4 acc2[2][4];
#pragma unroll
  for (int i = 0; i < 2; i++)
#pragma unroll
    for (int m = 0; m < 4; m++) acc2[i][m] = f32x4{0.f, 0.f, 0.f, 0.f};
  asm volatile("s_waitcnt vmcnt(4)"
               : "+v"(w2[0]), "+v"(w2[1]), "+v"(w2[2]), "+v"(w2[3]), "+v"(w2[4]), "+v"(w2[5]), "+v"(w2[6]), "+v"(w2[7]), "+v"(w2[8]), "+v"(w2[9]), "+v"(w2[10]),
                 "+v"(w2[11]), "+v"(w2[12]), "+v"(w2[13]), "+v"(w2[14]), "+v"(w2[15]), "+v"(w2[16]), "+v"(w2[17]), "+v"(w2[18]), "+v"(w2[19]), "+v"(w2[20]), "+v"(w2[21])
               ::"memory");
  __builtin_amdgcn_s_barrier();
  {
    const char* base = sm + kq * 4096 + lane * 16;
    u32x4 hb[2][4];
#pragma unroll
    for (int m = 0; m < 4; m++) hb[0][m] = *(const u32x4*)(base + m * 1024);
#pragma unroll
    for (int s = 0; s < 11; s++) {
      if (s == 1) { asm volatile("s_waitcnt vmcnt(4)" ::: "memory"); __builtin_amdgcn_s_barrier(); }
      else if (s == 3 || s == 5) { asm volatile("s_waitcnt vmcnt(6)" ::: "memory"); __builtin_amdgcn_s_barrier(); }
      else if (s == 7) { asm volatile("s_waitcnt vmcnt(2)" ::: "memory"); __builtin_amdgcn_s_barrier(); }
      else if (s == 9) { asm volatile("s_waitcnt vmcnt(0)" ::: "memory"); __builtin_amdgcn_s_barrier(); }
      const int slot = s + 1 < 9 ? s + 1 : s + 1 - 9;
#pragma unroll
      for (int m = 0; m < 4; m++) {
        acc2[0][m] = mfma(w2[2 * s], hb[s & 1][m], acc2[0][m]);
        SCHED_FENCE();
        if (s < 10) hb[(s + 1) & 1][m] = *(const u32x4*)(base + slot * 16384 + m * 1024);
        SCHED_FENCE();
        acc2[1][m] = mfma(w2[2 * s + 1], hb[s & 1][m], acc2[1][m]);
        SCHED_FENCE();
        if (s < 2) stage1(4 + 2 * s + (m >> 1), m & 1);
        else if (s < 5 && (m & 1)) stage1(s + 6, m >> 1);
        SCHED_FENCE();
      }
    }
  }
  asm volatile("s_waitcnt lgkmcnt(0)" ::: "memory");
  __builtin_amdgcn_s_barrier();
  {
    char* red = sm + L_RED + dh * 32768;
    f32x4 o0, o1;
    auto scatter = [&](auto KQ) {
      constexpr int k = decltype(KQ)::value;
#pragma unroll
      for (int m = 0; m < 4; m++) {
        if (m != k) {
#pragma unroll
          for (int i = 0; i < 2; i++) *(f32x4*)(red + (((k * 4 + m) * 2 + i) * 64 + lane) * 16) = acc2[i][m];
        }
      }
      o0 = acc2[0][k]; o1 = acc2[1][k];
      asm volatile("" : "+v"(o0), "+v"(o1));
    };
    if (kq == 0) scatter(IntC<0>{}); else if (kq == 1) scatter(IntC<1>{}); else if (kq == 2) scatter(IntC<2>{}); else scatter(IntC<3>{});
    __syncthreads();
#pragma unroll
    for (int j = 1; j < 4; j++) {
      const int src = (kq + j) & 3;
      o0 += *(const f32x4*)(red + (((src * 4 + kq) * 2 + 0) * 64 + lane) * 16);
      o1 += *(const f32x4*)(red + (((src * 4 + kq) * 2 + 1) * 64 + lane) * 16);
    }
    const u32x4 hres = *(const u32x4*)(sm + L_RES + (w * 64 + lane) * 16);
    u32x4 res;
#pragma unroll
    for (int r = 0; r < 4; r++) res[r] = pack_bf(bf_lo(hres[r]) + rb(o0[r]), bf_hi(hres[r]) + rb(o1[r]));
    *(u32x4*)(OUT + (size_t)(row0 + 16 * kq + n) * D + 64 * c + 32 * dh + 8 * g) = res;
  }
}

__global__ __launch_bounds__(512, 2) void k34_ffn(char* __restrict__ ws, u16* __restrict__ OUT) {
  __shared__ __attribute__((aligned(16))) char sm[L_TOT];
  const int b = blockIdx.x, t = threadIdx.x, lane = t & 63;
  const int w = __builtin_amdgcn_readfirstlane(t >> 6);
  const int rt = 8 * (b & 3) + ((b >> 3) & 7), c = 4 * ((b >> 2) & 1) + (b >> 6);
  if (w == 0) ffn_wave<0>(ws, OUT, sm, lane, w, rt, c);
  else if (w == 1) ffn_wave<1>(ws, OUT, sm, lane, w, rt, c);
  else if (w == 2) ffn_wave<2>(ws, OUT, sm, lane, w, rt, c);
  else if (w == 3) ffn_wave<3>(ws, OUT, sm, lane, w, rt, c);
  else ffn_wave<4>(ws, OUT, sm, lane, w, rt, c);
}

extern "C" void kernel_launch(void* const* d_in, const int* in_sizes, int n_in, void* d_out, int out_size, void* d_ws,
                              size_t ws_size, hipStream_t stream) {
  const u16* X = (const u16*)d_in[0];
  const u16* K = (const u16*)d_in[1];
  const u16* V = (const u16*)d_in[2];
  const u16* SK = (const u16*)d_in[3];
  const u16* HK = (const u16*)d_in[4];
  const u16* DV = (const u16*)d_in[5];
  const u16* W1 = (const u16*)d_in[7];
  const u16* W2 = (const u16*)d_in[8];
  char* ws = (char*)d_ws;
  k1_prep<<<256, 512, 0, stream>>>(X, K, V, SK, ws);
  k2_conv<<<256, 512, 0, stream>>>(HK, DV, W1, W2, ws);
  k34_ffn<<<256, 512, 0, stream>>>(ws, (u16*)d_out);
}
```

```cpp
#include <hip/hip_runtime.h>
#include <stdint.h>

typedef unsigned short u16;
typedef unsigned int u32;
typedef unsigned long long u64;
typedef __bf16 bf16x8 __attribute__((ext_vector_type(8)));
typedef __bf16 bf16x2 __attribute__((ext_vector_type(2)));
typedef short s16x4 __attribute__((ext_vector_type(4)));
typedef float f32x4 __attribute__((ext_vector_type(4)));
typedef float f32x2 __attribute__((ext_vector_type(2)));
typedef u32 u32x4 __attribute__((ext_vector_type(4)));
typedef u32 u32x2 __attribute__((ext_vector_type(2)));
#define GAS __attribute__((address_space(1)))
#define LAS __attribute__((address_space(3)))
#define RLX_AGENT __ATOMIC_RELAXED, __HIP_MEMORY_SCOPE_AGENT
#define SCHED_FENCE() __builtin_amdgcn_sched_barrier(0)

constexpr int S = 2048, D = 512, F = 1376;

constexpr size_t WS_FLAG = 0;
constexpr size_t WS_KVT  = 1 << 16;
constexpr size_t WS_XT   = WS_KVT + (size_t)D * S * 2;
constexpr size_t WS_HT   = WS_XT + (size_t)D * S * 2;
constexpr size_t WS_W1T  = WS_HT + (size_t)D * S * 2;
constexpr size_t WS_W2T  = WS_W1T + 8 * 11 * 16 * 1024;
constexpr size_t WS_CSP  = WS_W2T + 8 * 4 * 44 * 1024;
constexpr size_t WS_ST1  = WS_CSP + 16 * 8 * 11 * 16 * 4;
constexpr size_t WS_HF   = WS_ST1 + S * 8;
constexpr size_t WS_END  = WS_HF + (size_t)32 * 44 * 4 * 1024;

__device__ __forceinline__ float bf_lo(u32 v) { return __uint_as_float(v << 16); }
__device__ __forceinline__ float bf_hi(u32 v) { return __uint_as_float(v & 0xffff0000u); }
__device__ __forceinline__ u32 pack_bf(float a, float b) {
  f32x2 z = {a, b};
  return __builtin_bit_cast(u32, __builtin_convertvector(z, bf16x2));
}
__device__ __forceinline__ float rb(float a) { return bf_lo(pack_bf(a, 0.f)); }
__device__ __forceinline__ float dot2(u32 a, u32 b, float c) {
  return __builtin_amdgcn_fdot2_f32_bf16(__builtin_bit_cast(bf16x2, a), __builtin_bit_cast(bf16x2, b), c, false);
}
__device__ __forceinline__ u32 perm_lo(u32 a, u32 b) { return __builtin_amdgcn_perm(b, a, 0x05040100u); }
__device__ __forceinline__ u32 perm_hi(u32 a, u32 b) { return __builtin_amdgcn_perm(b, a, 0x07060302u); }
__device__ __forceinline__ f32x4 mfma(u32x4 a, u32x4 b, f32x4 c) {
  return __builtin_amdgcn_mfma_f32_16x16x32_bf16(__builtin_bit_cast(bf16x8, a), __builtin_bit_cast(bf16x8, b), c, 0, 0, 0);
}
constexpr u32 ONES = 0x3F803F80u;
template <int V> struct IntC { static constexpr int value = V; };
__device__ __forceinline__ float sum_g(float x) {
  auto s = __builtin_amdgcn_permlane16_swap(__float_as_uint(x), __float_as_uint(x), false, false);
  x = __uint_as_float(s[0]) + __uint_as_float(s[1]);
  auto t = __builtin_amdgcn_permlane32_swap(__float_as_uint(x), __float_as_uint(x), false, false);
  return __uint_as_float(t[0]) + __uint_as_float(t[1]);
}
template <int CTRL>
__device__ __forceinline__ float dpp_add(float x) {
  return x + __uint_as_float(__builtin_amdgcn_update_dpp(0u, __float_as_uint(x), CTRL, 0xf, 0xf, true));
}
__device__ __forceinline__ float wave_sum(float x) {
  x = dpp_add<0xB1>(x);
  x = dpp_add<0x4E>(x);
  x = dpp_add<0x141>(x);
  x = dpp_add<0x140>(x);
  return sum_g(x);
}
__device__ __forceinline__ __amdgpu_buffer_rsrc_t mk_rsrc(const void* p, u32 bytes) {
  return __builtin_amdgcn_make_buffer_rsrc((void*)p, 0, bytes, 0x00020000);
}
__device__ __forceinline__ u32x4 bload16(__amdgpu_buffer_rsrc_t r, u32 off) { return __builtin_amdgcn_raw_buffer_load_b128(r, off, 0, 0); }
__device__ __forceinline__ u32x2 bload8(__amdgpu_buffer_rsrc_t r, u32 off) { return __builtin_amdgcn_raw_buffer_load_b64(r, off, 0, 0); }
template <bool SC1>
__device__ __forceinline__ void glds16(__amdgpu_buffer_rsrc_t r, u32 voff, u32 soff, u32 lds_dst) {
  if (SC1)
    asm volatile("s_mov_b32 m0, %0\n\ts_nop 0\n\tbuffer_load_dwordx4 %1, %2, %3 offen sc1 lds" ::"s"(lds_dst), "v"(voff), "s"(r), "s"(soff) : "memory");
  else
    asm volatile("s_mov_b32 m0, %0\n\ts_nop 0\n\tbuffer_load_dwordx4 %1, %2, %3 offen lds" ::"s"(lds_dst), "v"(voff), "s"(r), "s"(soff) : "memory");
}
__device__ __forceinline__ void bload16_asm(u32x4& d, __amdgpu_buffer_rsrc_t r, u32 voff, u32 soff) {
  asm volatile("buffer_load_dwordx4 %0, %1, %2, %3 offen" : "=v"(d) : "v"(voff), "s"(r), "s"(soff) : "memory");
}
#define K1_AUX 0
#define WT_AUX 16
#define K2_AUX 0
template <int AUX>
__device__ __forceinline__ void gstore16(char* base, size_t off, u32x4 v) {
  const __amdgpu_buffer_rsrc_t r = mk_rsrc(base, 0x7fffffffu);
  __builtin_amdgcn_raw_buffer_store_b128(v, r, (u32)off, 0, AUX);
}
template <int AUX>
__device__ __forceinline__ void gstore8(char* base, size_t off, u32x2 v) {
  const __amdgpu_buffer_rsrc_t r = mk_rsrc(base, 0x7fffffffu);
  __builtin_amdgcn_raw_buffer_store_b64(v, r, (u32)off, 0, AUX);
}
__device__ __forceinline__ u32x4 bload16s(__amdgpu_buffer_rsrc_t r, u32 voff, u32 soff) { return __builtin_amdgcn_raw_buffer_load_b128(r, voff, soff, 0); }
__device__ __forceinline__ u32x2 tr_read(const char* p) {
  s16x4 r = __builtin_amdgcn_ds_read_tr16_b64_v4i16((LAS s16x4*)p);
  return __builtin_bit_cast(u32x2, r);
}

__device__ __forceinline__ void code_prefetch(int part, int nparts, int lane) {
  u64 pc; u32 len;
  asm volatile("s_getpc_b64 %0\n\ts_mov_b32 %1, ffn_text_end-." : "=s"(pc), "=s"(len));
  const __amdgpu_buffer_rsrc_t r = mk_rsrc((const void*)pc, len);
  u32 acc = 0;
  for (u32 o = part * 1024; o < len; o += nparts * 1024) acc |= __builtin_amdgcn_raw_buffer_load_b128(r, o + lane * 16, 0, 0)[0];
  asm volatile("" ::"v"(acc));
}
__device__ __forceinline__ int fcol(int ks, int g, int j) {
  const int slot = 8 * (g >> 1) + j;
  if (ks < 40) { const int ct = ks / 5, p = ks - 5 * ct; return 128 * (2 * p + (g & 1)) + 8 * slot + ct; }
  return 1280 + 8 * slot + 2 * (ks - 40) + (g & 1);
}

__global__ __launch_bounds__(512, 2) void k1_prep(const u16* __restrict__ X, const u16* __restrict__ K, const u16* __restrict__ V,
                                                  const u16* __restrict__ SK, char* __restrict__ ws) {
  __shared__ __attribute__((aligned(16))) char sm[16384];
  const int b = blockIdx.x, t = threadIdx.x, lane = t & 63;
  const int w = __builtin_amdgcn_readfirstlane(t >> 6);
  const int dt = b & 7, st = b >> 3;
  if (w < 4) {
    const int rp = t >> 3, c = t & 7;
    const int s = st * 64 + 2 * rp, ch0 = dt * 64 + c * 8;
    const __amdgpu_buffer_rsrc_t rk = mk_rsrc(K, S * D * 2);
    const u32 off = (u32)(s * D + ch0) * 2;
    u32x4 kk[5];
#pragma unroll
    for (int i = 0; i < 5; i++) kk[i] = bload16(rk, off + (1 - i) * 1024);
    const u32x4 v0 = *(const u32x4*)((const char*)V + off), v1 = *(const u32x4*)((const char*)V + off + 1024);
    const u32x4 x0 = *(const u32x4*)((const char*)X + off), x1 = *(const u32x4*)((const char*)X + off + 1024);
    u32x4 wa[4];
#pragma unroll
    for (int i = 0; i < 4; i++) wa[i] = *(const u32x4*)(SK + ch0 * 4 + i * 8);
    SCHED_FENCE();
    const int swz = ((rp >> 2) ^ c) << 4;
    char* pk = sm + (8 * c) * 128 + swz + ((3 - (rp & 3)) << 2);
    char* px = sm + 8192 + (8 * c) * 128 + swz + ((rp & 3) << 2);
#pragma unroll
    for (int i = 0; i < 4; i++) {
      const u32 l01 = perm_lo(kk[0][i], kk[1][i]), l12 = perm_lo(kk[1][i], kk[2][i]), l23 = perm_lo(kk[2][i], kk[3][i]), l34 = perm_lo(kk[3][i], kk[4][i]);
      const u32 h01 = perm_hi(kk[0][i], kk[1][i]), h12 = perm_hi(kk[1][i], kk[2][i]), h23 = perm_hi(kk[2][i], kk[3][i]), h34 = perm_hi(kk[3][i], kk[4][i]);
      const float a1 = dot2(l01, wa[i][0], dot2(l23, wa[i][1], 0.f)) * bf_lo(v1[i]);
      const float a0 = dot2(l12, wa[i][0], dot2(l34, wa[i][1], 0.f)) * bf_lo(v0[i]);
      const float b1 = dot2(h01, wa[i][2], dot2(h23, wa[i][3], 0.f)) * bf_hi(v1[i]);
      const float b0 = dot2(h12, wa[i][2], dot2(h34, wa[i][3], 0.f)) * bf_hi(v0[i]);
      *(u32*)(pk + (2 * i) * 128) = pack_bf(a1, a0);
      *(u32*)(pk + (2 * i + 1) * 128) = pack_bf(b1, b0);
      *(u32*)(px + (2 * i) * 128) = perm_lo(x0[i], x1[i]);
      *(u32*)(px + (2 * i + 1) * 128) = perm_hi(x0[i], x1[i]);
    }
    __syncthreads();
#pragma unroll
    for (int i = 0; i < 2; i++) {
      const int u = t + 256 * i, ch = u >> 3, cc = u & 7;
      const u32x4 o = *(const u32x4*)(sm + ch * 128 + cc * 16);
      const int grp = cc ^ ((ch >> 3) & 7);
      const size_t go = (size_t)(dt * 64 + ch) * S + st * 64 + grp * 8;
      gstore16<K1_AUX>(ws, WS_KVT + go * 2, o);
      gstore16<K1_AUX>(ws, WS_XT + go * 2, *(const u32x4*)(sm + 8192 + ch * 128 + cc * 16));
    }
  } else {
    const int lrow = 256 * (b & 7) + 8 * (b >> 3) + 2 * (w - 4);
    u32x4 xr[2];
#pragma unroll
    for (int i = 0; i < 2; i++) xr[i] = *(const u32x4*)(X + (size_t)(lrow + i) * D + lane * 8);
    if (t == 256) __hip_atomic_store((GAS u32*)(ws + WS_FLAG) + b * 64, 0u, RLX_AGENT);
    code_prefetch((b >> 3) * 4 + (w - 4), 128, lane);
    __builtin_amdgcn_s_barrier();
#pragma unroll
    for (int q = 0; q < 2; q++) {
      float sum = 0.f;
#pragma unroll
      for (int i = 0; i < 4; i++) sum = dot2(xr[q][i], ONES, sum);
      const float mean = wave_sum(sum) * (1.f / D);
      float sq = 0.f;
#pragma unroll
      for (int i = 0; i < 4; i++) { const float d0 = bf_lo(xr[q][i]) - mean, d1 = bf_hi(xr[q][i]) - mean; sq += d0 * d0 + d1 * d1; }
      sq = wave_sum(sq);
      const float sd = rb(__builtin_amdgcn_sqrtf(rb(rb(sq * (1.f / D)) + 1e-5f)));
      const float r1 = __builtin_amdgcn_rcpf(sd);
      if (lane == 0) { f32x2 o2 = {r1, -rb(mean) * r1}; *(f32x2*)(ws + WS_ST1 + (size_t)(lrow + q) * 8) = o2; }
    }
  }
}

constexpr int HC_STRIDE = 4608;
constexpr int HC_CH = 8 * HC_STRIDE;
constexpr int U_CH = (240 + 2048) * 2;
constexpr int K2_H = 0, K2_U = 2 * HC_CH, K2_R = K2_U + 2 * U_CH + 32, K2_F = K2_R + 4 * 4096, K2_END = K2_F + 32;
__device__ __forceinline__ constexpr int hoff(int c) {
  return c == 0 ? 4 : c == 1 ? 2 : c == 2 ? 8 : c == 3 ? 3 : c == 4 ? 7 : c == 5 ? 0 : c == 6 ? 15 : 12;
}
__device__ __forceinline__ int hoff_rt(int c) { return (0xCF073824u >> (4 * c)) & 15; }
__device__ __forceinline__ void lds_wait(const char* p, u32 want) {
  for (u32 spins = 0; spins < (1u << 22); spins++) {
    if (__builtin_amdgcn_readfirstlane(*(volatile LAS u32*)p) == want) break;
    __builtin_amdgcn_s_sleep(1);
  }
  asm volatile("" ::: "memory");
}

__global__ __launch_bounds__(512, 2) void k2_conv(const u16* __restrict__ HK, const u16* __restrict__ DV,
                                                  const u16* __restrict__ W1, const u16* __restrict__ W2, char* __restrict__ ws) {
  __shared__ __attribute__((aligned(16))) char sm[K2_END];
  const int b = blockIdx.x, t = threadIdx.x, lane = t & 63;
  const int w = __builtin_amdgcn_readfirstlane(t >> 6);
  const int chp = 64 * (b & 7) + 2 * (b >> 3);
  const int wcl = (w >> 1) & 1, rh = w & 1;
  const int n = lane & 15, g = lane >> 4;
  {
    const int cl = t >> 8, j = t & 255;
    const __amdgpu_buffer_rsrc_t rh_ = mk_rsrc(HK + (size_t)chp * S, 2 * S * 2);
    const u32 ho = (u32)(cl * S + 8 * j) * 2;
    u32x4 ha, hb, uu;
    const __amdgpu_buffer_rsrc_t ru = mk_rsrc(ws + WS_KVT + (size_t)chp * S * 2, 2 * S * 2);
    bload16_asm(ha, rh_, ho - 16, 0);
    bload16_asm(hb, rh_, ho, 0);
    bload16_asm(uu, ru, ho, 0);
    asm volatile("s_waitcnt vmcnt(0)" : "+v"(ha), "+v"(hb), "+v"(uu)::"memory");
    if (t < 8) *(volatile LAS u32*)(sm + K2_F + t * 4) = 0u;
    SCHED_FENCE();
    if (j == 0) ha = u32x4{0, 0, 0, 0};
    char* hbase = sm + K2_H + cl * HC_CH;
    const u32 W[8] = {ha[0], ha[1], ha[2], ha[3], hb[0], hb[1], hb[2], hb[3]};
#pragma unroll
    for (int c = 0; c < 8; c++) {
      u32x4 o;
#pragma unroll
      for (int a = 0; a < 4; a++) o[a] = (c & 1) ? __builtin_amdgcn_alignbit(W[(c >> 1) + a + 1], W[(c >> 1) + a], 16) : W[(c >> 1) + a];
      *(u32x4*)(hbase + c * HC_STRIDE + 16 * (j + 1 + hoff(c))) = o;
    }
    const u32x4 z4 = {0, 0, 0, 0};
    if (j < 8) *(u32x4*)(hbase + j * HC_STRIDE + 16 * hoff_rt(j)) = z4;
    if (j == 255) *(u32x4*)(hbase + 16 * (257 + hoff(0))) = hb;
    char* ub = sm + K2_U + cl * U_CH;
    *(u32x4*)(ub + 480 + 16 * j) = uu;
    if (j < 30) *(u32x4*)(ub + 16 * j) = z4;
  }
  u32x2 xq[4];
  f32x4 sa[4], sb[4];
  u32 dvr = 0;
  if (w < 4) {
    const __amdgpu_buffer_rsrc_t rs1 = mk_rsrc(ws + WS_ST1, S * 8);
    const __amdgpu_buffer_rsrc_t rx = mk_rsrc(ws + WS_XT + (size_t)(chp + wcl) * S * 2, S * 2);
    const u32 p0 = 1024 * rh + 16 * n + 4 * g;
#pragma unroll
    for (int i = 0; i < 4; i++) {
      asm volatile("buffer_load_dwordx2 %0, %1, %2, %3 offen" : "=v"(xq[i]) : "v"(p0 * 2), "s"(rx), "s"(i * 512) : "memory");
      asm volatile("buffer_load_dwordx4 %0, %1, %2, %3 offen" : "=v"(sa[i]) : "v"(p0 * 8), "s"(rs1), "s"(i * 2048) : "memory");
      asm volatile("buffer_load_dwordx4 %0, %1, %2, %3 offen offset:16" : "=v"(sb[i]) : "v"(p0 * 8), "s"(rs1), "s"(i * 2048) : "memory");
    }
    dvr = *(const u32*)(DV + chp) >> (16 * wcl);
  }
  __syncthreads();
  if (w < 4) {
    f32x4 acc[8];
#pragma unroll
    for (int i = 0; i < 8; i++) acc[i] = f32x4{0.f, 0.f, 0.f, 0.f};
    const int idx0 = 25 + n - 8 * g;
    const int c = idx0 & 7;
    const char* fbase = sm + K2_H + wcl * HC_CH + c * HC_STRIDE + 16 * ((idx0 >> 3) + hoff_rt(c)) + 64 * 4 * rh;
    const char* gbase = sm + K2_U + wcl * U_CH + 2 * (16 * n + 8 * g + 224) - 64 * 4 * rh;
    u32x4 Ff[2][8], Gf[2][8];
#pragma unroll
    for (int a = 0; a < 8; a++) { Gf[0][a] = *(const u32x4*)(gbase + 64 * (8 * a)); Ff[0][a] = *(const u32x4*)(fbase + 64 * (8 * a)); }
    SCHED_FENCE();
#pragma unroll
    for (int rr = 0; rr < 4; rr++) {
      const int cur = rr & 1, nxt = cur ^ 1;
      int m = 0;
#pragma unroll
      for (int bb = 0; bb < 8; bb++) {
#pragma unroll
        for (int a = 0; a + bb < 8; a++) {
          acc[a + bb] = mfma(Ff[cur][a], Gf[cur][bb], acc[a + bb]);
          if (rr < 3 && (m & 1) == 1 && m < 32) {
            const int q = m >> 2;
            SCHED_FENCE();
            if (m & 2) Ff[nxt][q] = *(const u32x4*)(fbase + 64 * (8 * q + rr + 1));
            else Gf[nxt][q] = *(const u32x4*)(gbase + 64 * (8 * q - rr - 1));
            SCHED_FENCE();
          }
          m++;
        }
      }
      SCHED_FENCE();
    }
    f32x4* rbuf = (f32x4*)(sm + K2_R) + w * 256;
    if (rh) {
#pragma unroll
      for (int i = 0; i < 4; i++) rbuf[i * 64 + lane] = acc[i];
#pragma unroll
      for (int i = 0; i < 4; i++) acc[i] = acc[4 + i];
    } else {
#pragma unroll
      for (int i = 0; i < 4; i++) rbuf[i * 64 + lane] = acc[4 + i];
    }
    asm volatile("s_waitcnt lgkmcnt(0)" ::: "memory");
    if (lane == 0) *(volatile LAS u32*)(sm + K2_F + w * 4) = 1u;
    const int ch = chp + wcl;
    const char* ub = sm + K2_U + wcl * U_CH;
    u32x2 uq[4];
#pragma unroll
    for (int i = 0; i < 4; i++) uq[i] = *(const u32x2*)(ub + 2 * (240 + 256 * (4 * rh + i) + 16 * n + 8 * (g >> 1) + ((g & 1) ? 0 : 4)));
    asm volatile("s_waitcnt vmcnt(0)" : "+v"(xq[0]), "+v"(xq[1]), "+v"(xq[2]), "+v"(xq[3]), "+v"(sa[0]), "+v"(sa[1]), "+v"(sa[2]), "+v"(sa[3]),
                 "+v"(sb[0]), "+v"(sb[1]), "+v"(sb[2]), "+v"(sb[3])::"memory");
    lds_wait(sm + K2_F + (w ^ 1) * 4, 1u);
    const float dv = bf_lo(dvr);
    const f32x4* pbuf = (const f32x4*)(sm + K2_R) + (w ^ 1) * 256;
#pragma unroll
    for (int i = 0; i < 4; i++) {
      const int T = 4 * rh + i;
      const f32x4 a = acc[i] + pbuf[i * 64 + lane];
      const int pos = 256 * T + 16 * n + 4 * g;
      const float y0 = fmaf(bf_hi(uq[i][1]), dv, a[0]), y1 = fmaf(bf_lo(uq[i][1]), dv, a[1]), y2 = fmaf(bf_hi(uq[i][0]), dv, a[2]), y3 = fmaf(bf_lo(uq[i][0]), dv, a[3]);
      const float x0 = bf_lo(xq[i][0]), x1 = bf_hi(xq[i][0]), x2 = bf_lo(xq[i][1]), x3 = bf_hi(xq[i][1]);
      const float h0 = fmaf(fmaf(x0, sa[i][0], sa[i][1]), y0, x0), h1 = fmaf(fmaf(x1, sa[i][2], sa[i][3]), y1, x1);
      const float h2 = fmaf(fmaf(x2, sb[i][0], sb[i][1]), y2, x2), h3 = fmaf(fmaf(x3, sb[i][2], sb[i][3]), y3, x3);
      const u32x2 o = {pack_bf(h0, h1), pack_bf(h2, h3)};
      gstore8<K2_AUX>(ws, WS_HT + ((size_t)ch * S + pos) * 2, o);
    }
  } else {
    const int unit = b + 256 * (w - 4);
    const int lc = lane & 15, lg = lane >> 4;
    if (unit < 176) {
      const __amdgpu_buffer_rsrc_t rw = mk_rsrc(W1, D * F * 2);
      const int blk = unit >> 4, ks = unit & 15;
      const int col = 128 * blk + 8 * lc;
      const u32 o0 = col < F ? (u32)((32 * ks + 8 * lg) * F + col) * 2 : 0x80000000u;
      u32x4 wl[8];
#pragma unroll
      for (int j = 0; j < 8; j++) wl[j] = bload16(rw, o0 + j * F * 2);
#pragma unroll
      for (int i = 0; i < 8; i++) {
        u32x4 o;
#pragma unroll
        for (int a = 0; a < 4; a++) o[a] = (i & 1) ? perm_hi(wl[2 * a][i >> 1], wl[2 * a + 1][i >> 1]) : perm_lo(wl[2 * a][i >> 1], wl[2 * a + 1][i >> 1]);
        gstore16<WT_AUX>(ws, WS_W1T + ((size_t)((i * 11 + blk) * 16 + ks) * 64 + lane) * 16, o);
        const float cs = sum_g(dot2(o[0], ONES, dot2(o[1], ONES, dot2(o[2], ONES, dot2(o[3], ONES, 0.f)))));
        if (lane < 16) *(float*)(ws + WS_CSP + ((size_t)(i * 176 + blk * 16 + lane) * 16 + ks) * 4) = cs;
      }
    } else if (unit < 528) {
      const __amdgpu_buffer_rsrc_t rw = mk_rsrc(W2, D * F * 2);
      const int u2 = unit - 176, ks = u2 >> 3, c2 = u2 & 7;
      u32 wx[8], wy[8];
#pragma unroll
      for (int j = 0; j < 8; j++) {
        const u32 o = (u32)(fcol(ks, lg, j) * D + 64 * c2 + 8 * (lc >> 2) + 2 * (lc & 3)) * 2;
        wx[j] = __builtin_amdgcn_raw_buffer_load_b32(rw, o, 0, 0);
        wy[j] = __builtin_amdgcn_raw_buffer_load_b32(rw, o + 64, 0, 0);
      }
#pragma unroll
      for (int i = 0; i < 4; i++) {
        u32x4 o;
#pragma unroll
        for (int a = 0; a < 4; a++) {
          const u32 e0 = (i >> 1) ? wy[2 * a] : wx[2 * a], e1 = (i >> 1) ? wy[2 * a + 1] : wx[2 * a + 1];
          o[a] = (i & 1) ? perm_hi(e0, e1) : perm_lo(e0, e1);
        }
        gstore16<WT_AUX>(ws, WS_W2T + ((size_t)(((c2 * 2 + (i >> 1)) * 44 + ks) * 2 + (i & 1)) * 64 + lane) * 16, o);
      }
    }
  }
}

constexpr int L_HT = 0;
constexpr int L_ST = 147456;
constexpr int L_CS = L_ST + 512;
constexpr int L_RES = L_CS + 768;
constexpr int L_END = L_RES + 8192;
constexpr int L_RED = 0;
constexpr int L_FLG = L_END;
constexpr int L_TOT = L_END + 16;
#define W1_PRE 4
#define W2_LOADS                                                                                                                   \
  "buffer_load_dwordx4 %0, %14, %15, %16 offen\n\tbuffer_load_dwordx4 %1, %14, %15, %16 offen offset:1024\n\t"                      \
  "buffer_load_dwordx4 %2, %14, %15, %16 offen offset:2048\n\tbuffer_load_dwordx4 %3, %14, %15, %16 offen offset:3072\n\t"          \
  "buffer_load_dwordx4 %4, %14, %15, %17 offen\n\tbuffer_load_dwordx4 %5, %14, %15, %17 offen offset:1024\n\t"                      \
  "buffer_load_dwordx4 %6, %14, %15, %17 offen offset:2048\n\tbuffer_load_dwordx4 %7, %14, %15, %17 offen offset:3072\n\t"          \
  "buffer_load_dwordx4 %8, %14, %15, %18 offen\n\tbuffer_load_dwordx4 %9, %14, %15, %18 offen offset:1024\n\t"                      \
  "buffer_load_dwordx4 %10, %14, %15, %18 offen offset:2048\n\tbuffer_load_dwordx4 %11, %14, %15, %18 offen offset:3072\n\t"        \
  "buffer_load_dwordx4 %12, %14, %15, %19 offen\n\tbuffer_load_dwordx4 %13, %14, %15, %19 offen offset:1024\n"
__device__ __forceinline__ void load_w2_tail_support(u32x4 (&w2)[22], __amdgpu_buffer_rsrc_t r, u32 voff, int w) {
  asm volatile("s_cmp_lt_u32 %20, 4\n\ts_cbranch_scc1 1f\n\t" W2_LOADS "1:"
               : "=&v"(w2[8]), "=&v"(w2[9]), "=&v"(w2[10]), "=&v"(w2[11]), "=&v"(w2[12]), "=&v"(w2[13]), "=&v"(w2[14]), "=&v"(w2[15]), "=&v"(w2[16]),
                 "=&v"(w2[17]), "=&v"(w2[18]), "=&v"(w2[19]), "=&v"(w2[20]), "=&v"(w2[21])
               : "v"(voff), "s"(r), "s"(8192), "s"(12288), "s"(16384), "s"(20480), "s"(w) : "memory", "scc");
}
__device__ __forceinline__ void load_w2_tail_main(u32x4 (&w2)[22], __amdgpu_buffer_rsrc_t r, u32 voff, int w) {
  asm volatile("s_cmp_lt_u32 %20, 4\n\ts_cbranch_scc0 1f\n\t" W2_LOADS "1:"
               : "+v"(w2[8]), "+v"(w2[9]), "+v"(w2[10]), "+v"(w2[11]), "+v"(w2[12]), "+v"(w2[13]), "+v"(w2[14]), "+v"(w2[15]), "+v"(w2[16]),
                 "+v"(w2[17]), "+v"(w2[18]), "+v"(w2[19]), "+v"(w2[20]), "+v"(w2[21])
               : "v"(voff), "s"(r), "s"(8192), "s"(12288), "s"(16384), "s"(20480), "s"(w) : "memory", "scc");
}
__device__ __forceinline__ float silu_val(float acc, float rs, float bias) {
  const float tt = fmaf(acc, rs, bias);
  const float e = __builtin_amdgcn_exp2f(tt);
  return tt * __builtin_amdgcn_rcpf(fmaf(e, -1.44269504089f, -1.44269504089f));
}

__global__ __launch_bounds__(512, 2) void k34_ffn(char* __restrict__ ws, u16* __restrict__ OUT) {
  __shared__ __attribute__((aligned(16))) char sm[L_TOT];
  const int b = blockIdx.x, t = threadIdx.x, lane = t & 63;
  const int w = __builtin_amdgcn_readfirstlane(t >> 6);
  const int rt = 8 * (b & 3) + ((b >> 3) & 7), c = 4 * ((b >> 2) & 1) + (b >> 6);
  const int n = lane & 15, g = lane >> 4;
  const int row0 = 64 * rt;
  {
    const __amdgpu_buffer_rsrc_t rh = mk_rsrc(ws + WS_HT + ((size_t)(64 * w) * S + row0) * 2, 64 * S * 2);
    const int f0 = 2 * ((lane >> 4) & 1);
    const u32 v0 = (u32)(lane >> 3) * S * 2 + (((lane & 7) ^ f0) << 4), v1 = (u32)(lane >> 3) * S * 2 + (((lane & 7) ^ (f0 + 4)) << 4);
#pragma unroll
    for (int i = 0; i < 8; i++) glds16<false>(rh, (i & 1) ? v1 : v0, i * 8 * S * 2, (u32)(size_t)(sm + L_HT) + (64 * w + 8 * i) * 128);
  }
  if (t < 4) *(volatile LAS u32*)(sm + L_FLG + t * 4) = 0u;
  __builtin_amdgcn_s_barrier();
  const __amdgpu_buffer_rsrc_t rw2 = mk_rsrc(ws + WS_W2T + (size_t)((c * 2 + (w & 1)) * 44 + 11 * (w >> 1)) * 2048, 22 * 1024);
  const int q4 = (lane & 15) >> 2, p4 = lane & 3;
  const int fsw = 2 * ((q4 >> 1) & 1) + 4 * (g & 1);
  auto save_residual = [&]() {
    const char* p = sm + L_HT + (8 * g + q4) * 128 + (((2 * (w >> 1) + (p4 >> 1)) ^ fsw) << 4) + 8 * (p4 & 1) + (2 * c + (w & 1)) * 4096;
    const u32x2 a = tr_read(p), bq = tr_read(p + 512);
    *(u32x4*)(sm + L_RES + (w * 64 + lane) * 16) = u32x4{a[0], a[1], bq[0], bq[1]};
  };
  if (w >= 4) {
    f32x4 csp[4];
    const __amdgpu_buffer_rsrc_t rc = mk_rsrc(ws + WS_CSP + (size_t)c * 176 * 64, 176 * 64);
    const int cj = (w - 4) * 64 + lane;
#pragma unroll
    for (int i = 0; i < 4; i++) csp[i] = __builtin_bit_cast(f32x4, bload16s(rc, (u32)(cj < 176 ? cj : 175) * 64, i * 16));
    asm volatile("s_waitcnt vmcnt(4) lgkmcnt(0)" ::: "memory");
    __builtin_amdgcn_s_barrier();
    __builtin_amdgcn_s_setprio(3);
    const char* p = sm + L_HT + (8 * g + q4) * 128 + (((2 * (w - 4) + (p4 >> 1)) ^ fsw) << 4) + 8 * (p4 & 1);
    u32x2 fa[16][2];
#pragma unroll
    for (int ks = 0; ks < 16; ks++) { fa[ks][0] = tr_read(p + ks * 4096); fa[ks][1] = tr_read(p + ks * 4096 + 512); }
    f32x4 q0 = {0.f, 0.f, 0.f, 0.f}, q1 = {0.f, 0.f, 0.f, 0.f};
    const u32x4 ones = {ONES, ONES, ONES, ONES};
#pragma unroll
    for (int ks = 0; ks < 16; ks++) {
      const u32x4 f0 = {fa[ks][0][0], fa[ks][0][1], fa[ks][1][0], fa[ks][1][1]};
      q0 = mfma(f0, f0, q0);
      q1 = mfma(ones, f0, q1);
    }
    {
      const float lo = (n & 1) ? q0[1] : q0[0], hi = (n & 1) ? q0[3] : q0[2];
      const float q = (n & 2) ? hi : lo;
      const float mu = q1[0] * (1.f / D);
      const float var = fmaxf(q * (1.f / D) - mu * mu, 0.f);
      const float rstd = __builtin_amdgcn_rcpf(rb(__builtin_amdgcn_sqrtf(rb(rb(var) + 1e-5f))));
      if (g == (n >> 2)) *(f32x2*)(sm + L_ST + ((w - 4) * 16 + n) * 8) = f32x2{-1.44269504089f * rstd, rb(mu) * rstd * 1.44269504089f};
    }
    {
      const f32x4 s4 = (csp[0] + csp[1]) + (csp[2] + csp[3]);
      if (cj < 192) *(float*)(sm + L_CS + cj * 4) = (s4[0] + s4[1]) + (s4[2] + s4[3]);
    }
    save_residual();
    asm volatile("s_waitcnt lgkmcnt(0)" ::: "memory");
    if (lane == 0) __hip_atomic_fetch_add((LAS u32*)(sm + L_FLG + 4), 1u, __ATOMIC_RELAXED, __HIP_MEMORY_SCOPE_WORKGROUP);
    __builtin_amdgcn_s_setprio(0);
    lds_wait(sm + L_FLG + 8, 1u);
  } else {
    constexpr int NT = 3, NW1 = 48, PRE = W1_PRE * NT;
    u32x4 w1[NW1];
    const __amdgpu_buffer_rsrc_t rw1 = mk_rsrc(ws + WS_W1T + (size_t)(c * 11 + 3 * w) * 16 * 1024, 48 * 1024);
    auto w1load = [&](int k) { w1[k] = bload16s(rw1, lane * 16, (k % NT) * 16384 + (k / NT) * 1024); };
    auto issued = [&](int ks) constexpr { const int v = PRE + NT * (ks + 1); return v < NW1 ? v : NW1; };
#pragma unroll
    for (int i = 0; i < PRE; i++) w1load(i);
    asm volatile("s_waitcnt vmcnt(%0) lgkmcnt(0)" ::"n"(PRE) : "memory");
    __builtin_amdgcn_s_barrier();
    const char* tb[4];
#pragma unroll
    for (int m = 0; m < 4; m++) tb[m] = sm + L_HT + (8 * g + q4) * 128 + (((2 * m + (p4 >> 1)) ^ fsw) << 4) + 8 * (p4 & 1);
    f32x4 acc[NT][4];
    u32x4 hb[3][4];
#pragma unroll
    for (int i = 0; i < NT; i++)
#pragma unroll
      for (int m = 0; m < 4; m++) acc[i][m] = f32x4{0.f, 0.f, 0.f, 0.f};
#pragma unroll
    for (int d = 0; d < 2; d++)
#pragma unroll
      for (int m = 0; m < 4; m++) {
        const u32x2 a = tr_read(tb[m] + d * 4096), bq = tr_read(tb[m] + d * 4096 + 512);
        hb[d][m] = u32x4{a[0], a[1], bq[0], bq[1]};
      }
#pragma unroll
    for (int ks = 0; ks < 16; ks++) {
#pragma unroll
      for (int m = 0; m < 4; m++) {
        acc[0][m] = mfma(w1[ks * NT], hb[ks % 3][m], acc[0][m]);
        SCHED_FENCE();
        if (ks + 2 < 16) { const u32x2 a = tr_read(tb[m] + (ks + 2) * 4096); hb[(ks + 2) % 3][m][0] = a[0]; hb[(ks + 2) % 3][m][1] = a[1]; }
        SCHED_FENCE();
        acc[1][m] = mfma(w1[ks * NT + 1], hb[ks % 3][m], acc[1][m]);
        SCHED_FENCE();
        if (ks + 2 < 16) { const u32x2 a = tr_read(tb[m] + (ks + 2) * 4096 + 512); hb[(ks + 2) % 3][m][2] = a[0]; hb[(ks + 2) % 3][m][3] = a[1]; }
        SCHED_FENCE();
        acc[2][m] = mfma(w1[ks * NT + 2], hb[ks % 3][m], acc[2][m]);
        SCHED_FENCE();
        if (m < NT && issued(ks - 1) + m < issued(ks)) w1load(issued(ks - 1) + m);
        SCHED_FENCE();
      }
      if (ks == 15 - W1_PRE && w == 0 && lane == 0) *(volatile LAS u32*)(sm + L_FLG + 8) = 1u;
    }
    save_residual();
    lds_wait(sm + L_FLG + 4, 4u);
    float rs[4], mr[4];
    f32x4 cs[NT];
#pragma unroll
    for (int m = 0; m < 4; m++) { const f32x2 v = *(const f32x2*)(sm + L_ST + (m * 16 + n) * 8); rs[m] = v[0]; mr[m] = v[1]; }
#pragma unroll
    for (int i = 0; i < NT; i++) cs[i] = *(const f32x4*)(sm + L_CS + ((3 * w + i) * 16 + 4 * g) * 4);
    const __amdgpu_buffer_rsrc_t hrs = mk_rsrc(ws + WS_HF + (size_t)rt * 44 * 4096, 44 * 4096);
    u32 o_t[NT];
#pragma unroll
    for (int i = 0; i < NT; i++) {
      const int tl = 3 * w + i;
      const int ks2 = tl < 10 ? 5 * c + (tl >> 1) : 40 + (c >> 1), par = tl < 10 ? (tl & 1) : (c & 1);
      o_t[i] = (tl < 11 && (g & 1) == par) ? (u32)(ks2 * 4096 + lane * 16) : 0x40000000u;
    }
#pragma unroll
    for (int m = 0; m < 4; m++) {
#pragma unroll
      for (int i = 0; i < NT; i++) {
        float h1[4];
#pragma unroll
        for (int r = 0; r < 4; r++) h1[r] = silu_val(acc[i][m][r], rs[m], mr[m] * cs[i][r]);
        const u32 p0 = pack_bf(h1[0], h1[1]), p1 = pack_bf(h1[2], h1[3]);
        const auto s0 = __builtin_amdgcn_permlane16_swap(p0, p0, false, false), s1 = __builtin_amdgcn_permlane16_swap(p1, p1, false, false);
        __builtin_amdgcn_raw_buffer_store_b128(u32x4{s0[0], s1[0], s0[1], s1[1]}, hrs, o_t[i] + m * 1024, 0, 16);
      }
      SCHED_FENCE();
    }
  }
  u32x4 w2[22];
#pragma unroll
  for (int i = 0; i < 8; i++) bload16_asm(w2[i], rw2, lane * 16, i * 1024);
  load_w2_tail_support(w2, rw2, lane * 16, w);
  asm volatile("s_cmp_lt_u32 %0, 4\n\ts_cbranch_scc0 1f\n\ts_waitcnt vmcnt(0)\n1:\n\ts_waitcnt lgkmcnt(0)" ::"s"(w) : "memory", "scc");
  __builtin_amdgcn_s_barrier();
  if (w == 7) {
    GAS u32* flags = (GAS u32*)(ws + WS_FLAG);
    if (lane == 0) __hip_atomic_store(flags + b * 64, 1u, RLX_AGENT);
    const int pb = (b & 3) + (b & 56) + 4 * ((lane >> 2) & 1) + 64 * (lane & 3);
    for (u32 spins = 0; spins < (1u << 20); spins++) {
      const u32 v = __hip_atomic_load(flags + pb * 64, RLX_AGENT);
      if (__all(v == 1u)) break;
      __builtin_amdgcn_s_sleep(1);
    }
  }
  if (w < 4) __builtin_amdgcn_s_sleep(2);
  load_w2_tail_main(w2, rw2, lane * 16, w);
  __builtin_amdgcn_s_barrier();

  const int dh = w & 1, kq = w >> 1;
  const __amdgpu_buffer_rsrc_t rhf = mk_rsrc(ws + WS_HF + (size_t)rt * 44 * 4096 + (size_t)(11 * kq * 4 + 2 * dh) * 1024, 11 * 4096);
  const u32 ldst = (u32)(size_t)sm + (kq * 4 + 2 * dh) * 1024;
  auto stage1 = [&](int s, int e) {
    const int slot = s < 9 ? s : s - 9;
    glds16<true>(rhf, lane * 16, s * 4096 + e * 1024, ldst + slot * 16384 + e * 1024);
  };
  auto stage = [&](int s) { stage1(s, 0); stage1(s, 1); };
  stage(0); stage(1); stage(2); stage(3);
  f32x4 acc2[2][4];
#pragma unroll
  for (int i = 0; i < 2; i++)
#pragma unroll
    for (int m = 0; m < 4; m++) acc2[i][m] = f32x4{0.f, 0.f, 0.f, 0.f};
  asm volatile("s_waitcnt vmcnt(4)"
               : "+v"(w2[0]), "+v"(w2[1]), "+v"(w2[2]), "+v"(w2[3]), "+v"(w2[4]), "+v"(w2[5]), "+v"(w2[6]), "+v"(w2[7]), "+v"(w2[8]), "+v"(w2[9]), "+v"(w2[10]),
                 "+v"(w2[11]), "+v"(w2[12]), "+v"(w2[13]), "+v"(w2[14]), "+v"(w2[15]), "+v"(w2[16]), "+v"(w2[17]), "+v"(w2[18]), "+v"(w2[19]), "+v"(w2[20]), "+v"(w2[21])
               ::"memory");
  __builtin_amdgcn_s_barrier();
  {
    const char* base = sm + kq * 4096 + lane * 16;
    u32x4 hb[2][4];
#pragma unroll
    for (int m = 0; m < 4; m++) hb[0][m] = *(const u32x4*)(base + m * 1024);
#pragma unroll
    for (int s = 0; s < 11; s++) {
      if (s == 1) { asm volatile("s_waitcnt vmcnt(4)" ::: "memory"); __builtin_amdgcn_s_barrier(); }
      else if (s == 3 || s == 5) { asm volatile("s_waitcnt vmcnt(6)" ::: "memory"); __builtin_amdgcn_s_barrier(); }
      else if (s == 7) { asm volatile("s_waitcnt vmcnt(2)" ::: "memory"); __builtin_amdgcn_s_barrier(); }
      else if (s == 9) { asm volatile("s_waitcnt vmcnt(0)" ::: "memory"); __builtin_amdgcn_s_barrier(); }
      const int slot = s + 1 < 9 ? s + 1 : s + 1 - 9;
#pragma unroll
      for (int m = 0; m < 4; m++) {
        acc2[0][m] = mfma(w2[2 * s], hb[s & 1][m], acc2[0][m]);
        SCHED_FENCE();
        if (s < 10) hb[(s + 1) & 1][m] = *(const u32x4*)(base + slot * 16384 + m * 1024);
        SCHED_FENCE();
        acc2[1][m] = mfma(w2[2 * s + 1], hb[s & 1][m], acc2[1][m]);
        SCHED_FENCE();
        if (s < 2) stage1(4 + 2 * s + (m >> 1), m & 1);
        else if (s < 5 && (m & 1)) stage1(s + 6, m >> 1);
        SCHED_FENCE();
      }
    }
  }
  asm volatile("s_waitcnt lgkmcnt(0)" ::: "memory");
  __builtin_amdgcn_s_barrier();
  {
    char* red = sm + L_RED + dh * 32768;
    f32x4 o0, o1;
    auto scatter = [&](auto KQ) {
      constexpr int k = decltype(KQ)::value;
#pragma unroll
      for (int m = 0; m < 4; m++) {
        if (m != k) {
#pragma unroll
          for (int i = 0; i < 2; i++) *(f32x4*)(red + (((k * 4 + m) * 2 + i) * 64 + lane) * 16) = acc2[i][m];
        }
      }
      o0 = acc2[0][k]; o1 = acc2[1][k];
      asm volatile("" : "+v"(o0), "+v"(o1));
    };
    if (kq == 0) scatter(IntC<0>{}); else if (kq == 1) scatter(IntC<1>{}); else if (kq == 2) scatter(IntC<2>{}); else scatter(IntC<3>{});
    __syncthreads();
#pragma unroll
    for (int j = 1; j < 4; j++) {
      const int src = (kq + j) & 3;
      o0 += *(const f32x4*)(red + (((src * 4 + kq) * 2 + 0) * 64 + lane) * 16);
      o1 += *(const f32x4*)(red + (((src * 4 + kq) * 2 + 1) * 64 + lane) * 16);
    }
    const u32x4 hres = *(const u32x4*)(sm + L_RES + (w * 64 + lane) * 16);
    u32x4 res;
#pragma unroll
    for (int r = 0; r < 4; r++) res[r] = pack_bf(bf_lo(hres[r]) + rb(o0[r]), bf_hi(hres[r]) + rb(o1[r]));
    *(u32x4*)(OUT + (size_t)(row0 + 16 * kq + n) * D + 64 * c + 32 * dh + 8 * g) = res;
  }
  asm volatile("ffn_text_end:");
}

extern "C" void kernel_launch(void* const* d_in, const int* in_sizes, int n_in, void* d_out, int out_size, void* d_ws,
                              size_t ws_size, hipStream_t stream) {
  const u16* X = (const u16*)d_in[0];
  const u16* K = (const u16*)d_in[1];
  const u16* V = (const u16*)d_in[2];
  const u16* SK = (const u16*)d_in[3];
  const u16* HK = (const u16*)d_in[4];
  const u16* DV = (const u16*)d_in[5];
  const u16* W1 = (const u16*)d_in[7];
  const u16* W2 = (const u16*)d_in[8];
  char* ws = (char*)d_ws;
  k1_prep<<<256, 512, 0, stream>>>(X, K, V, SK, ws);
  k2_conv<<<256, 512, 0, stream>>>(HK, DV, W1, W2, ws);
  k34_ffn<<<256, 512, 0, stream>>>(ws, (u16*)d_out);
}
```

```cpp
#include <hip/hip_runtime.h>
#include <stdint.h>

typedef unsigned short u16;
typedef unsigned int u32;
typedef unsigned long long u64;
typedef __bf16 bf16x8 __attribute__((ext_vector_type(8)));
typedef __bf16 bf16x2 __attribute__((ext_vector_type(2)));
typedef short s16x4 __attribute__((ext_vector_type(4)));
typedef float f32x4 __attribute__((ext_vector_type(4)));
typedef float f32x2 __attribute__((ext_vector_type(2)));
typedef u32 u32x4 __attribute__((ext_vector_type(4)));
typedef u32 u32x2 __attribute__((ext_vector_type(2)));
#define GAS __attribute__((address_space(1)))
#define LAS __attribute__((address_space(3)))
#define RLX_AGENT __ATOMIC_RELAXED, __HIP_MEMORY_SCOPE_AGENT
#define SCHED_FENCE() __builtin_amdgcn_sched_barrier(0)

constexpr int S = 2048, D = 512, F = 1376;

constexpr size_t WS_FLAG = 0;
constexpr size_t WS_KVT  = 1 << 16;
constexpr size_t WS_XT   = WS_KVT + (size_t)D * S * 2;
constexpr size_t WS_HT   = WS_XT + (size_t)D * S * 2;
constexpr size_t WS_W1T  = WS_HT + (size_t)D * S * 2;
constexpr size_t WS_W2T  = WS_W1T + 8 * 11 * 16 * 1024;
constexpr size_t WS_CSP  = WS_W2T + 8 * 4 * 44 * 1024;
constexpr size_t WS_ST1  = WS_CSP + 16 * 8 * 11 * 16 * 4;
constexpr size_t WS_HF   = WS_ST1 + S * 8;
constexpr size_t WS_END  = WS_HF + (size_t)32 * 44 * 4 * 1024;

__device__ __forceinline__ float bf_lo(u32 v) { return __uint_as_float(v << 16); }
__device__ __forceinline__ float bf_hi(u32 v) { return __uint_as_float(v & 0xffff0000u); }
__device__ __forceinline__ u32 pack_bf(float a, float b) {
  f32x2 z = {a, b};
  return __builtin_bit_cast(u32, __builtin_convertvector(z, bf16x2));
}
__device__ __forceinline__ float rb(float a) { return bf_lo(pack_bf(a, 0.f)); }
__device__ __forceinline__ float dot2(u32 a, u32 b, float c) {
  return __builtin_amdgcn_fdot2_f32_bf16(__builtin_bit_cast(bf16x2, a), __builtin_bit_cast(bf16x2, b), c, false);
}
__device__ __forceinline__ u32 perm_lo(u32 a, u32 b) { return __builtin_amdgcn_perm(b, a, 0x05040100u); }
__device__ __forceinline__ u32 perm_hi(u32 a, u32 b) { return __builtin_amdgcn_perm(b, a, 0x07060302u); }
__device__ __forceinline__ f32x4 mfma(u32x4 a, u32x4 b, f32x4 c) {
  return __builtin_amdgcn_mfma_f32_16x16x32_bf16(__builtin_bit_cast(bf16x8, a), __builtin_bit_cast(bf16x8, b), c, 0, 0, 0);
}
constexpr u32 ONES = 0x3F803F80u;
template <int V> struct IntC { static constexpr int value = V; };
__device__ __forceinline__ float sum_g(float x) {
  auto s = __builtin_amdgcn_permlane16_swap(__float_as_uint(x), __float_as_uint(x), false, false);
  x = __uint_as_float(s[0]) + __uint_as_float(s[1]);
  auto t = __builtin_amdgcn_permlane32_swap(__float_as_uint(x), __float_as_uint(x), false, false);
  return __uint_as_float(t[0]) + __uint_as_float(t[1]);
}
template <int CTRL>
__device__ __forceinline__ float dpp_add(float x) {
  return x + __uint_as_float(__builtin_amdgcn_update_dpp(0u, __float_as_uint(x), CTRL, 0xf, 0xf, true));
}
__device__ __forceinline__ float wave_sum(float x) {
  x = dpp_add<0xB1>(x);
  x = dpp_add<0x4E>(x);
  x = dpp_add<0x141>(x);
  x = dpp_add<0x140>(x);
  return sum_g(x);
}
__device__ __forceinline__ __amdgpu_buffer_rsrc_t mk_rsrc(const void* p, u32 bytes) {
  return __builtin_amdgcn_make_buffer_rsrc((void*)p, 0, bytes, 0x00020000);
}
__device__ __forceinline__ u32x4 bload16(__amdgpu_buffer_rsrc_t r, u32 off) { return __builtin_amdgcn_raw_buffer_load_b128(r, off, 0, 0); }
__device__ __forceinline__ u32x2 bload8(__amdgpu_buffer_rsrc_t r, u32 off) { return __builtin_amdgcn_raw_buffer_load_b64(r, off, 0, 0); }
template <bool SC1>
__device__ __forceinline__ void glds16(__amdgpu_buffer_rsrc_t r, u32 voff, u32 soff, u32 lds_dst) {
  if (SC1)
    asm volatile("s_mov_b32 m0, %0\n\ts_nop 0\n\tbuffer_load_dwordx4 %1, %2, %3 offen sc1 lds" ::"s"(lds_dst), "v"(voff), "s"(r), "s"(soff) : "memory");
  else
    asm volatile("s_mov_b32 m0, %0\n\ts_nop 0\n\tbuffer_load_dwordx4 %1, %2, %3 offen lds" ::"s"(lds_dst), "v"(voff), "s"(r), "s"(soff) : "memory");
}
__device__ __forceinline__ void bload16_asm(u32x4& d, __amdgpu_buffer_rsrc_t r, u32 voff, u32 soff) {
  asm volatile("buffer_load_dwordx4 %0, %1, %2, %3 offen" : "=v"(d) : "v"(voff), "s"(r), "s"(soff) : "memory");
}
#define K1_AUX 0
#define WT_AUX 16
#define K2_AUX 0
template <int AUX>
__device__ __forceinline__ void gstore16(char* base, size_t off, u32x4 v) {
  const __amdgpu_buffer_rsrc_t r = mk_rsrc(base, 0x7fffffffu);
  __builtin_amdgcn_raw_buffer_store_b128(v, r, (u32)off, 0, AUX);
}
template <int AUX>
__device__ __forceinline__ void gstore8(char* base, size_t off, u32x2 v) {
  const __amdgpu_buffer_rsrc_t r = mk_rsrc(base, 0x7fffffffu);
  __builtin_amdgcn_raw_buffer_store_b64(v, r, (u32)off, 0, AUX);
}
__device__ __forceinline__ u32x4 bload16s(__amdgpu_buffer_rsrc_t r, u32 voff, u32 soff) { return __builtin_amdgcn_raw_buffer_load_b128(r, voff, soff, 0); }
__device__ __forceinline__ u32x2 tr_read(const char* p) {
  s16x4 r = __builtin_amdgcn_ds_read_tr16_b64_v4i16((LAS s16x4*)p);
  return __builtin_bit_cast(u32x2, r);
}

__device__ __forceinline__ void code_prefetch(int part, int nparts, int lane) {
  u64 pc; u32 len;
  asm volatile("s_getpc_b64 %0\n\ts_mov_b32 %1, ffn_text_end-." : "=s"(pc), "=s"(len));
  const __amdgpu_buffer_rsrc_t r = mk_rsrc((const void*)pc, len);
  u32 acc = 0;
  for (u32 o = part * 1024; o < len; o += nparts * 1024) acc |= __builtin_amdgcn_raw_buffer_load_b128(r, o + lane * 16, 0, 0)[0];
  asm volatile("" ::"v"(acc));
}
__device__ __forceinline__ int fcol(int ks, int g, int j) {
  const int slot = 8 * (g >> 1) + j;
  if (ks < 40) { const int ct = ks / 5, p = ks - 5 * ct; return 128 * (2 * p + (g & 1)) + 8 * slot + ct; }
  return 1280 + 8 * slot + 2 * (ks - 40) + (g & 1);
}

__global__ __launch_bounds__(512, 2) void k1_prep(const u16* __restrict__ X, const u16* __restrict__ K, const u16* __restrict__ V,
                                                  const u16* __restrict__ SK, char* __restrict__ ws) {
  __shared__ __attribute__((aligned(16))) char sm[16384];
  const int b = blockIdx.x, t = threadIdx.x, lane = t & 63;
  const int w = __builtin_amdgcn_readfirstlane(t >> 6);
  const int dt = b & 7, st = b >> 3;
  if (w < 4) {
    const int rp = t >> 3, c = t & 7;
    const int s = st * 64 + 2 * rp, ch0 = dt * 64 + c * 8;
    const __amdgpu_buffer_rsrc_t rk = mk_rsrc(K, S * D * 2);
    const u32 off = (u32)(s * D + ch0) * 2;
    u32x4 kk[5];
#pragma unroll
    for (int i = 0; i < 5; i++) kk[i] = bload16(rk, off + (1 - i) * 1024);
    const u32x4 v0 = *(const u32x4*)((const char*)V + off), v1 = *(const u32x4*)((const char*)V + off + 1024);
    const u32x4 x0 = *(const u32x4*)((const char*)X + off), x1 = *(const u32x4*)((const char*)X + off + 1024);
    u32x4 wa[4];
#pragma unroll
    for (int i = 0; i < 4; i++) wa[i] = *(const u32x4*)(SK + ch0 * 4 + i * 8);
    SCHED_FENCE();
    const int swz = ((rp >> 2) ^ c) << 4;
    char* pk = sm + (8 * c) * 128 + swz + ((3 - (rp & 3)) << 2);
    char* px = sm + 8192 + (8 * c) * 128 + swz + ((rp & 3) << 2);
#pragma unroll
    for (int i = 0; i < 4; i++) {
      const u32 l01 = perm_lo(kk[0][i], kk[1][i]), l12 = perm_lo(kk[1][i], kk[2][i]), l23 = perm_lo(kk[2][i], kk[3][i]), l34 = perm_lo(kk[3][i], kk[4][i]);
      const u32 h01 = perm_hi(kk[0][i], kk[1][i]), h12 = perm_hi(kk[1][i], kk[2][i]), h23 = perm_hi(kk[2][i], kk[3][i]), h34 = perm_hi(kk[3][i], kk[4][i]);
      const float a1 = dot2(l01, wa[i][0], dot2(l23, wa[i][1], 0.f)) * bf_lo(v1[i]);
      const float a0 = dot2(l12, wa[i][0], dot2(l34, wa[i][1], 0.f)) * bf_lo(v0[i]);
      const float b1 = dot2(h01, wa[i][2], dot2(h23, wa[i][3], 0.f)) * bf_hi(v1[i]);
      const float b0 = dot2(h12, wa[i][2], dot2(h34, wa[i][3], 0.f)) * bf_hi(v0[i]);
      *(u32*)(pk + (2 * i) * 128) = pack_bf(a1, a0);
      *(u32*)(pk + (2 * i + 1) * 128) = pack_bf(b1, b0);
      *(u32*)(px + (2 * i) * 128) = perm_lo(x0[i], x1[i]);
      *(u32*)(px + (2 * i + 1) * 128) = perm_hi(x0[i], x1[i]);
    }
    __syncthreads();
#pragma unroll
    for (int i = 0; i < 2; i++) {
      const int u = t + 256 * i, ch = u >> 3, cc = u & 7;
      const u32x4 o = *(const u32x4*)(sm + ch * 128 + cc * 16);
      const int grp = cc ^ ((ch >> 3) & 7);
      const size_t go = (size_t)(dt * 64 + ch) * S + st * 64 + grp * 8;
      gstore16<K1_AUX>(ws, WS_KVT + go * 2, o);
      gstore16<K1_AUX>(ws, WS_XT + go * 2, *(const u32x4*)(sm + 8192 + ch * 128 + cc * 16));
    }
  } else {
    const int lrow = 256 * (b & 7) + 8 * (b >> 3) + 2 * (w - 4);
    u32x4 xr[2];
#pragma unroll
    for (int i = 0; i < 2; i++) xr[i] = *(const u32x4*)(X + (size_t)(lrow + i) * D + lane * 8);
    if (t == 256) __hip_atomic_store((GAS u32*)(ws + WS_FLAG) + b * 64, 0u, RLX_AGENT);
    code_prefetch((b >> 3) * 4 + (w - 4), 128, lane);
    __builtin_amdgcn_s_barrier();
#pragma unroll
    for (int q = 0; q < 2; q++) {
      float sum = 0.f;
#pragma unroll
      for (int i = 0; i < 4; i++) sum = dot2(xr[q][i], ONES, sum);
      const float mean = wave_sum(sum) * (1.f / D);
      float sq = 0.f;
#pragma unroll
      for (int i = 0; i < 4; i++) { const float d0 = bf_lo(xr[q][i]) - mean, d1 = bf_hi(xr[q][i]) - mean; sq += d0 * d0 + d1 * d1; }
      sq = wave_sum(sq);
      const float sd = rb(__builtin_amdgcn_sqrtf(rb(rb(sq * (1.f / D)) + 1e-5f)));
      const float r1 = __builtin_amdgcn_rcpf(sd);
      if (lane == 0) { f32x2 o2 = {r1, -rb(mean) * r1}; *(f32x2*)(ws + WS_ST1 + (size_t)(lrow + q) * 8) = o2; }
    }
  }
}

constexpr int HC_STRIDE = 4608;
constexpr int HC_CH = 8 * HC_STRIDE;
constexpr int U_CH = (240 + 2048) * 2;
constexpr int K2_H = 0, K2_U = 2 * HC_CH, K2_R = K2_U + 2 * U_CH + 32, K2_F = K2_R + 4 * 4096, K2_END = K2_F + 32;
__device__ __forceinline__ constexpr int hoff(int c) {
  return c == 0 ? 4 : c == 1 ? 2 : c == 2 ? 8 : c == 3 ? 3 : c == 4 ? 7 : c == 5 ? 0 : c == 6 ? 15 : 12;
}
__device__ __forceinline__ int hoff_rt(int c) { return (0xCF073824u >> (4 * c)) & 15; }
__device__ __forceinline__ void lds_wait(const char* p, u32 want) {
  for (u32 spins = 0; spins < (1u << 22); spins++) {
    if (__builtin_amdgcn_readfirstlane(*(volatile LAS u32*)p) == want) break;
    __builtin_amdgcn_s_sleep(1);
  }
  asm volatile("" ::: "memory");
}

__global__ __launch_bounds__(512, 2) void k2_conv(const u16* __restrict__ HK, const u16* __restrict__ DV,
                                                  const u16* __restrict__ W1, const u16* __restrict__ W2, char* __restrict__ ws) {
  __shared__ __attribute__((aligned(16))) char sm[K2_END];
  const int b = blockIdx.x, t = threadIdx.x, lane = t & 63;
  const int w = __builtin_amdgcn_readfirstlane(t >> 6);
  const int chp = 64 * (b & 7) + 2 * (b >> 3);
  const int wcl = (w >> 1) & 1, rh = w & 1;
  const int n = lane & 15, g = lane >> 4;
  {
    const int cl = t >> 8, j = t & 255;
    const __amdgpu_buffer_rsrc_t rh_ = mk_rsrc(HK + (size_t)chp * S, 2 * S * 2);
    const u32 ho = (u32)(cl * S + 8 * j) * 2;
    u32x4 ha, hb, uu;
    const __amdgpu_buffer_rsrc_t ru = mk_rsrc(ws + WS_KVT + (size_t)chp * S * 2, 2 * S * 2);
    bload16_asm(ha, rh_, ho - 16, 0);
    bload16_asm(hb, rh_, ho, 0);
    bload16_asm(uu, ru, ho, 0);
    asm volatile("s_waitcnt vmcnt(0)" : "+v"(ha), "+v"(hb), "+v"(uu)::"memory");
    if (t < 8) *(volatile LAS u32*)(sm + K2_F + t * 4) = 0u;
    SCHED_FENCE();
    if (j == 0) ha = u32x4{0, 0, 0, 0};
    char* hbase = sm + K2_H + cl * HC_CH;
    const u32 W[8] = {ha[0], ha[1], ha[2], ha[3], hb[0], hb[1], hb[2], hb[3]};
#pragma unroll
    for (int c = 0; c < 8; c++) {
      u32x4 o;
#pragma unroll
      for (int a = 0; a < 4; a++) o[a] = (c & 1) ? __builtin_amdgcn_alignbit(W[(c >> 1) + a + 1], W[(c >> 1) + a], 16) : W[(c >> 1) + a];
      *(u32x4*)(hbase + c * HC_STRIDE + 16 * (j + 1 + hoff(c))) = o;
    }
    const u32x4 z4 = {0, 0, 0, 0};
    if (j < 8) *(u32x4*)(hbase + j * HC_STRIDE + 16 * hoff_rt(j)) = z4;
    if (j == 255) *(u32x4*)(hbase + 16 * (257 + hoff(0))) = hb;
    char* ub = sm + K2_U + cl * U_CH;
    *(u32x4*)(ub + 480 + 16 * j) = uu;
    if (j < 30) *(u32x4*)(ub + 16 * j) = z4;
  }
  u32x2 xq[4];
  f32x4 sa[4], sb[4];
  u32 dvr = 0;
  if (w < 4) {
    const __amdgpu_buffer_rsrc_t rs1 = mk_rsrc(ws + WS_ST1, S * 8);
    const __amdgpu_buffer_rsrc_t rx = mk_rsrc(ws + WS_XT + (size_t)(chp + wcl) * S * 2, S * 2);
    const u32 p0 = 1024 * rh + 16 * n + 4 * g;
#pragma unroll
    for (int i = 0; i < 4; i++) {
      asm volatile("buffer_load_dwordx2 %0, %1, %2, %3 offen" : "=v"(xq[i]) : "v"(p0 * 2), "s"(rx), "s"(i * 512) : "memory");
      asm volatile("buffer_load_dwordx4 %0, %1, %2, %3 offen" : "=v"(sa[i]) : "v"(p0 * 8), "s"(rs1), "s"(i * 2048) : "memory");
      asm volatile("buffer_load_dwordx4 %0, %1, %2, %3 offen offset:16" : "=v"(sb[i]) : "v"(p0 * 8), "s"(rs1), "s"(i * 2048) : "memory");
    }
    dvr = *(const u32*)(DV + chp) >> (16 * wcl);
  }
  __syncthreads();
  if (w < 4) {
    f32x4 acc[8];
#pragma unroll
    for (int i = 0; i < 8; i++) acc[i] = f32x4{0.f, 0.f, 0.f, 0.f};
    const int idx0 = 25 + n - 8 * g;
    const int c = idx0 & 7;
    const char* fbase = sm + K2_H + wcl * HC_CH + c * HC_STRIDE + 16 * ((idx0 >> 3) + hoff_rt(c)) + 64 * 4 * rh;
    const char* gbase = sm + K2_U + wcl * U_CH + 2 * (16 * n + 8 * g + 224) - 64 * 4 * rh;
    u32x4 Ff[2][8], Gf[2][8];
#pragma unroll
    for (int a = 0; a < 8; a++) { Gf[0][a] = *(const u32x4*)(gbase + 64 * (8 * a)); Ff[0][a] = *(const u32x4*)(fbase + 64 * (8 * a)); }
    SCHED_FENCE();
#pragma unroll
    for (int rr = 0; rr < 4; rr++) {
      const int cur = rr & 1, nxt = cur ^ 1;
      int m = 0;
#pragma unroll
      for (int bb = 0; bb < 8; bb++) {
#pragma unroll
        for (int a = 0; a + bb < 8; a++) {
          acc[a + bb] = mfma(Ff[cur][a], Gf[cur][bb], acc[a + bb]);
          if (rr < 3 && (m & 1) == 1 && m < 32) {
            const int q = m >> 2;
            SCHED_FENCE();
            if (m & 2) Ff[nxt][q] = *(const u32x4*)(fbase + 64 * (8 * q + rr + 1));
            else Gf[nxt][q] = *(const u32x4*)(gbase + 64 * (8 * q - rr - 1));
            SCHED_FENCE();
          }
          m++;
        }
      }
      SCHED_FENCE();
    }
    f32x4* rbuf = (f32x4*)(sm + K2_R) + w * 256;
    if (rh) {
#pragma unroll
      for (int i = 0; i < 4; i++) rbuf[i * 64 + lane] = acc[i];
#pragma unroll
      for (int i = 0; i < 4; i++) acc[i] = acc[4 + i];
    } else {
#pragma unroll
      for (int i = 0; i < 4; i++) rbuf[i * 64 + lane] = acc[4 + i];
    }
    asm volatile("s_waitcnt lgkmcnt(0)" ::: "memory");
    if (lane == 0) *(volatile LAS u32*)(sm + K2_F + w * 4) = 1u;
    const int ch = chp + wcl;
    const char* ub = sm + K2_U + wcl * U_CH;
    u32x2 uq[4];
#pragma unroll
    for (int i = 0; i < 4; i++) uq[i] = *(const u32x2*)(ub + 2 * (240 + 256 * (4 * rh + i) + 16 * n + 8 * (g >> 1) + ((g & 1) ? 0 : 4)));
    asm volatile("s_waitcnt vmcnt(0)" : "+v"(xq[0]), "+v"(xq[1]), "+v"(xq[2]), "+v"(xq[3]), "+v"(sa[0]), "+v"(sa[1]), "+v"(sa[2]), "+v"(sa[3]),
                 "+v"(sb[0]), "+v"(sb[1]), "+v"(sb[2]), "+v"(sb[3])::"memory");
    lds_wait(sm + K2_F + (w ^ 1) * 4, 1u);
    const float dv = bf_lo(dvr);
    const f32x4* pbuf = (const f32x4*)(sm + K2_R) + (w ^ 1) * 256;
#pragma unroll
    for (int i = 0; i < 4; i++) {
      const int T = 4 * rh + i;
      const f32x4 a = acc[i] + pbuf[i * 64 + lane];
      const int pos = 256 * T + 16 * n + 4 * g;
      const float y0 = fmaf(bf_hi(uq[i][1]), dv, a[0]), y1 = fmaf(bf_lo(uq[i][1]), dv, a[1]), y2 = fmaf(bf_hi(uq[i][0]), dv, a[2]), y3 = fmaf(bf_lo(uq[i][0]), dv, a[3]);
      const float x0 = bf_lo(xq[i][0]), x1 = bf_hi(xq[i][0]), x2 = bf_lo(xq[i][1]), x3 = bf_hi(xq[i][1]);
      const float h0 = fmaf(fmaf(x0, sa[i][0], sa[i][1]), y0, x0), h1 = fmaf(fmaf(x1, sa[i][2], sa[i][3]), y1, x1);
      const float h2 = fmaf(fmaf(x2, sb[i][0], sb[i][1]), y2, x2), h3 = fmaf(fmaf(x3, sb[i][2], sb[i][3]), y3, x3);
      const u32x2 o = {pack_bf(h0, h1), pack_bf(h2, h3)};
      gstore8<K2_AUX>(ws, WS_HT + ((size_t)ch * S + pos) * 2, o);
    }
  } else {
    const int unit = b + 256 * (w - 4);
    const int lc = lane & 15, lg = lane >> 4;
    if (unit < 176) {
      const __amdgpu_buffer_rsrc_t rw = mk_rsrc(W1, D * F * 2);
      const int blk = unit >> 4, ks = unit & 15;
      const int col = 128 * blk + 8 * lc;
      const u32 o0 = col < F ? (u32)((32 * ks + 8 * lg) * F + col) * 2 : 0x80000000u;
      u32x4 wl[8];
#pragma unroll
      for (int j = 0; j < 8; j++) wl[j] = bload16(rw, o0 + j * F * 2);
#pragma unroll
      for (int i = 0; i < 8; i++) {
        u32x4 o;
#pragma unroll
        for (int a = 0; a < 4; a++) o[a] = (i & 1) ? perm_hi(wl[2 * a][i >> 1], wl[2 * a + 1][i >> 1]) : perm_lo(wl[2 * a][i >> 1], wl[2 * a + 1][i >> 1]);
        gstore16<WT_AUX>(ws, WS_W1T + ((size_t)((i * 11 + blk) * 16 + ks) * 64 + lane) * 16, o);
        const float cs = sum_g(dot2(o[0], ONES, dot2(o[1], ONES, dot2(o[2], ONES, dot2(o[3], ONES, 0.f)))));
        if (lane < 16) *(float*)(ws + WS_CSP + ((size_t)(i * 176 + blk * 16 + lane) * 16 + ks) * 4) = cs;
      }
    } else if (unit < 528) {
      const __amdgpu_buffer_rsrc_t rw = mk_rsrc(W2, D * F * 2);
      const int u2 = unit - 176, ks = u2 >> 3, c2 = u2 & 7;
      u32 wx[8], wy[8];
#pragma unroll
      for (int j = 0; j < 8; j++) {
        const u32 o = (u32)(fcol(ks, lg, j) * D + 64 * c2 + 8 * (lc >> 2) + 2 * (lc & 3)) * 2;
        wx[j] = __builtin_amdgcn_raw_buffer_load_b32(rw, o, 0, 0);
        wy[j] = __builtin_amdgcn_raw_buffer_load_b32(rw, o + 64, 0, 0);
      }
#pragma unroll
      for (int i = 0; i < 4; i++) {
        u32x4 o;
#pragma unroll
        for (int a = 0; a < 4; a++) {
          const u32 e0 = (i >> 1) ? wy[2 * a] : wx[2 * a], e1 = (i >> 1) ? wy[2 * a + 1] : wx[2 * a + 1];
          o[a] = (i & 1) ? perm_hi(e0, e1) : perm_lo(e0, e1);
        }
        gstore16<WT_AUX>(ws, WS_W2T + ((size_t)(((c2 * 2 + (i >> 1)) * 44 + ks) * 2 + (i & 1)) * 64 + lane) * 16, o);
      }
    }
  }
}

constexpr int L_HT = 0;
constexpr int L_ST = 147456;
constexpr int L_CS = L_ST + 512;
constexpr int L_RES = L_CS + 768;
constexpr int L_END = L_RES + 8192;
constexpr int L_RED = 0;
constexpr int L_FLG = L_END;
constexpr int L_TOT = L_END + 16;
#define W1_PRE 4
#define W2_LOADS                                                                                                                   \
  "buffer_load_dwordx4 %0, %14, %15, %16 offen\n\tbuffer_load_dwordx4 %1, %14, %15, %16 offen offset:1024\n\t"                      \
  "buffer_load_dwordx4 %2, %14, %15, %16 offen offset:2048\n\tbuffer_load_dwordx4 %3, %14, %15, %16 offen offset:3072\n\t"          \
  "buffer_load_dwordx4 %4, %14, %15, %17 offen\n\tbuffer_load_dwordx4 %5, %14, %15, %17 offen offset:1024\n\t"                      \
  "buffer_load_dwordx4 %6, %14, %15, %17 offen offset:2048\n\tbuffer_load_dwordx4 %7, %14, %15, %17 offen offset:3072\n\t"          \
  "buffer_load_dwordx4 %8, %14, %15, %18 offen\n\tbuffer_load_dwordx4 %9, %14, %15, %18 offen offset:1024\n\t"                      \
  "buffer_load_dwordx4 %10, %14, %15, %18 offen offset:2048\n\tbuffer_load_dwordx4 %11, %14, %15, %18 offen offset:3072\n\t"        \
  "buffer_load_dwordx4 %12, %14, %15, %19 offen\n\tbuffer_load_dwordx4 %13, %14, %15, %19 offen offset:1024\n"
__device__ __forceinline__ void load_w2_tail_support(u32x4 (&w2)[22], __amdgpu_buffer_rsrc_t r, u32 voff, int w) {
  asm volatile("s_cmp_lt_u32 %20, 4\n\ts_cbranch_scc1 1f\n\t" W2_LOADS "1:"
               : "=&v"(w2[8]), "=&v"(w2[9]), "=&v"(w2[10]), "=&v"(w2[11]), "=&v"(w2[12]), "=&v"(w2[13]), "=&v"(w2[14]), "=&v"(w2[15]), "=&v"(w2[16]),
                 "=&v"(w2[17]), "=&v"(w2[18]), "=&v"(w2[19]), "=&v"(w2[20]), "=&v"(w2[21])
               : "v"(voff), "s"(r), "s"(8192), "s"(12288), "s"(16384), "s"(20480), "s"(w) : "memory", "scc");
}
__device__ __forceinline__ void load_w2_tail_main(u32x4 (&w2)[22], __amdgpu_buffer_rsrc_t r, u32 voff, int w) {
  asm volatile("s_cmp_lt_u32 %20, 4\n\ts_cbranch_scc0 1f\n\t" W2_LOADS "1:"
               : "+v"(w2[8]), "+v"(w2[9]), "+v"(w2[10]), "+v"(w2[11]), "+v"(w2[12]), "+v"(w2[13]), "+v"(w2[14]), "+v"(w2[15]), "+v"(w2[16]),
                 "+v"(w2[17]), "+v"(w2[18]), "+v"(w2[19]), "+v"(w2[20]), "+v"(w2[21])
               : "v"(voff), "s"(r), "s"(8192), "s"(12288), "s"(16384), "s"(20480), "s"(w) : "memory", "scc");
}
__device__ __forceinline__ float silu_val(float acc, float rs, float bias) {
  const float tt = fmaf(acc, rs, bias);
  const float e = __builtin_amdgcn_exp2f(tt);
  return tt * __builtin_amdgcn_rcpf(fmaf(e, -1.44269504089f, -1.44269504089f));
}

__global__ __launch_bounds__(512, 2) void k34_ffn(char* __restrict__ ws, u16* __restrict__ OUT) {
  __shared__ __attribute__((aligned(16))) char sm[L_TOT];
  const int b = blockIdx.x, t = threadIdx.x, lane = t & 63;
  const int w = __builtin_amdgcn_readfirstlane(t >> 6);
  const int rt = 8 * (b & 3) + ((b >> 3) & 7), c = 4 * ((b >> 2) & 1) + (b >> 6);
  const int n = lane & 15, g = lane >> 4;
  const int row0 = 64 * rt;
  {
    const __amdgpu_buffer_rsrc_t rh = mk_rsrc(ws + WS_HT + ((size_t)(64 * w) * S + row0) * 2, 64 * S * 2);
    const int f0 = 2 * ((lane >> 4) & 1);
    const u32 v0 = (u32)(lane >> 3) * S * 2 + (((lane & 7) ^ f0) << 4), v1 = (u32)(lane >> 3) * S * 2 + (((lane & 7) ^ (f0 + 4)) << 4);
#pragma unroll
    for (int i = 0; i < 8; i++) glds16<false>(rh, (i & 1) ? v1 : v0, i * 8 * S * 2, (u32)(size_t)(sm + L_HT) + (64 * w + 8 * i) * 128);
  }
  if (t < 4) *(volatile LAS u32*)(sm + L_FLG + t * 4) = 0u;
  __builtin_amdgcn_s_barrier();
  const __amdgpu_buffer_rsrc_t rw2 = mk_rsrc(ws + WS_W2T + (size_t)((c * 2 + (w & 1)) * 44 + 11 * (w >> 1)) * 2048, 22 * 1024);
  const int q4 = (lane & 15) >> 2, p4 = lane & 3;
  const int fsw = 2 * ((q4 >> 1) & 1) + 4 * (g & 1);
  auto save_residual = [&]() {
    const char* p = sm + L_HT + (8 * g + q4) * 128 + (((2 * (w >> 1) + (p4 >> 1)) ^ fsw) << 4) + 8 * (p4 & 1) + (2 * c + (w & 1)) * 4096;
    const u32x2 a = tr_read(p), bq = tr_read(p + 512);
    *(u32x4*)(sm + L_RES + (w * 64 + lane) * 16) = u32x4{a[0], a[1], bq[0], bq[1]};
  };
  if (w >= 4) {
    f32x4 csp[4];
    const __amdgpu_buffer_rsrc_t rc = mk_rsrc(ws + WS_CSP + (size_t)c * 176 * 64, 176 * 64);
    const int cj = (w - 4) * 64 + lane;
#pragma unroll
    for (int i = 0; i < 4; i++) csp[i] = __builtin_bit_cast(f32x4, bload16s(rc, (u32)(cj < 176 ? cj : 175) * 64, i * 16));
    asm volatile("s_waitcnt vmcnt(4) lgkmcnt(0)" ::: "memory");
    __builtin_amdgcn_s_barrier();
    __builtin_amdgcn_s_setprio(3);
    const char* p = sm + L_HT + (8 * g + q4) * 128 + (((2 * (w - 4) + (p4 >> 1)) ^ fsw) << 4) + 8 * (p4 & 1);
    u32x2 fa[16][2];
#pragma unroll
    for (int ks = 0; ks < 16; ks++) { fa[ks][0] = tr_read(p + ks * 4096); fa[ks][1] = tr_read(p + ks * 4096 + 512); }
    f32x4 q0 = {0.f, 0.f, 0.f, 0.f}, q1 = {0.f, 0.f, 0.f, 0.f};
    const u32x4 ones = {ONES, ONES, ONES, ONES};
#pragma unroll
    for (int ks = 0; ks < 16; ks++) {
      const u32x4 f0 = {fa[ks][0][0], fa[ks][0][1], fa[ks][1][0], fa[ks][1][1]};
      q0 = mfma(f0, f0, q0);
      q1 = mfma(ones, f0, q1);
    }
    {
      const float lo = (n & 1) ? q0[1] : q0[0], hi = (n & 1) ? q0[3] : q0[2];
      const float q = (n & 2) ? hi : lo;
      const float mu = q1[0] * (1.f / D);
      const float var = fmaxf(q * (1.f / D) - mu * mu, 0.f);
      const float rstd = __builtin_amdgcn_rcpf(rb(__builtin_amdgcn_sqrtf(rb(rb(var) + 1e-5f))));
      if (g == (n >> 2)) *(f32x2*)(sm + L_ST + ((w - 4) * 16 + n) * 8) = f32x2{-1.44269504089f * rstd, rb(mu) * rstd * 1.44269504089f};
    }
    {
      const f32x4 s4 = (csp[0] + csp[1]) + (csp[2] + csp[3]);
      if (cj < 192) *(float*)(sm + L_CS + cj * 4) = (s4[0] + s4[1]) + (s4[2] + s4[3]);
    }
    save_residual();
    asm volatile("s_waitcnt lgkmcnt(0)" ::: "memory");
    if (lane == 0) __hip_atomic_fetch_add((LAS u32*)(sm + L_FLG + 4), 1u, __ATOMIC_RELAXED, __HIP_MEMORY_SCOPE_WORKGROUP);
    __builtin_amdgcn_s_setprio(0);
    lds_wait(sm + L_FLG + 8, 1u);
  } else {
    constexpr int NT = 3, NW1 = 48, PRE = W1_PRE * NT;
    u32x4 w1[NW1];
    const __amdgpu_buffer_rsrc_t rw1 = mk_rsrc(ws + WS_W1T + (size_t)(c * 11 + 3 * w) * 16 * 1024, 48 * 1024);
    auto w1load = [&](int k) { w1[k] = bload16s(rw1, lane * 16, (k % NT) * 16384 + (k / NT) * 1024); };
    auto issued = [&](int ks) constexpr { const int v = PRE + NT * (ks + 1); return v < NW1 ? v : NW1; };
#pragma unroll
    for (int i = 0; i < PRE; i++) w1load(i);
    asm volatile("s_waitcnt vmcnt(%0) lgkmcnt(0)" ::"n"(PRE) : "memory");
    __builtin_amdgcn_s_barrier();
    const char* tb[4];
#pragma unroll
    for (int m = 0; m < 4; m++) tb[m] = sm + L_HT + (8 * g + q4) * 128 + (((2 * m + (p4 >> 1)) ^ fsw) << 4) + 8 * (p4 & 1);
    f32x4 acc[NT][4];
    u32x4 hb[3][4];
#pragma unroll
    for (int i = 0; i < NT; i++)
#pragma unroll
      for (int m = 0; m < 4; m++) acc[i][m] = f32x4{0.f, 0.f, 0.f, 0.f};
#pragma unroll
    for (int d = 0; d < 2; d++)
#pragma unroll
      for (int m = 0; m < 4; m++) {
        const u32x2 a = tr_read(tb[m] + d * 4096), bq = tr_read(tb[m] + d * 4096 + 512);
        hb[d][m] = u32x4{a[0], a[1], bq[0], bq[1]};
      }
#pragma unroll
    for (int ks = 0; ks < 16; ks++) {
#pragma unroll
      for (int m = 0; m < 4; m++) {
        acc[0][m] = mfma(w1[ks * NT], hb[ks % 3][m], acc[0][m]);
        SCHED_FENCE();
        if (ks + 2 < 16) { const u32x2 a = tr_read(tb[m] + (ks + 2) * 4096); hb[(ks + 2) % 3][m][0] = a[0]; hb[(ks + 2) % 3][m][1] = a[1]; }
        SCHED_FENCE();
        acc[1][m] = mfma(w1[ks * NT + 1], hb[ks % 3][m], acc[1][m]);
        SCHED_FENCE();
        if (ks + 2 < 16) { const u32x2 a = tr_read(tb[m] + (ks + 2) * 4096 + 512); hb[(ks + 2) % 3][m][2] = a[0]; hb[(ks + 2) % 3][m][3] = a[1]; }
        SCHED_FENCE();
        acc[2][m] = mfma(w1[ks * NT + 2], hb[ks % 3][m], acc[2][m]);
        SCHED_FENCE();
        if (m < NT && issued(ks - 1) + m < issued(ks)) w1load(issued(ks - 1) + m);
        SCHED_FENCE();
      }
      if (ks == 15 - W1_PRE && w == 0 && lane == 0) *(volatile LAS u32*)(sm + L_FLG + 8) = 1u;
    }
    save_residual();
    lds_wait(sm + L_FLG + 4, 4u);
    float rs[4], mr[4];
    f32x4 cs[NT];
#pragma unroll
    for (int m = 0; m < 4; m++) { const f32x2 v = *(const f32x2*)(sm + L_ST + (m * 16 + n) * 8); rs[m] = v[0]; mr[m] = v[1]; }
#pragma unroll
    for (int i = 0; i < NT; i++) cs[i] = *(const f32x4*)(sm + L_CS + ((3 * w + i) * 16 + 4 * g) * 4);
    const __amdgpu_buffer_rsrc_t hrs = mk_rsrc(ws + WS_HF + (size_t)rt * 44 * 4096, 44 * 4096);
    u32 o_t[NT];
#pragma unroll
    for (int i = 0; i < NT; i++) {
      const int tl = 3 * w + i;
      const int ks2 = tl < 10 ? 5 * c + (tl >> 1) : 40 + (c >> 1), par = tl < 10 ? (tl & 1) : (c & 1);
      o_t[i] = (tl < 11 && (g & 1) == par) ? (u32)(ks2 * 4096 + lane * 16) : 0x40000000u;
    }
#pragma unroll
    for (int m = 0; m < 4; m++) {
#pragma unroll
      for (int i = 0; i < NT; i++) {
        float h1[4];
#pragma unroll
        for (int r = 0; r < 4; r++) h1[r] = silu_val(acc[i][m][r], rs[m], mr[m] * cs[i][r]);
        const u32 p0 = pack_bf(h1[0], h1[1]), p1 = pack_bf(h1[2], h1[3]);
        const auto s0 = __builtin_amdgcn_permlane16_swap(p0, p0, false, false), s1 = __builtin_amdgcn_permlane16_swap(p1, p1, false, false);
        __builtin_amdgcn_raw_buffer_store_b128(u32x4{s0[0], s1[0], s0[1], s1[1]}, hrs, o_t[i] + m * 1024, 0, 16);
      }
      SCHED_FENCE();
    }
  }
  u32x4 w2[22];
#pragma unroll
  for (int i = 0; i < 8; i++) bload16_asm(w2[i], rw2, lane * 16, i * 1024);
  load_w2_tail_support(w2, rw2, lane * 16, w);
  asm volatile("s_cmp_lt_u32 %0, 4\n\ts_cbranch_scc0 1f\n\ts_waitcnt vmcnt(0)\n1:\n\ts_waitcnt lgkmcnt(0)" ::"s"(w) : "memory", "scc");
  __builtin_amdgcn_s_barrier();
  if (w == 7) {
    GAS u32* flags = (GAS u32*)(ws + WS_FLAG);
    if (lane == 0) __hip_atomic_store(flags + b * 64, 1u, RLX_AGENT);
    const int pb = (b & 3) + (b & 56) + 4 * ((lane >> 2) & 1) + 64 * (lane & 3);
    for (u32 spins = 0; spins < (1u << 20); spins++) {
      const u32 v = __hip_atomic_load(flags + pb * 64, RLX_AGENT);
      if (__all(v == 1u)) break;
      __builtin_amdgcn_s_sleep(1);
    }
  }
  if (w < 4) __builtin_amdgcn_s_sleep(2);
  load_w2_tail_main(w2, rw2, lane * 16, w);
  __builtin_amdgcn_s_barrier();

  const int dh = w & 1, kq = w >> 1;
  const __amdgpu_buffer_rsrc_t rhf = mk_rsrc(ws + WS_HF + (size_t)rt * 44 * 4096 + (size_t)(11 * kq * 4 + 2 * dh) * 1024, 11 * 4096);
  const u32 ldst = (u32)(size_t)sm + (kq * 4 + 2 * dh) * 1024;
  auto stage1 = [&](int s, int e) {
    const int slot = s < 9 ? s : s - 9;
    glds16<true>(rhf, lane * 16, s * 4096 + e * 1024, ldst + slot * 16384 + e * 1024);
  };
  auto stage = [&](int s) { stage1(s, 0); stage1(s, 1); };
  stage(0); stage(1); stage(2); stage(3);
  f32x4 acc2[2][4];
#pragma unroll
  for (int i = 0; i < 2; i++)
#pragma unroll
    for (int m = 0; m < 4; m++) acc2[i][m] = f32x4{0.f, 0.f, 0.f, 0.f};
  asm volatile("s_waitcnt vmcnt(4)"
               : "+v"(w2[0]), "+v"(w2[1]), "+v"(w2[2]), "+v"(w2[3]), "+v"(w2[4]), "+v"(w2[5]), "+v"(w2[6]), "+v"(w2[7]), "+v"(w2[8]), "+v"(w2[9]), "+v"(w2[10]),
                 "+v"(w2[11]), "+v"(w2[12]), "+v"(w2[13]), "+v"(w2[14]), "+v"(w2[15]), "+v"(w2[16]), "+v"(w2[17]), "+v"(w2[18]), "+v"(w2[19]), "+v"(w2[20]), "+v"(w2[21])
               ::"memory");
  __builtin_amdgcn_s_barrier();
  {
    const char* base = sm + kq * 4096 + lane * 16;
    u32x4 hb[2][4];
#pragma unroll
    for (int m = 0; m < 4; m++) hb[0][m] = *(const u32x4*)(base + m * 1024);
#pragma unroll
    for (int s = 0; s < 11; s++) {
      if (s == 1) { asm volatile("s_waitcnt vmcnt(4)" ::: "memory"); __builtin_amdgcn_s_barrier(); }
      else if (s == 3 || s == 5) { asm volatile("s_waitcnt vmcnt(6)" ::: "memory"); __builtin_amdgcn_s_barrier(); }
      else if (s == 7) { asm volatile("s_waitcnt vmcnt(2)" ::: "memory"); __builtin_amdgcn_s_barrier(); }
      else if (s == 9) { asm volatile("s_waitcnt vmcnt(0)" ::: "memory"); __builtin_amdgcn_s_barrier(); }
      const int slot = s + 1 < 9 ? s + 1 : s + 1 - 9;
#pragma unroll
      for (int m = 0; m < 4; m++) {
        acc2[0][m] = mfma(w2[2 * s], hb[s & 1][m], acc2[0][m]);
        SCHED_FENCE();
        if (s < 10) hb[(s + 1) & 1][m] = *(const u32x4*)(base + slot * 16384 + m * 1024);
        SCHED_FENCE();
        acc2[1][m] = mfma(w2[2 * s + 1], hb[s & 1][m], acc2[1][m]);
        SCHED_FENCE();
        if (s < 2) stage1(4 + 2 * s + (m >> 1), m & 1);
        else if (s < 5 && (m & 1)) stage1(s + 6, m >> 1);
        SCHED_FENCE();
      }
    }
  }
  asm volatile("s_waitcnt lgkmcnt(0)" ::: "memory");
  __builtin_amdgcn_s_barrier();
  {
    char* red = sm + L_RED + dh * 32768;
    f32x4 o0, o1;
    auto scatter = [&](auto KQ) {
      constexpr int k = decltype(KQ)::value;
#pragma unroll
      for (int m = 0; m < 4; m++) {
        if (m != k) {
#pragma unroll
          for (int i = 0; i < 2; i++) *(f32x4*)(red + (((k * 4 + m) * 2 + i) * 64 + lane) * 16) = acc2[i][m];
        }
      }
      o0 = acc2[0][k]; o1 = acc2[1][k];
      asm volatile("" : "+v"(o0), "+v"(o1));
    };
    if (kq == 0) scatter(IntC<0>{}); else if (kq == 1) scatter(IntC<1>{}); else if (kq == 2) scatter(IntC<2>{}); else scatter(IntC<3>{});
    __syncthreads();
#pragma unroll
    for (int j = 1; j < 4; j++) {
      const int src = (kq + j) & 3;
      o0 += *(const f32x4*)(red + (((src * 4 + kq) * 2 + 0) * 64 + lane) * 16);
      o1 += *(const f32x4*)(red + (((src * 4 + kq) * 2 + 1) * 64 + lane) * 16);
    }
    const u32x4 hres = *(const u32x4*)(sm + L_RES + (w * 64 + lane) * 16);
    u32x4 res;
#pragma unroll
    for (int r = 0; r < 4; r++) res[r] = pack_bf(bf_lo(hres[r]) + rb(o0[r]), bf_hi(hres[r]) + rb(o1[r]));
    *(u32x4*)(OUT + (size_t)(row0 + 16 * kq + n) * D + 64 * c + 32 * dh + 8 * g) = res;
  }
  asm volatile("ffn_text_end:");
}

__global__ __launch_bounds__(512) void touch(const char* p, int rot) {
  const u32x4* base = (const u32x4*)(p + (size_t)((blockIdx.x + rot) & 7) * (24 * 8192));
  u32x4 r[24];
#pragma unroll
  for (int j = 0; j < 24; j++) r[j] = base[(size_t)j * 512 + threadIdx.x];
  u32 a = 0;
#pragma unroll
  for (int j = 0; j < 24; j++) a += r[j][0] + r[j][3];
  asm volatile("" ::"v"(a));
}
extern "C" void kernel_launch(void* const* d_in, const int* in_sizes, int n_in, void* d_out, int out_size, void* d_ws,
                              size_t ws_size, hipStream_t stream) {
  const u16* X = (const u16*)d_in[0];
  const u16* K = (const u16*)d_in[1];
  const u16* V = (const u16*)d_in[2];
  const u16* SK = (const u16*)d_in[3];
  const u16* HK = (const u16*)d_in[4];
  const u16* DV = (const u16*)d_in[5];
  const u16* W1 = (const u16*)d_in[7];
  const u16* W2 = (const u16*)d_in[8];
  char* ws = (char*)d_ws;
  k1_prep<<<256, 512, 0, stream>>>(X, K, V, SK, ws);
  k2_conv<<<256, 512, 0, stream>>>(HK, DV, W1, W2, ws);
  k34_ffn<<<256, 512, 0, stream>>>(ws, (u16*)d_out);
  for (int i = 0; i < 12; i++) touch<<<256, 512, 0, stream>>>(ws + (64 << 20), 0);
}
```

```cpp
#include <hip/hip_runtime.h>
#include <stdint.h>

typedef unsigned short u16;
typedef unsigned int u32;
typedef unsigned long long u64;
typedef __bf16 bf16x8 __attribute__((ext_vector_type(8)));
typedef __bf16 bf16x2 __attribute__((ext_vector_type(2)));
typedef short s16x4 __attribute__((ext_vector_type(4)));
typedef float f32x4 __attribute__((ext_vector_type(4)));
typedef float f32x2 __attribute__((ext_vector_type(2)));
typedef u32 u32x4 __attribute__((ext_vector_type(4)));
typedef u32 u32x2 __attribute__((ext_vector_type(2)));
#define GAS __attribute__((address_space(1)))
#define LAS __attribute__((address_space(3)))
#define RLX_AGENT __ATOMIC_RELAXED, __HIP_MEMORY_SCOPE_AGENT
#define SCHED_FENCE() __builtin_amdgcn_sched_barrier(0)

constexpr int S = 2048, D = 512, F = 1376;

constexpr size_t WS_EP   = 0;
constexpr size_t WS_F1   = 4096;
constexpr size_t WS_F2   = WS_F1 + 16384;
constexpr size_t WS_F3   = WS_F2 + 16384;
constexpr size_t WS_KVT  = 1 << 16;
constexpr size_t WS_XT   = WS_KVT + (size_t)D * S * 2;
constexpr size_t WS_HT   = WS_XT + (size_t)D * S * 2;
constexpr size_t WS_W1T  = WS_HT + (size_t)D * S * 2;
constexpr size_t WS_W2T  = WS_W1T + 8 * 11 * 16 * 1024;
constexpr size_t WS_CSP  = WS_W2T + 8 * 4 * 44 * 1024;
constexpr size_t WS_ST1  = WS_CSP + 16 * 8 * 11 * 16 * 4;
constexpr size_t WS_HF   = WS_ST1 + S * 16;
constexpr size_t WS_END  = WS_HF + (size_t)32 * 44 * 4 * 1024;

__device__ __forceinline__ float bf_lo(u32 v) { return __uint_as_float(v << 16); }
__device__ __forceinline__ float bf_hi(u32 v) { return __uint_as_float(v & 0xffff0000u); }
__device__ __forceinline__ u32 pack_bf(float a, float b) {
  f32x2 z = {a, b};
  return __builtin_bit_cast(u32, __builtin_convertvector(z, bf16x2));
}
__device__ __forceinline__ float rb(float a) { return bf_lo(pack_bf(a, 0.f)); }
__device__ __forceinline__ float dot2(u32 a, u32 b, float c) {
  return __builtin_amdgcn_fdot2_f32_bf16(__builtin_bit_cast(bf16x2, a), __builtin_bit_cast(bf16x2, b), c, false);
}
__device__ __forceinline__ u32 perm_lo(u32 a, u32 b) { return __builtin_amdgcn_perm(b, a, 0x05040100u); }
__device__ __forceinline__ u32 perm_hi(u32 a, u32 b) { return __builtin_amdgcn_perm(b, a, 0x07060302u); }
__device__ __forceinline__ f32x4 mfma(u32x4 a, u32x4 b, f32x4 c) {
  return __builtin_amdgcn_mfma_f32_16x16x32_bf16(__builtin_bit_cast(bf16x8, a), __builtin_bit_cast(bf16x8, b), c, 0, 0, 0);
}
constexpr u32 ONES = 0x3F803F80u;
template <int V> struct IntC { static constexpr int value = V; };
__device__ __forceinline__ float sum_g(float x) {
  auto s = __builtin_amdgcn_permlane16_swap(__float_as_uint(x), __float_as_uint(x), false, false);
  x = __uint_as_float(s[0]) + __uint_as_float(s[1]);
  auto t = __builtin_amdgcn_permlane32_swap(__float_as_uint(x), __float_as_uint(x), false, false);
  return __uint_as_float(t[0]) + __uint_as_float(t[1]);
}
template <int CTRL>
__device__ __forceinline__ float dpp_add(float x) {
  return x + __uint_as_float(__builtin_amdgcn_update_dpp(0u, __float_as_uint(x), CTRL, 0xf, 0xf, true));
}
__device__ __forceinline__ float wave_sum(float x) {
  x = dpp_add<0xB1>(x);
  x = dpp_add<0x4E>(x);
  x = dpp_add<0x141>(x);
  x = dpp_add<0x140>(x);
  return sum_g(x);
}
__device__ __forceinline__ __amdgpu_buffer_rsrc_t mk_rsrc(const void* p, u32 bytes) {
  return __builtin_amdgcn_make_buffer_rsrc((void*)p, 0, bytes, 0x00020000);
}
__device__ __forceinline__ u32x4 bload16(__amdgpu_buffer_rsrc_t r, u32 off) { return __builtin_amdgcn_raw_buffer_load_b128(r, off, 0, 0); }
__device__ __forceinline__ u32x2 bload8(__amdgpu_buffer_rsrc_t r, u32 off) { return __builtin_amdgcn_raw_buffer_load_b64(r, off, 0, 0); }
template <bool SC1>
__device__ __forceinline__ void glds16(__amdgpu_buffer_rsrc_t r, u32 voff, u32 soff, u32 lds_dst) {
  if (SC1)
    asm volatile("s_mov_b32 m0, %0\n\ts_nop 0\n\tbuffer_load_dwordx4 %1, %2, %3 offen sc1 lds" ::"s"(lds_dst), "v"(voff), "s"(r), "s"(soff) : "memory");
  else
    asm volatile("s_mov_b32 m0, %0\n\ts_nop 0\n\tbuffer_load_dwordx4 %1, %2, %3 offen lds" ::"s"(lds_dst), "v"(voff), "s"(r), "s"(soff) : "memory");
}
__device__ __forceinline__ void bload16_asm(u32x4& d, __amdgpu_buffer_rsrc_t r, u32 voff, u32 soff) {
  asm volatile("buffer_load_dwordx4 %0, %1, %2, %3 offen" : "=v"(d) : "v"(voff), "s"(r), "s"(soff) : "memory");
}
__device__ __forceinline__ void bload16_asm_sc1(u32x4& d, __amdgpu_buffer_rsrc_t r, u32 voff, u32 soff) {
  asm volatile("buffer_load_dwordx4 %0, %1, %2, %3 offen sc1" : "=v"(d) : "v"(voff), "s"(r), "s"(soff) : "memory");
}
template <int AUX>
__device__ __forceinline__ void gstore16(char* base, size_t off, u32x4 v) {
  const __amdgpu_buffer_rsrc_t r = mk_rsrc(base, 0x7fffffffu);
  __builtin_amdgcn_raw_buffer_store_b128(v, r, (u32)off, 0, AUX);
}
template <int AUX>
__device__ __forceinline__ void gstore8(char* base, size_t off, u32x2 v) {
  const __amdgpu_buffer_rsrc_t r = mk_rsrc(base, 0x7fffffffu);
  __builtin_amdgcn_raw_buffer_store_b64(v, r, (u32)off, 0, AUX);
}
__device__ __forceinline__ u32x4 bload16s(__amdgpu_buffer_rsrc_t r, u32 voff, u32 soff) { return __builtin_amdgcn_raw_buffer_load_b128(r, voff, soff, 16); }
__device__ __forceinline__ u32x2 tr_read(const char* p) {
  s16x4 r = __builtin_amdgcn_ds_read_tr16_b64_v4i16((LAS s16x4*)p);
  return __builtin_bit_cast(u32x2, r);
}

__device__ __forceinline__ void code_prefetch(int part, int nparts, int lane) {
  u64 pc; u32 len;
  asm volatile("s_getpc_b64 %0\n\ts_mov_b32 %1, ffn_text_end-." : "=s"(pc), "=s"(len));
  const __amdgpu_buffer_rsrc_t r = mk_rsrc((const void*)pc, len);
  u32 acc = 0;
  for (u32 o = part * 1024; o < len; o += nparts * 1024) acc |= __builtin_amdgcn_raw_buffer_load_b128(r, o + lane * 16, 0, 0)[0];
  asm volatile("" ::"v"(acc));
}
__device__ __forceinline__ int fcol(int ks, int g, int j) {
  const int slot = 8 * (g >> 1) + j;
  if (ks < 40) { const int ct = ks / 5, p = ks - 5 * ct; return 128 * (2 * p + (g & 1)) + 8 * slot + ct; }
  return 1280 + 8 * slot + 2 * (ks - 40) + (g & 1);
}

constexpr int HC_STRIDE = 4608;
constexpr int HC_CH = 8 * HC_STRIDE;
constexpr int U_CH = (240 + 2048) * 2;
constexpr int P_H = 0, P_U = 2 * HC_CH, P_R = P_U + 2 * U_CH + 32, P_T = P_R + 4 * 4096, P_S = P_T + 16384;
constexpr int L_HT = 0;
constexpr int L_ST = 147456;
constexpr int L_CS = L_ST + 512;
constexpr int L_RES = L_CS + 768;
constexpr int L_RED = 0;
constexpr int L_FLG = L_RES + 8192;
constexpr int L_TOT = L_FLG + 128;
enum { W_EPLO = 0, W_EPHI = 4, W_EPRDY = 8, W_CA = 12, W_CB = 16, W_POLL1 = 20, W_HRDY = 24, W_CD = 28, W_PART = 32, W_SROWS = 48, W_STRDY = 52,
       W_WDONE = 56, W_CE = 60, W_LN2 = 64, W_W1S = 68 };
static_assert(P_S + 64 <= L_ST && L_TOT <= 163840, "LDS map");
#define W1_PRE 4
__device__ __forceinline__ constexpr int hoff(int c) {
  return c == 0 ? 4 : c == 1 ? 2 : c == 2 ? 8 : c == 3 ? 3 : c == 4 ? 7 : c == 5 ? 0 : c == 6 ? 15 : 12;
}
__device__ __forceinline__ int hoff_rt(int c) { return (0xCF073824u >> (4 * c)) & 15; }
__device__ __forceinline__ void lds_wait(const char* p, u32 want) {
  for (u32 spins = 0; spins < (1u << 22); spins++) {
    if (__builtin_amdgcn_readfirstlane(*(volatile LAS u32*)p) == want) break;
    __builtin_amdgcn_s_sleep(1);
  }
  asm volatile("" ::: "memory");
}
__device__ __forceinline__ void lds_arrive(char* p, int lane) {
  asm volatile("s_waitcnt lgkmcnt(0)" ::: "memory");
  if (lane == 0) __hip_atomic_fetch_add((LAS u32*)p, 1u, __ATOMIC_RELAXED, __HIP_MEMORY_SCOPE_WORKGROUP);
}
__device__ __forceinline__ void lds_set(char* p, u32 v, int lane) {
  asm volatile("s_waitcnt lgkmcnt(0)" ::: "memory");
  if (lane == 0) *(volatile LAS u32*)p = v;
}
__device__ __forceinline__ u64 lds_epoch(const char* sm) {
  lds_wait(sm + L_FLG + W_EPRDY, 1u);
  const u32 lo = __builtin_amdgcn_readfirstlane(*(volatile LAS u32*)(sm + L_FLG + W_EPLO)), hi = __builtin_amdgcn_readfirstlane(*(volatile LAS u32*)(sm + L_FLG + W_EPHI));
  return ((u64)hi << 32) | lo;
}
template <int N>
__device__ __forceinline__ void poll_flags(const char* base, int first, int stride, u64 ep) {
  for (u32 spins = 0; spins < (1u << 20); spins++) {
    bool ok = true;
#pragma unroll
    for (int i = 0; i < N; i++) ok &= __hip_atomic_load((GAS u64*)(base + (size_t)(first + i * stride) * 64), RLX_AGENT) == ep;
    if (__all(ok)) break;
    __builtin_amdgcn_s_sleep(1);
  }
  asm volatile("" ::: "memory");
}

#define W2_LOADS                                                                                                                   \
  "buffer_load_dwordx4 %0, %14, %15, %16 offen sc1\n\tbuffer_load_dwordx4 %1, %14, %15, %16 offen offset:1024 sc1\n\t"                      \
  "buffer_load_dwordx4 %2, %14, %15, %16 offen offset:2048 sc1\n\tbuffer_load_dwordx4 %3, %14, %15, %16 offen offset:3072 sc1\n\t"          \
  "buffer_load_dwordx4 %4, %14, %15, %17 offen sc1\n\tbuffer_load_dwordx4 %5, %14, %15, %17 offen offset:1024 sc1\n\t"                      \
  "buffer_load_dwordx4 %6, %14, %15, %17 offen offset:2048 sc1\n\tbuffer_load_dwordx4 %7, %14, %15, %17 offen offset:3072 sc1\n\t"          \
  "buffer_load_dwordx4 %8, %14, %15, %18 offen sc1\n\tbuffer_load_dwordx4 %9, %14, %15, %18 offen offset:1024 sc1\n\t"                      \
  "buffer_load_dwordx4 %10, %14, %15, %18 offen offset:2048 sc1\n\tbuffer_load_dwordx4 %11, %14, %15, %18 offen offset:3072 sc1\n\t"        \
  "buffer_load_dwordx4 %12, %14, %15, %19 offen sc1\n\tbuffer_load_dwordx4 %13, %14, %15, %19 offen offset:1024 sc1\n"
__device__ __forceinline__ void load_w2_tail_support(u32x4 (&w2)[22], __amdgpu_buffer_rsrc_t r, u32 voff, int w) {
  asm volatile("s_cmp_lt_u32 %20, 4\n\ts_cbranch_scc1 1f\n\t" W2_LOADS "1:"
               : "=&v"(w2[8]), "=&v"(w2[9]), "=&v"(w2[10]), "=&v"(w2[11]), "=&v"(w2[12]), "=&v"(w2[13]), "=&v"(w2[14]), "=&v"(w2[15]), "=&v"(w2[16]),
                 "=&v"(w2[17]), "=&v"(w2[18]), "=&v"(w2[19]), "=&v"(w2[20]), "=&v"(w2[21])
               : "v"(voff), "s"(r), "s"(8192), "s"(12288), "s"(16384), "s"(20480), "s"(w) : "memory", "scc");
}
__device__ __forceinline__ void load_w2_tail_main(u32x4 (&w2)[22], __amdgpu_buffer_rsrc_t r, u32 voff, int w) {
  asm volatile("s_cmp_lt_u32 %20, 4\n\ts_cbranch_scc0 1f\n\t" W2_LOADS "1:"
               : "+v"(w2[8]), "+v"(w2[9]), "+v"(w2[10]), "+v"(w2[11]), "+v"(w2[12]), "+v"(w2[13]), "+v"(w2[14]), "+v"(w2[15]), "+v"(w2[16]),
                 "+v"(w2[17]), "+v"(w2[18]), "+v"(w2[19]), "+v"(w2[20]), "+v"(w2[21])
               : "v"(voff), "s"(r), "s"(8192), "s"(12288), "s"(16384), "s"(20480), "s"(w) : "memory", "scc");
}
__device__ __forceinline__ float silu_val(float acc, float rs, float bias) {
  const float tt = fmaf(acc, rs, bias);
  const float e = __builtin_amdgcn_exp2f(tt);
  return tt * __builtin_amdgcn_rcpf(fmaf(e, -1.44269504089f, -1.44269504089f));
}

__global__ __launch_bounds__(512, 2) void fused_block(const u16* __restrict__ X, const u16* __restrict__ K, const u16* __restrict__ V,
                                                      const u16* __restrict__ SK, const u16* __restrict__ HK, const u16* __restrict__ DV,
                                                      const u16* __restrict__ W1, const u16* __restrict__ W2, char* __restrict__ ws,
                                                      u16* __restrict__ OUT) {
  __shared__ __attribute__((aligned(16))) char sm[L_TOT];
  const int b = blockIdx.x, t = threadIdx.x, lane = t & 63;
  const int w = __builtin_amdgcn_readfirstlane(t >> 6);
  const int n = lane & 15, g = lane >> 4;
  char* lf = sm + L_FLG;
  if (t < 32) *(volatile LAS u32*)(lf + t * 4) = 0u;
  asm volatile("s_waitcnt lgkmcnt(0)" ::: "memory");
  __builtin_amdgcn_s_barrier();
  const int chp = 64 * (b & 7) + 2 * (b >> 3);
  if (w < 4) {
    u64 ep = 0;
    if (w == 0) ep = __hip_atomic_load((GAS u64*)(ws + WS_EP), RLX_AGENT) + 1;
    SCHED_FENCE();
    const int dt = b & 7, st = b >> 3;
    const int rp = t >> 3, c = t & 7;
    const int s = st * 64 + 2 * rp, ch0 = dt * 64 + c * 8;
    const __amdgpu_buffer_rsrc_t rk = mk_rsrc(K, S * D * 2);
    const u32 off = (u32)(s * D + ch0) * 2;
    u32x4 kk[5];
#pragma unroll
    for (int i = 0; i < 5; i++) kk[i] = bload16(rk, off + (1 - i) * 1024);
    const u32x4 v0 = *(const u32x4*)((const char*)V + off), v1 = *(const u32x4*)((const char*)V + off + 1024);
    const u32x4 x0 = *(const u32x4*)((const char*)X + off), x1 = *(const u32x4*)((const char*)X + off + 1024);
    u32x4 wa[4];
#pragma unroll
    for (int i = 0; i < 4; i++) wa[i] = *(const u32x4*)(SK + ch0 * 4 + i * 8);
    if (w == 0) {
      if (lane == 0) { *(volatile LAS u32*)(lf + W_EPLO) = (u32)ep; *(volatile LAS u32*)(lf + W_EPHI) = (u32)(ep >> 32); }
      lds_set(lf + W_EPRDY, 1u, lane);
    }
    const int swz = ((rp >> 2) ^ c) << 4;
    char* pk = sm + P_T + (8 * c) * 128 + swz + ((3 - (rp & 3)) << 2);
    char* px = sm + P_T + 8192 + (8 * c) * 128 + swz + ((rp & 3) << 2);
#pragma unroll
    for (int i = 0; i < 4; i++) {
      const u32 l01 = perm_lo(kk[0][i], kk[1][i]), l12 = perm_lo(kk[1][i], kk[2][i]), l23 = perm_lo(kk[2][i], kk[3][i]), l34 = perm_lo(kk[3][i], kk[4][i]);
      const u32 h01 = perm_hi(kk[0][i], kk[1][i]), h12 = perm_hi(kk[1][i], kk[2][i]), h23 = perm_hi(kk[2][i], kk[3][i]), h34 = perm_hi(kk[3][i], kk[4][i]);
      const float a1 = dot2(l01, wa[i][0], dot2(l23, wa[i][1], 0.f)) * bf_lo(v1[i]);
      const float a0 = dot2(l12, wa[i][0], dot2(l34, wa[i][1], 0.f)) * bf_lo(v0[i]);
      const float b1 = dot2(h01, wa[i][2], dot2(h23, wa[i][3], 0.f)) * bf_hi(v1[i]);
      const float b0 = dot2(h12, wa[i][2], dot2(h34, wa[i][3], 0.f)) * bf_hi(v0[i]);
      *(u32*)(pk + (2 * i) * 128) = pack_bf(a1, a0);
      *(u32*)(pk + (2 * i + 1) * 128) = pack_bf(b1, b0);
      *(u32*)(px + (2 * i) * 128) = perm_lo(x0[i], x1[i]);
      *(u32*)(px + (2 * i + 1) * 128) = perm_hi(x0[i], x1[i]);
    }
    lds_arrive(lf + W_CA, lane);
    lds_wait(lf + W_CA, 4u);
#pragma unroll
    for (int i = 0; i < 2; i++) {
      const int u = t + 256 * i, ch = u >> 3, cc = u & 7;
      const u32x4 o = *(const u32x4*)(sm + P_T + ch * 128 + cc * 16);
      const u32x4 o2 = *(const u32x4*)(sm + P_T + 8192 + ch * 128 + cc * 16);
      const int grp = cc ^ ((ch >> 3) & 7);
      const size_t go = (size_t)(dt * 64 + ch) * S + st * 64 + grp * 8;
      gstore16<16>(ws, WS_KVT + go * 2, o);
      gstore16<16>(ws, WS_XT + go * 2, o2);
    }
    asm volatile("s_waitcnt vmcnt(0)" ::: "memory");
    lds_arrive(lf + W_CB, lane);
    ep = lds_epoch(sm);
    if (w == 0) {
      lds_wait(lf + W_CB, 4u);
      if (lane == 0) __hip_atomic_store((GAS u64*)(ws + WS_F1 + (size_t)b * 64), ep, RLX_AGENT);
      poll_flags<1>(ws + WS_F1, (b & 7) + 8 * (lane & 31), 0, ep);
      lds_set(lf + W_POLL1, 1u, lane);
    } else {
      lds_wait(lf + W_POLL1, 1u);
    }
    const int wcl = (w >> 1) & 1, rh = w & 1;
    u32x4 u0, u1;
    u32x2 xq[4];
    {
      const __amdgpu_buffer_rsrc_t ru = mk_rsrc(ws + WS_KVT + (size_t)chp * S * 2, 2 * S * 2);
      bload16_asm_sc1(u0, ru, t * 16, 0);
      bload16_asm_sc1(u1, ru, t * 16, S * 2);
      const __amdgpu_buffer_rsrc_t rx = mk_rsrc(ws + WS_XT + (size_t)(chp + wcl) * S * 2, S * 2);
      const u32 p0 = 1024 * rh + 16 * n + 4 * g;
#pragma unroll
      for (int i = 0; i < 4; i++) asm volatile("buffer_load_dwordx2 %0, %1, %2, %3 offen sc1" : "=v"(xq[i]) : "v"(p0 * 2), "s"(rx), "s"(i * 512) : "memory");
      asm volatile("s_waitcnt vmcnt(4)" : "+v"(u0), "+v"(u1)::"memory");
      *(u32x4*)(sm + P_U + 480 + 16 * t) = u0;
      *(u32x4*)(sm + P_U + U_CH + 480 + 16 * t) = u1;
      if (t < 60) *(u32x4*)(sm + P_U + (t < 30 ? 0 : U_CH - 480) + 16 * t) = u32x4{0, 0, 0, 0};
    }
    const u32 dvr = *(const u32*)(DV + chp) >> (16 * wcl);
    lds_arrive(lf + W_CD, lane);
    lds_wait(lf + W_HRDY, 4u);
    lds_wait(lf + W_CD, 4u);
    f32x4 acc[8];
#pragma unroll
    for (int i = 0; i < 8; i++) acc[i] = f32x4{0.f, 0.f, 0.f, 0.f};
    const int idx0 = 25 + n - 8 * g;
    const int cc = idx0 & 7;
    const char* fbase = sm + P_H + wcl * HC_CH + cc * HC_STRIDE + 16 * ((idx0 >> 3) + hoff_rt(cc)) + 64 * 4 * rh;
    const char* gbase = sm + P_U + wcl * U_CH + 2 * (16 * n + 8 * g + 224) - 64 * 4 * rh;
    u32x4 Ff[2][8], Gf[2][8];
#pragma unroll
    for (int a = 0; a < 8; a++) { Gf[0][a] = *(const u32x4*)(gbase + 64 * (8 * a)); Ff[0][a] = *(const u32x4*)(fbase + 64 * (8 * a)); }
    SCHED_FENCE();
#pragma unroll
    for (int rr = 0; rr < 4; rr++) {
      const int cur = rr & 1, nxt = cur ^ 1;
      int m = 0;
#pragma unroll
      for (int bb = 0; bb < 8; bb++) {
#pragma unroll
        for (int a = 0; a + bb < 8; a++) {
          acc[a + bb] = mfma(Ff[cur][a], Gf[cur][bb], acc[a + bb]);
          if (rr < 3 && (m & 1) == 1 && m < 32) {
            const int q = m >> 2;
            SCHED_FENCE();
            if (m & 2) Ff[nxt][q] = *(const u32x4*)(fbase + 64 * (8 * q + rr + 1));
            else Gf[nxt][q] = *(const u32x4*)(gbase + 64 * (8 * q - rr - 1));
            SCHED_FENCE();
          }
          m++;
        }
      }
      SCHED_FENCE();
    }
    f32x4* rbuf = (f32x4*)(sm + P_R) + w * 256;
    if (rh) {
#pragma unroll
      for (int i = 0; i < 4; i++) rbuf[i * 64 + lane] = acc[i];
#pragma unroll
      for (int i = 0; i < 4; i++) acc[i] = acc[4 + i];
    } else {
#pragma unroll
      for (int i = 0; i < 4; i++) rbuf[i * 64 + lane] = acc[4 + i];
    }
    lds_set(lf + W_PART + w * 4, 1u, lane);
    const int ch = chp + wcl;
    const char* ub = sm + P_U + wcl * U_CH;
    u32x2 uq[4];
    f32x4 sa[4], sb[4];
#pragma unroll
    for (int i = 0; i < 4; i++) uq[i] = *(const u32x2*)(ub + 2 * (240 + 256 * (4 * rh + i) + 16 * n + 8 * (g >> 1) + ((g & 1) ? 0 : 4)));
    lds_wait(lf + W_STRDY, 4u);
#pragma unroll
    for (int i = 0; i < 4; i++) {
      const char* sp = sm + P_T + (256 * (4 * rh + i) + 16 * n + 4 * g) * 8;
      sa[i] = *(const f32x4*)sp; sb[i] = *(const f32x4*)(sp + 16);
    }
    asm volatile("s_waitcnt vmcnt(0)" : "+v"(xq[0]), "+v"(xq[1]), "+v"(xq[2]), "+v"(xq[3])::"memory");
    lds_wait(lf + W_PART + (w ^ 1) * 4, 1u);
    const float dv = bf_lo(dvr);
    const f32x4* pbuf = (const f32x4*)(sm + P_R) + (w ^ 1) * 256;
#pragma unroll
    for (int i = 0; i < 4; i++) {
      const int T = 4 * rh + i;
      const f32x4 a = acc[i] + pbuf[i * 64 + lane];
      const int pos = 256 * T + 16 * n + 4 * g;
      const float y0 = fmaf(bf_hi(uq[i][1]), dv, a[0]), y1 = fmaf(bf_lo(uq[i][1]), dv, a[1]), y2 = fmaf(bf_hi(uq[i][0]), dv, a[2]), y3 = fmaf(bf_lo(uq[i][0]), dv, a[3]);
      const float x0 = bf_lo(xq[i][0]), x1 = bf_hi(xq[i][0]), x2 = bf_lo(xq[i][1]), x3 = bf_hi(xq[i][1]);
      const float h0 = fmaf(fmaf(x0, sa[i][0], sa[i][1]), y0, x0), h1 = fmaf(fmaf(x1, sa[i][2], sa[i][3]), y1, x1);
      const float h2 = fmaf(fmaf(x2, sb[i][0], sb[i][1]), y2, x2), h3 = fmaf(fmaf(x3, sb[i][2], sb[i][3]), y3, x3);
      gstore8<16>(ws, WS_HT + ((size_t)ch * S + pos) * 2, u32x2{pack_bf(h0, h1), pack_bf(h2, h3)});
    }
    asm volatile("s_waitcnt vmcnt(0)" ::: "memory");
    lds_arrive(lf + W_CE, lane);
    if (w == 0) {
      lds_wait(lf + W_CE, 4u);
      lds_wait(lf + W_WDONE, 4u);
      if (lane == 0) __hip_atomic_store((GAS u64*)(ws + WS_F2 + (size_t)b * 64), ep, RLX_AGENT);
      poll_flags<4>(ws + WS_F2, lane, 64, ep);
      if (b == 0 && lane == 0) __hip_atomic_store((GAS u64*)(ws + WS_EP), ep, RLX_AGENT);
    }
  } else {
    const int s = t - 256;
    code_prefetch((b >> 3) * 4 + (w - 4), 128, lane);
    lds_wait(lf + W_CA, 4u);
    u32x4 ha[2], hb[2];
    const __amdgpu_buffer_rsrc_t rh_ = mk_rsrc(HK + (size_t)chp * S, 2 * S * 2);
#pragma unroll
    for (int cl = 0; cl < 2; cl++) { ha[cl] = bload16(rh_, (u32)(cl * S + 8 * s) * 2 - 16); hb[cl] = bload16(rh_, (u32)(cl * S + 8 * s) * 2); }
    const int lrow = 8 * b + 2 * (w - 4);
    u32x4 xr[2];
#pragma unroll
    for (int i = 0; i < 2; i++) xr[i] = *(const u32x4*)(X + (size_t)(lrow + i) * D + lane * 8);
    SCHED_FENCE();
#pragma unroll
    for (int cl = 0; cl < 2; cl++) {
      if (s == 0) ha[cl] = u32x4{0, 0, 0, 0};
      char* hbase = sm + P_H + cl * HC_CH;
      const u32 W[8] = {ha[cl][0], ha[cl][1], ha[cl][2], ha[cl][3], hb[cl][0], hb[cl][1], hb[cl][2], hb[cl][3]};
#pragma unroll
      for (int c = 0; c < 8; c++) {
        u32x4 o;
#pragma unroll
        for (int a = 0; a < 4; a++) o[a] = (c & 1) ? __builtin_amdgcn_alignbit(W[(c >> 1) + a + 1], W[(c >> 1) + a], 16) : W[(c >> 1) + a];
        *(u32x4*)(hbase + c * HC_STRIDE + 16 * (s + 1 + hoff(c))) = o;
      }
      if (s < 8) *(u32x4*)(hbase + s * HC_STRIDE + 16 * hoff_rt(s)) = u32x4{0, 0, 0, 0};
      if (s == 255) *(u32x4*)(hbase + 16 * (257 + hoff(0))) = hb[cl];
    }
    lds_arrive(lf + W_HRDY, lane);
    const u64 ep = lds_epoch(sm);
#pragma unroll
    for (int q = 0; q < 2; q++) {
      float sum = 0.f;
#pragma unroll
      for (int i = 0; i < 4; i++) sum = dot2(xr[q][i], ONES, sum);
      const float mean = wave_sum(sum) * (1.f / D);
      float sq = 0.f;
#pragma unroll
      for (int i = 0; i < 4; i++) { const float d0 = bf_lo(xr[q][i]) - mean, d1 = bf_hi(xr[q][i]) - mean; sq += d0 * d0 + d1 * d1; }
      sq = wave_sum(sq);
      const float sd = rb(__builtin_amdgcn_sqrtf(rb(rb(sq * (1.f / D)) + 1e-5f)));
      const float r1 = __builtin_amdgcn_rcpf(sd);
      if (lane == 0) *(f32x2*)(sm + P_S + (2 * (w - 4) + q) * 8) = f32x2{r1, -rb(mean) * r1};
    }
    {
      asm volatile("s_waitcnt lgkmcnt(0)" ::: "memory");
      u32 old = 0;
      if (lane == 0) old = __hip_atomic_fetch_add((LAS u32*)(lf + W_SROWS), 1u, __ATOMIC_RELAXED, __HIP_MEMORY_SCOPE_WORKGROUP);
      if (__builtin_amdgcn_readfirstlane(old) == 3u) {
        const u32 val = *(volatile LAS u32*)(sm + P_S + (lane & 15) * 4);
        if (lane < 16) __hip_atomic_store((GAS u64*)(ws + WS_ST1 + (size_t)b * 128 + lane * 8), ((u64)(u32)ep << 32) | val, RLX_AGENT);
      }
    }
    {
      const int unit = b + 256 * (w - 4);
      const int lc = lane & 15, lg = lane >> 4;
      if (unit < 176) {
        const __amdgpu_buffer_rsrc_t rw = mk_rsrc(W1, D * F * 2);
        const int blk = unit >> 4, ks = unit & 15;
        const int col = 128 * blk + 8 * lc;
        const u32 o0 = col < F ? (u32)((32 * ks + 8 * lg) * F + col) * 2 : 0x80000000u;
        u32x4 wl[8];
#pragma unroll
        for (int j = 0; j < 8; j++) wl[j] = bload16(rw, o0 + j * F * 2);
#pragma unroll
        for (int i = 0; i < 8; i++) {
          u32x4 o;
#pragma unroll
          for (int a = 0; a < 4; a++) o[a] = (i & 1) ? perm_hi(wl[2 * a][i >> 1], wl[2 * a + 1][i >> 1]) : perm_lo(wl[2 * a][i >> 1], wl[2 * a + 1][i >> 1]);
          gstore16<16>(ws, WS_W1T + ((size_t)((i * 11 + blk) * 16 + ks) * 64 + lane) * 16, o);
          const float cs = sum_g(dot2(o[0], ONES, dot2(o[1], ONES, dot2(o[2], ONES, dot2(o[3], ONES, 0.f)))));
          if (lane < 16) __hip_atomic_store((GAS u32*)(ws + WS_CSP + ((size_t)(i * 176 + blk * 16 + lane) * 16 + ks) * 4), __float_as_uint(cs), RLX_AGENT);
        }
      } else if (unit < 528) {
        const __amdgpu_buffer_rsrc_t rw = mk_rsrc(W2, D * F * 2);
        const int u2 = unit - 176, ks = u2 >> 3, c2 = u2 & 7;
        u32 wx[8], wy[8];
#pragma unroll
        for (int j = 0; j < 8; j++) {
          const u32 o = (u32)(fcol(ks, lg, j) * D + 64 * c2 + 8 * (lc >> 2) + 2 * (lc & 3)) * 2;
          wx[j] = __builtin_amdgcn_raw_buffer_load_b32(rw, o, 0, 0);
          wy[j] = __builtin_amdgcn_raw_buffer_load_b32(rw, o + 64, 0, 0);
        }
#pragma unroll
        for (int i = 0; i < 4; i++) {
          u32x4 o;
#pragma unroll
          for (int a = 0; a < 4; a++) {
            const u32 e0 = (i >> 1) ? wy[2 * a] : wx[2 * a], e1 = (i >> 1) ? wy[2 * a + 1] : wx[2 * a + 1];
            o[a] = (i & 1) ? perm_hi(e0, e1) : perm_lo(e0, e1);
          }
          gstore16<16>(ws, WS_W2T + ((size_t)(((c2 * 2 + (i >> 1)) * 44 + ks) * 2 + (i & 1)) * 64 + lane) * 16, o);
        }
      }
    }
    asm volatile("s_waitcnt vmcnt(0)" ::: "memory");
    lds_arrive(lf + W_WDONE, lane);
    lds_wait(lf + W_POLL1, 1u);
    {
      const __amdgpu_buffer_rsrc_t rs1 = mk_rsrc(ws + WS_ST1, S * 16);
      const u32 tag = (u32)ep;
      for (u32 spins = 0; spins < (1u << 16); spins++) {
        u32x4 gr[8];
#pragma unroll
        for (int i = 0; i < 8; i++) gr[i] = bload16s(rs1, (u32)s * 16, i * 4096);
        bool ok = true;
#pragma unroll
        for (int i = 0; i < 8; i++) {
          ok &= gr[i][1] == tag && gr[i][3] == tag;
          *(u32x2*)(sm + P_T + (s + 256 * i) * 8) = u32x2{gr[i][0], gr[i][2]};
        }
        if (__all(ok)) break;
        __builtin_amdgcn_s_sleep(16);
      }
    }
    lds_arrive(lf + W_STRDY, lane);
  }
  asm volatile("s_waitcnt lgkmcnt(0)" ::: "memory");
  __builtin_amdgcn_s_barrier();
  const u64 ep = lds_epoch(sm);
  const int rt = 8 * (b & 3) + ((b >> 3) & 7), c = 4 * ((b >> 2) & 1) + (b >> 6);
  const int row0 = 64 * rt;
  {
    const __amdgpu_buffer_rsrc_t rh = mk_rsrc(ws + WS_HT + ((size_t)(64 * w) * S + row0) * 2, 64 * S * 2);
    const int f0 = 2 * ((lane >> 4) & 1);
    const u32 v0 = (u32)(lane >> 3) * S * 2 + (((lane & 7) ^ f0) << 4), v1 = (u32)(lane >> 3) * S * 2 + (((lane & 7) ^ (f0 + 4)) << 4);
#pragma unroll
    for (int i = 0; i < 8; i++) glds16<true>(rh, (i & 1) ? v1 : v0, i * 8 * S * 2, (u32)(size_t)(sm + L_HT) + (64 * w + 8 * i) * 128);
  }
  __builtin_amdgcn_s_barrier();
  const __amdgpu_buffer_rsrc_t rw2 = mk_rsrc(ws + WS_W2T + (size_t)((c * 2 + (w & 1)) * 44 + 11 * (w >> 1)) * 2048, 22 * 1024);
  const int q4 = (lane & 15) >> 2, p4 = lane & 3;
  const int fsw = 2 * ((q4 >> 1) & 1) + 4 * (g & 1);
  auto save_residual = [&]() {
    const char* p = sm + L_HT + (8 * g + q4) * 128 + (((2 * (w >> 1) + (p4 >> 1)) ^ fsw) << 4) + 8 * (p4 & 1) + (2 * c + (w & 1)) * 4096;
    const u32x2 a = tr_read(p), bq = tr_read(p + 512);
    *(u32x4*)(sm + L_RES + (w * 64 + lane) * 16) = u32x4{a[0], a[1], bq[0], bq[1]};
  };
  if (w >= 4) {
    f32x4 csp[4];
    const __amdgpu_buffer_rsrc_t rc = mk_rsrc(ws + WS_CSP + (size_t)c * 176 * 64, 176 * 64);
    const int cj = (w - 4) * 64 + lane;
#pragma unroll
    for (int i = 0; i < 4; i++) csp[i] = __builtin_bit_cast(f32x4, bload16s(rc, (u32)(cj < 176 ? cj : 175) * 64, i * 16));
    asm volatile("s_waitcnt vmcnt(4) lgkmcnt(0)" ::: "memory");
    __builtin_amdgcn_s_barrier();
    __builtin_amdgcn_s_setprio(3);
    const char* p = sm + L_HT + (8 * g + q4) * 128 + (((2 * (w - 4) + (p4 >> 1)) ^ fsw) << 4) + 8 * (p4 & 1);
    u32x2 fa[16][2];
#pragma unroll
    for (int ks = 0; ks < 16; ks++) { fa[ks][0] = tr_read(p + ks * 4096); fa[ks][1] = tr_read(p + ks * 4096 + 512); }
    f32x4 q0 = {0.f, 0.f, 0.f, 0.f}, q1 = {0.f, 0.f, 0.f, 0.f};
    const u32x4 ones = {ONES, ONES, ONES, ONES};
#pragma unroll
    for (int ks = 0; ks < 16; ks++) {
      const u32x4 f0 = {fa[ks][0][0], fa[ks][0][1], fa[ks][1][0], fa[ks][1][1]};
      q0 = mfma(f0, f0, q0);
      q1 = mfma(ones, f0, q1);
    }
    {
      const float lo = (n & 1) ? q0[1] : q0[0], hi = (n & 1) ? q0[3] : q0[2];
      const float q = (n & 2) ? hi : lo;
      const float mu = q1[0] * (1.f / D);
      const float var = fmaxf(q * (1.f / D) - mu * mu, 0.f);
      const float rstd = __builtin_amdgcn_rcpf(rb(__builtin_amdgcn_sqrtf(rb(rb(var) + 1e-5f))));
      if (g == (n >> 2)) *(f32x2*)(sm + L_ST + ((w - 4) * 16 + n) * 8) = f32x2{-1.44269504089f * rstd, rb(mu) * rstd * 1.44269504089f};
    }
    {
      const f32x4 s4 = (csp[0] + csp[1]) + (csp[2] + csp[3]);
      if (cj < 192) *(float*)(sm + L_CS + cj * 4) = (s4[0] + s4[1]) + (s4[2] + s4[3]);
    }
    save_residual();
    asm volatile("s_waitcnt lgkmcnt(0)" ::: "memory");
    if (lane == 0) __hip_atomic_fetch_add((LAS u32*)(lf + W_LN2), 1u, __ATOMIC_RELAXED, __HIP_MEMORY_SCOPE_WORKGROUP);
    __builtin_amdgcn_s_setprio(0);
    lds_wait(lf + W_W1S, 1u);
  } else {
    constexpr int NT = 3, NW1 = 48, PRE = W1_PRE * NT;
    u32x4 w1[NW1];
    const __amdgpu_buffer_rsrc_t rw1 = mk_rsrc(ws + WS_W1T + (size_t)(c * 11 + 3 * w) * 16 * 1024, 48 * 1024);
    auto w1load = [&](int k) { w1[k] = bload16s(rw1, lane * 16, (k % NT) * 16384 + (k / NT) * 1024); };
    auto issued = [&](int ks) constexpr { const int v = PRE + NT * (ks + 1); return v < NW1 ? v : NW1; };
#pragma unroll
    for (int i = 0; i < PRE; i++) w1load(i);
    asm volatile("s_waitcnt vmcnt(%0) lgkmcnt(0)" ::"n"(PRE) : "memory");
    __builtin_amdgcn_s_barrier();
    const char* tb[4];
#pragma unroll
    for (int m = 0; m < 4; m++) tb[m] = sm + L_HT + (8 * g + q4) * 128 + (((2 * m + (p4 >> 1)) ^ fsw) << 4) + 8 * (p4 & 1);
    f32x4 acc[NT][4];
    u32x4 hb[3][4];
#pragma unroll
    for (int i = 0; i < NT; i++)
#pragma unroll
      for (int m = 0; m < 4; m++) acc[i][m] = f32x4{0.f, 0.f, 0.f, 0.f};
#pragma unroll
    for (int d = 0; d < 2; d++)
#pragma unroll
      for (int m = 0; m < 4; m++) {
        const u32x2 a = tr_read(tb[m] + d * 4096), bq = tr_read(tb[m] + d * 4096 + 512);
        hb[d][m] = u32x4{a[0], a[1], bq[0], bq[1]};
      }
#pragma unroll
    for (int ks = 0; ks < 16; ks++) {
#pragma unroll
      for (int m = 0; m < 4; m++) {
        acc[0][m] = mfma(w1[ks * NT], hb[ks % 3][m], acc[0][m]);
        SCHED_FENCE();
        if (ks + 2 < 16) { const u32x2 a = tr_read(tb[m] + (ks + 2) * 4096); hb[(ks + 2) % 3][m][0] = a[0]; hb[(ks + 2) % 3][m][1] = a[1]; }
        SCHED_FENCE();
        acc[1][m] = mfma(w1[ks * NT + 1], hb[ks % 3][m], acc[1][m]);
        SCHED_FENCE();
        if (ks + 2 < 16) { const u32x2 a = tr_read(tb[m] + (ks + 2) * 4096 + 512); hb[(ks + 2) % 3][m][2] = a[0]; hb[(ks + 2) % 3][m][3] = a[1]; }
        SCHED_FENCE();
        acc[2][m] = mfma(w1[ks * NT + 2], hb[ks % 3][m], acc[2][m]);
        SCHED_FENCE();
        if (m < NT && issued(ks - 1) + m < issued(ks)) w1load(issued(ks - 1) + m);
        SCHED_FENCE();
      }
      if (ks == 15 - W1_PRE && w == 0 && lane == 0) *(volatile LAS u32*)(lf + W_W1S) = 1u;
    }
    save_residual();
    lds_wait(lf + W_LN2, 4u);
    float rs[4], mr[4];
    f32x4 cs[NT];
#pragma unroll
    for (int m = 0; m < 4; m++) { const f32x2 v = *(const f32x2*)(sm + L_ST + (m * 16 + n) * 8); rs[m] = v[0]; mr[m] = v[1]; }
#pragma unroll
    for (int i = 0; i < NT; i++) cs[i] = *(const f32x4*)(sm + L_CS + ((3 * w + i) * 16 + 4 * g) * 4);
    const __amdgpu_buffer_rsrc_t hrs = mk_rsrc(ws + WS_HF + (size_t)rt * 44 * 4096, 44 * 4096);
    u32 o_t[NT];
#pragma unroll
    for (int i = 0; i < NT; i++) {
      const int tl = 3 * w + i;
      const int ks2 = tl < 10 ? 5 * c + (tl >> 1) : 40 + (c >> 1), par = tl < 10 ? (tl & 1) : (c & 1);
      o_t[i] = (tl < 11 && (g & 1) == par) ? (u32)(ks2 * 4096 + lane * 16) : 0x40000000u;
    }
#pragma unroll
    for (int m = 0; m < 4; m++) {
#pragma unroll
      for (int i = 0; i < NT; i++) {
        float h1[4];
#pragma unroll
        for (int r = 0; r < 4; r++) h1[r] = silu_val(acc[i][m][r], rs[m], mr[m] * cs[i][r]);
        const u32 p0 = pack_bf(h1[0], h1[1]), p1 = pack_bf(h1[2], h1[3]);
        const auto s0 = __builtin_amdgcn_permlane16_swap(p0, p0, false, false), s1 = __builtin_amdgcn_permlane16_swap(p1, p1, false, false);
        __builtin_amdgcn_raw_buffer_store_b128(u32x4{s0[0], s1[0], s0[1], s1[1]}, hrs, o_t[i] + m * 1024, 0, 16);
      }
      SCHED_FENCE();
    }
  }
  u32x4 w2[22];
#pragma unroll
  for (int i = 0; i < 8; i++) bload16_asm_sc1(w2[i], rw2, lane * 16, i * 1024);
  load_w2_tail_support(w2, rw2, lane * 16, w);
  asm volatile("s_cmp_lt_u32 %0, 4\n\ts_cbranch_scc0 1f\n\ts_waitcnt vmcnt(0)\n1:\n\ts_waitcnt lgkmcnt(0)" ::"s"(w) : "memory", "scc");
  __builtin_amdgcn_s_barrier();
  if (w == 7) {
    if (lane == 0) __hip_atomic_store((GAS u64*)(ws + WS_F3 + (size_t)b * 64), ep, RLX_AGENT);
    poll_flags<1>(ws + WS_F3, (b & 3) + (b & 56) + 4 * ((lane >> 2) & 1) + 64 * (lane & 3), 0, ep);
  }
  if (w < 4) __builtin_amdgcn_s_sleep(2);
  load_w2_tail_main(w2, rw2, lane * 16, w);
  __builtin_amdgcn_s_barrier();

  const int dh = w & 1, kq = w >> 1;
  const __amdgpu_buffer_rsrc_t rhf = mk_rsrc(ws + WS_HF + (size_t)rt * 44 * 4096 + (size_t)(11 * kq * 4 + 2 * dh) * 1024, 11 * 4096);
  const u32 ldst = (u32)(size_t)sm + (kq * 4 + 2 * dh) * 1024;
  auto stage1 = [&](int s, int e) {
    const int slot = s < 9 ? s : s - 9;
    glds16<true>(rhf, lane * 16, s * 4096 + e * 1024, ldst + slot * 16384 + e * 1024);
  };
  auto stage = [&](int s) { stage1(s, 0); stage1(s, 1); };
  stage(0); stage(1); stage(2); stage(3);
  f32x4 acc2[2][4];
#pragma unroll
  for (int i = 0; i < 2; i++)
#pragma unroll
    for (int m = 0; m < 4; m++) acc2[i][m] = f32x4{0.f, 0.f, 0.f, 0.f};
  asm volatile("s_waitcnt vmcnt(4)"
               : "+v"(w2[0]), "+v"(w2[1]), "+v"(w2[2]), "+v"(w2[3]), "+v"(w2[4]), "+v"(w2[5]), "+v"(w2[6]), "+v"(w2[7]), "+v"(w2[8]), "+v"(w2[9]), "+v"(w2[10]),
                 "+v"(w2[11]), "+v"(w2[12]), "+v"(w2[13]), "+v"(w2[14]), "+v"(w2[15]), "+v"(w2[16]), "+v"(w2[17]), "+v"(w2[18]), "+v"(w2[19]), "+v"(w2[20]), "+v"(w2[21])
               ::"memory");
  __builtin_amdgcn_s_barrier();
  {
    const char* base = sm + kq * 4096 + lane * 16;
    u32x4 hb[2][4];
#pragma unroll
    for (int m = 0; m < 4; m++) hb[0][m] = *(const u32x4*)(base + m * 1024);
#pragma unroll
    for (int s = 0; s < 11; s++) {
      if (s == 1) { asm volatile("s_waitcnt vmcnt(4)" ::: "memory"); __builtin_amdgcn_s_barrier(); }
      else if (s == 3 || s == 5) { asm volatile("s_waitcnt vmcnt(6)" ::: "memory"); __builtin_amdgcn_s_barrier(); }
      else if (s == 7) { asm volatile("s_waitcnt vmcnt(2)" ::: "memory"); __builtin_amdgcn_s_barrier(); }
      else if (s == 9) { asm volatile("s_waitcnt vmcnt(0)" ::: "memory"); __builtin_amdgcn_s_barrier(); }
      const int slot = s + 1 < 9 ? s + 1 : s + 1 - 9;
#pragma unroll
      for (int m = 0; m < 4; m++) {
        acc2[0][m] = mfma(w2[2 * s], hb[s & 1][m], acc2[0][m]);
        SCHED_FENCE();
        if (s < 10) hb[(s + 1) & 1][m] = *(const u32x4*)(base + slot * 16384 + m * 1024);
        SCHED_FENCE();
        acc2[1][m] = mfma(w2[2 * s + 1], hb[s & 1][m], acc2[1][m]);
        SCHED_FENCE();
        if (s < 2) stage1(4 + 2 * s + (m >> 1), m & 1);
        else if (s < 5 && (m & 1)) stage1(s + 6, m >> 1);
        SCHED_FENCE();
      }
    }
  }
  asm volatile("s_waitcnt lgkmcnt(0)" ::: "memory");
  __builtin_amdgcn_s_barrier();
  {
    char* red = sm + L_RED + dh * 32768;
    f32x4 o0, o1;
    auto scatter = [&](auto KQ) {
      constexpr int k = decltype(KQ)::value;
#pragma unroll
      for (int m = 0; m < 4; m++) {
        if (m != k) {
#pragma unroll
          for (int i = 0; i < 2; i++) *(f32x4*)(red + (((k * 4 + m) * 2 + i) * 64 + lane) * 16) = acc2[i][m];
        }
      }
      o0 = acc2[0][k]; o1 = acc2[1][k];
      asm volatile("" : "+v"(o0), "+v"(o1));
    };
    if (kq == 0) scatter(IntC<0>{}); else if (kq == 1) scatter(IntC<1>{}); else if (kq == 2) scatter(IntC<2>{}); else scatter(IntC<3>{});
    __syncthreads();
#pragma unroll
    for (int j = 1; j < 4; j++) {
      const int src = (kq + j) & 3;
      o0 += *(const f32x4*)(red + (((src * 4 + kq) * 2 + 0) * 64 + lane) * 16);
      o1 += *(const f32x4*)(red + (((src * 4 + kq) * 2 + 1) * 64 + lane) * 16);
    }
    const u32x4 hres = *(const u32x4*)(sm + L_RES + (w * 64 + lane) * 16);
    u32x4 res;
#pragma unroll
    for (int r = 0; r < 4; r++) res[r] = pack_bf(bf_lo(hres[r]) + rb(o0[r]), bf_hi(hres[r]) + rb(o1[r]));
    *(u32x4*)(OUT + (size_t)(row0 + 16 * kq + n) * D + 64 * c + 32 * dh + 8 * g) = res;
  }
  asm volatile("ffn_text_end:");
}

extern "C" void kernel_launch(void* const* d_in, const int* in_sizes, int n_in, void* d_out, int out_size, void* d_ws,
                              size_t ws_size, hipStream_t stream) {
  fused_block<<<256, 512, 0, stream>>>((const u16*)d_in[0], (const u16*)d_in[1], (const u16*)d_in[2], (const u16*)d_in[3], (const u16*)d_in[4],
                                       (const u16*)d_in[5], (const u16*)d_in[7], (const u16*)d_in[8], (char*)d_ws + ((ws_size - (32u  __H0__ )) & ~(size_t)0xFFFFF), (u16*)d_out);
}
```

```cpp
#include <hip/hip_runtime.h>
#include <stdint.h>

typedef unsigned short u16;
typedef unsigned int u32;
typedef unsigned long long u64;
typedef __bf16 bf16x8 __attribute__((ext_vector_type(8)));
typedef __bf16 bf16x2 __attribute__((ext_vector_type(2)));
typedef short s16x4 __attribute__((ext_vector_type(4)));
typedef float f32x4 __attribute__((ext_vector_type(4)));
typedef float f32x2 __attribute__((ext_vector_type(2)));
typedef u32 u32x4 __attribute__((ext_vector_type(4)));
typedef u32 u32x2 __attribute__((ext_vector_type(2)));
#define GAS __attribute__((address_space(1)))
#define LAS __attribute__((address_space(3)))
#define RLX_AGENT __ATOMIC_RELAXED, __HIP_MEMORY_SCOPE_AGENT
#define SCHED_FENCE() __builtin_amdgcn_sched_barrier(0)

constexpr int S = 2048, D = 512, F = 1376;

constexpr size_t WS_EP   = 0;
constexpr size_t WS_F2   = 4096;
constexpr size_t WS_F3   = WS_F2 + 32768;
constexpr size_t WS_FW   = WS_F3 + 32768;
constexpr size_t WS_KVT  = 1 << 17;
constexpr size_t WS_YT   = WS_KVT + (size_t)D * S * 4;
constexpr size_t WS_W1T  = WS_YT + (size_t)D * S * 2;
constexpr size_t WS_W2T  = WS_W1T + 8 * 11 * 16 * 1024;
constexpr size_t WS_CSP  = WS_W2T + 8 * 4 * 44 * 1024;
constexpr size_t WS_HF   = WS_CSP + 16 * 8 * 11 * 16 * 4;
constexpr size_t WS_END  = WS_HF + (size_t)32 * 44 * 4 * 1024;

__device__ __forceinline__ float bf_lo(u32 v) { return __uint_as_float(v << 16); }
__device__ __forceinline__ float bf_hi(u32 v) { return __uint_as_float(v & 0xffff0000u); }
__device__ __forceinline__ u32 pack_bf(float a, float b) {
  f32x2 z = {a, b};
  return __builtin_bit_cast(u32, __builtin_convertvector(z, bf16x2));
}
__device__ __forceinline__ float rb(float a) { return bf_lo(pack_bf(a, 0.f)); }
__device__ __forceinline__ float dot2(u32 a, u32 b, float c) {
  return __builtin_amdgcn_fdot2_f32_bf16(__builtin_bit_cast(bf16x2, a), __builtin_bit_cast(bf16x2, b), c, false);
}
__device__ __forceinline__ u32 perm_lo(u32 a, u32 b) { return __builtin_amdgcn_perm(b, a, 0x05040100u); }
__device__ __forceinline__ u32 perm_hi(u32 a, u32 b) { return __builtin_amdgcn_perm(b, a, 0x07060302u); }
__device__ __forceinline__ f32x4 mfma(u32x4 a, u32x4 b, f32x4 c) {
  return __builtin_amdgcn_mfma_f32_16x16x32_bf16(__builtin_bit_cast(bf16x8, a), __builtin_bit_cast(bf16x8, b), c, 0, 0, 0);
}
constexpr u32 ONES = 0x3F803F80u;
template <int V> struct IntC { static constexpr int value = V; };
__device__ __forceinline__ float sum_g(float x) {
  auto s = __builtin_amdgcn_permlane16_swap(__float_as_uint(x), __float_as_uint(x), false, false);
  x = __uint_as_float(s[0]) + __uint_as_float(s[1]);
  auto t = __builtin_amdgcn_permlane32_swap(__float_as_uint(x), __float_as_uint(x), false, false);
  return __uint_as_float(t[0]) + __uint_as_float(t[1]);
}
template <int CTRL>
__device__ __forceinline__ float dpp_add(float x) {
  return x + __uint_as_float(__builtin_amdgcn_update_dpp(0u, __float_as_uint(x), CTRL, 0xf, 0xf, true));
}
__device__ __forceinline__ float wave_sum(float x) {
  x = dpp_add<0xB1>(x);
  x = dpp_add<0x4E>(x);
  x = dpp_add<0x141>(x);
  x = dpp_add<0x140>(x);
  return sum_g(x);
}
__device__ __forceinline__ __amdgpu_buffer_rsrc_t mk_rsrc(const void* p, u32 bytes) {
  return __builtin_amdgcn_make_buffer_rsrc((void*)p, 0, bytes, 0x00020000);
}
__device__ __forceinline__ u32x4 bload16(__amdgpu_buffer_rsrc_t r, u32 off) { return __builtin_amdgcn_raw_buffer_load_b128(r, off, 0, 0); }
__device__ __forceinline__ u32x2 bload8(__amdgpu_buffer_rsrc_t r, u32 off) { return __builtin_amdgcn_raw_buffer_load_b64(r, off, 0, 0); }
template <bool SC1>
__device__ __forceinline__ void glds16(__amdgpu_buffer_rsrc_t r, u32 voff, u32 soff, u32 lds_dst) {
  if (SC1)
    asm volatile("s_mov_b32 m0, %0\n\ts_nop 0\n\tbuffer_load_dwordx4 %1, %2, %3 offen sc1 lds" ::"s"(lds_dst), "v"(voff), "s"(r), "s"(soff) : "memory");
  else
    asm volatile("s_mov_b32 m0, %0\n\ts_nop 0\n\tbuffer_load_dwordx4 %1, %2, %3 offen lds" ::"s"(lds_dst), "v"(voff), "s"(r), "s"(soff) : "memory");
}
__device__ __forceinline__ void bload16_asm(u32x4& d, __amdgpu_buffer_rsrc_t r, u32 voff, u32 soff) {
  asm volatile("buffer_load_dwordx4 %0, %1, %2, %3 offen" : "=v"(d) : "v"(voff), "s"(r), "s"(soff) : "memory");
}
__device__ __forceinline__ void bload16_asm_sc1(u32x4& d, __amdgpu_buffer_rsrc_t r, u32 voff, u32 soff) {
  asm volatile("buffer_load_dwordx4 %0, %1, %2, %3 offen sc1" : "=v"(d) : "v"(voff), "s"(r), "s"(soff) : "memory");
}
template <int AUX>
__device__ __forceinline__ void gstore16(char* base, size_t off, u32x4 v) {
  const __amdgpu_buffer_rsrc_t r = mk_rsrc(base, 0x7fffffffu);
  __builtin_amdgcn_raw_buffer_store_b128(v, r, (u32)off, 0, AUX);
}
template <int AUX>
__device__ __forceinline__ void gstore8(char* base, size_t off, u32x2 v) {
  const __amdgpu_buffer_rsrc_t r = mk_rsrc(base, 0x7fffffffu);
  __builtin_amdgcn_raw_buffer_store_b64(v, r, (u32)off, 0, AUX);
}
__device__ __forceinline__ u32x4 bload16s(__amdgpu_buffer_rsrc_t r, u32 voff, u32 soff) { return __builtin_amdgcn_raw_buffer_load_b128(r, voff, soff, 16); }
__device__ __forceinline__ u32x2 tr_read(const char* p) {
  s16x4 r = __builtin_amdgcn_ds_read_tr16_b64_v4i16((LAS s16x4*)p);
  return __builtin_bit_cast(u32x2, r);
}

__device__ __forceinline__ void code_prefetch(int part, int nparts, int lane, u32 lds_dump) {
  u64 pc; u32 len;
  asm volatile("s_getpc_b64 %0\n\ts_mov_b32 %1, ffn_text_end-." : "=s"(pc), "=s"(len));
  const __amdgpu_buffer_rsrc_t r = mk_rsrc((const void*)pc, len);
  if ((u32)part * 1024 < len) glds16<false>(r, lane * 16, part * 1024, lds_dump);
}
__device__ __forceinline__ int fcol(int ks, int g, int j) {
  const int slot = 8 * (g >> 1) + j;
  if (ks < 40) { const int ct = ks / 5, p = ks - 5 * ct; return 128 * (2 * p + (g & 1)) + 8 * slot + ct; }
  return 1280 + 8 * slot + 2 * (ks - 40) + (g & 1);
}

constexpr int HC_STRIDE = 4608;
constexpr int HC_CH = 8 * HC_STRIDE;
constexpr int U_CH = (240 + 2048) * 2;
constexpr int P_H = 0, P_U = 2 * HC_CH, P_R = P_U + 2 * U_CH + 32;
constexpr int P_T = P_R + 4 * 4096;
constexpr int P_END = P_T + 8192;
constexpr int L_H = 0;
constexpr int L_Y = 65536;
constexpr int L_X1 = 131072;
constexpr int L_ST = 147456;
constexpr int L_CS = L_ST + 512;
constexpr int L_RES = L_CS + 768;
constexpr int L_RED = 0;
constexpr int L_FLG = L_RES + 8192;
constexpr int L_TOT = L_FLG + 128;
enum { W_CA = 12, W_CB = 16, W_HRDY = 24, W_CD = 28, W_PART = 32, W_WDONE = 56, W_CE = 60, W_FWOK = 20, W_LN2 = 64, W_W1S = 68 };
static_assert(P_END <= L_X1 && L_TOT <= 163840, "LDS map");
#define KV_SLEEP 8
#define KV_FIRST 32
#define W1_PRE 6
__device__ __forceinline__ constexpr int hoff(int c) {
  return c == 0 ? 4 : c == 1 ? 2 : c == 2 ? 8 : c == 3 ? 3 : c == 4 ? 7 : c == 5 ? 0 : c == 6 ? 15 : 12;
}
__device__ __forceinline__ int hoff_rt(int c) { return (0xCF073824u >> (4 * c)) & 15; }
__device__ __forceinline__ void lds_wait(const char* p, u32 want) {
  for (u32 spins = 0; spins < (1u << 22); spins++) {
    if (__builtin_amdgcn_readfirstlane(*(volatile LAS u32*)p) == want) break;
    __builtin_amdgcn_s_sleep(1);
  }
  asm volatile("" ::: "memory");
}
__device__ __forceinline__ void lds_arrive(char* p, int lane) {
  asm volatile("s_waitcnt lgkmcnt(0)" ::: "memory");
  if (lane == 0) __hip_atomic_fetch_add((LAS u32*)p, 1u, __ATOMIC_RELAXED, __HIP_MEMORY_SCOPE_WORKGROUP);
}
__device__ __forceinline__ void lds_set(char* p, u32 v, int lane) {
  asm volatile("s_waitcnt lgkmcnt(0)" ::: "memory");
  if (lane == 0) *(volatile LAS u32*)p = v;
}
__device__ __forceinline__ void flag_store(char* base, int idx, u64 ep, int lane) {
  if (lane < 16) __hip_atomic_store((GAS u64*)(base + (size_t)idx * 128 + lane * 8), ep, RLX_AGENT);
}
template <int N>
__device__ __forceinline__ void poll_flags(const char* base, int first, int stride, u64 ep) {
  for (u32 spins = 0; spins < (1u << 20); spins++) {
    bool ok = true;
#pragma unroll
    for (int i = 0; i < N; i++) ok &= __hip_atomic_load((GAS u64*)(base + (size_t)(first + i * stride) * 128), RLX_AGENT) == ep;
    if (__all(ok)) break;
    __builtin_amdgcn_s_sleep(1);
  }
  asm volatile("" ::: "memory");
}

#define W2_LOADS                                                                                                                   \
  "buffer_load_dwordx4 %0, %14, %15, %16 offen sc1\n\tbuffer_load_dwordx4 %1, %14, %15, %16 offen offset:1024 sc1\n\t"                      \
  "buffer_load_dwordx4 %2, %14, %15, %16 offen offset:2048 sc1\n\tbuffer_load_dwordx4 %3, %14, %15, %16 offen offset:3072 sc1\n\t"          \
  "buffer_load_dwordx4 %4, %14, %15, %17 offen sc1\n\tbuffer_load_dwordx4 %5, %14, %15, %17 offen offset:1024 sc1\n\t"                      \
  "buffer_load_dwordx4 %6, %14, %15, %17 offen offset:2048 sc1\n\tbuffer_load_dwordx4 %7, %14, %15, %17 offen offset:3072 sc1\n\t"          \
  "buffer_load_dwordx4 %8, %14, %15, %18 offen sc1\n\tbuffer_load_dwordx4 %9, %14, %15, %18 offen offset:1024 sc1\n\t"                      \
  "buffer_load_dwordx4 %10, %14, %15, %18 offen offset:2048 sc1\n\tbuffer_load_dwordx4 %11, %14, %15, %18 offen offset:3072 sc1\n\t"        \
  "buffer_load_dwordx4 %12, %14, %15, %19 offen sc1\n\tbuffer_load_dwordx4 %13, %14, %15, %19 offen offset:1024 sc1\n"
__device__ __forceinline__ void load_w2_tail_support(u32x4 (&w2)[22], __amdgpu_buffer_rsrc_t r, u32 voff, int w) {
  asm volatile("s_cmp_lt_u32 %20, 4\n\ts_cbranch_scc1 1f\n\t" W2_LOADS "1:"
               : "=&v"(w2[8]), "=&v"(w2[9]), "=&v"(w2[10]), "=&v"(w2[11]), "=&v"(w2[12]), "=&v"(w2[13]), "=&v"(w2[14]), "=&v"(w2[15]), "=&v"(w2[16]),
                 "=&v"(w2[17]), "=&v"(w2[18]), "=&v"(w2[19]), "=&v"(w2[20]), "=&v"(w2[21])
               : "v"(voff), "s"(r), "s"(8192), "s"(12288), "s"(16384), "s"(20480), "s"(w) : "memory", "scc");
}
__device__ __forceinline__ void load_w2_tail_main(u32x4 (&w2)[22], __amdgpu_buffer_rsrc_t r, u32 voff, int w) {
  asm volatile("s_cmp_lt_u32 %20, 4\n\ts_cbranch_scc0 1f\n\t" W2_LOADS "1:"
               : "+v"(w2[8]), "+v"(w2[9]), "+v"(w2[10]), "+v"(w2[11]), "+v"(w2[12]), "+v"(w2[13]), "+v"(w2[14]), "+v"(w2[15]), "+v"(w2[16]),
                 "+v"(w2[17]), "+v"(w2[18]), "+v"(w2[19]), "+v"(w2[20]), "+v"(w2[21])
               : "v"(voff), "s"(r), "s"(8192), "s"(12288), "s"(16384), "s"(20480), "s"(w) : "memory", "scc");
}
__device__ __forceinline__ float silu_val(float acc, float rs, float bias) {
  const float tt = fmaf(acc, rs, bias);
  const float e = __builtin_amdgcn_exp2f(tt);
  return tt * __builtin_amdgcn_rcpf(fmaf(e, -1.44269504089f, -1.44269504089f));
}

__global__ __launch_bounds__(512, 2) void fused_block(const u16* __restrict__ X, const u16* __restrict__ K, const u16* __restrict__ V,
                                                      const u16* __restrict__ SK, const u16* __restrict__ HK, const u16* __restrict__ DV,
                                                      const u16* __restrict__ W1, const u16* __restrict__ W2, char* __restrict__ ws,
                                                      u16* __restrict__ OUT) {
  __shared__ __attribute__((aligned(16))) char sm[L_TOT];
  const int b = blockIdx.x, t = threadIdx.x, lane = t & 63;
  const int w = __builtin_amdgcn_readfirstlane(t >> 6);
  const int n = lane & 15, g = lane >> 4;
  char* lf = sm + L_FLG;
  if (t < 32) *(volatile LAS u32*)(lf + t * 4) = 0u;
  asm volatile("s_waitcnt lgkmcnt(0)" ::: "memory");
  __builtin_amdgcn_s_barrier();
  const int chp = 64 * (b & 7) + 2 * (b >> 3);
  const int rt = 8 * (b & 3) + ((b >> 3) & 7), c = 4 * ((b >> 2) & 1) + (b >> 6);
  const int row0 = 64 * rt;
  const u64 ep = *(const u64*)(ws + WS_EP) + 1;
  const u32 dvp = *(const u32*)(DV + chp);

  u32x4 wl[8];
  const int unit = w >= 4 ? b + 256 * (w - 4) : 1 << 20;
  if (w < 4) {
    const int dt = b & 7, st = b >> 3;
    const int rp = t >> 3, c8 = t & 7;
    const int s = st * 64 + 2 * rp, ch0 = dt * 64 + c8 * 8;
    const __amdgpu_buffer_rsrc_t rk = mk_rsrc(K, S * D * 2);
    const u32 off = (u32)(s * D + ch0) * 2;
    u32x4 kk[5];
#pragma unroll
    for (int i = 0; i < 5; i++) kk[i] = bload16(rk, off + (1 - i) * 1024);
    const u32x4 v0 = *(const u32x4*)((const char*)V + off), v1 = *(const u32x4*)((const char*)V + off + 1024);
    u32x4 wa[4];
#pragma unroll
    for (int i = 0; i < 4; i++) wa[i] = *(const u32x4*)(SK + ch0 * 4 + i * 8);
    SCHED_FENCE();
    char* pk = sm + P_T + (8 * c8) * 128 + (((rp >> 2) ^ c8) << 4) + ((3 - (rp & 3)) << 2);
#pragma unroll
    for (int i = 0; i < 4; i++) {
      const u32 l01 = perm_lo(kk[0][i], kk[1][i]), l12 = perm_lo(kk[1][i], kk[2][i]), l23 = perm_lo(kk[2][i], kk[3][i]), l34 = perm_lo(kk[3][i], kk[4][i]);
      const u32 h01 = perm_hi(kk[0][i], kk[1][i]), h12 = perm_hi(kk[1][i], kk[2][i]), h23 = perm_hi(kk[2][i], kk[3][i]), h34 = perm_hi(kk[3][i], kk[4][i]);
      const float a1 = dot2(l01, wa[i][0], dot2(l23, wa[i][1], 0.f)) * bf_lo(v1[i]);
      const float a0 = dot2(l12, wa[i][0], dot2(l34, wa[i][1], 0.f)) * bf_lo(v0[i]);
      const float b1 = dot2(h01, wa[i][2], dot2(h23, wa[i][3], 0.f)) * bf_hi(v1[i]);
      const float b0 = dot2(h12, wa[i][2], dot2(h34, wa[i][3], 0.f)) * bf_hi(v0[i]);
      *(u32*)(pk + (2 * i) * 128) = pack_bf(a1, a0);
      *(u32*)(pk + (2 * i + 1) * 128) = pack_bf(b1, b0);
    }
    lds_arrive(lf + W_CA, lane);
    lds_wait(lf + W_CA, 4u);
    const u32 tag = (u32)ep;
#pragma unroll
    for (int i = 0; i < 2; i++) {
      const int u = t + 256 * i, ch = u >> 3, cc = u & 7;
#pragma unroll
      for (int hh = 0; hh < 2; hh++) {
        const int grp = 4 * hh + (cc >> 1);
        const u32x2 d = *(const u32x2*)(sm + P_T + ch * 128 + ((grp ^ ((ch >> 3) & 7)) << 4) + (cc & 1) * 8);
        gstore16<16>(ws, WS_KVT + (size_t)(dt * 64 + ch) * 8192 + (st * 8 + 4 * hh) * 32 + cc * 16, u32x4{d[0], tag, d[1], tag});
      }
    }
    lds_arrive(lf + W_CB, lane);
    {
      const __amdgpu_buffer_rsrc_t ru = mk_rsrc(ws + WS_KVT + (size_t)chp * 8192, 2 * 8192);
      if (t < 60) *(u32x4*)(sm + P_U + (t < 30 ? 0 : U_CH - 480) + 16 * t) = u32x4{0, 0, 0, 0};
      u32x4 ga[2][2];
      __builtin_amdgcn_s_sleep(KV_FIRST);
      for (u32 spins = 0; spins < (1u << 18); spins++) {
#pragma unroll
        for (int cl = 0; cl < 2; cl++) { ga[cl][0] = bload16s(ru, t * 32, cl * 8192); ga[cl][1] = bload16s(ru, t * 32 + 16, cl * 8192); }
        bool ok = true;
#pragma unroll
        for (int cl = 0; cl < 2; cl++) ok &= ga[cl][0][1] == tag && ga[cl][0][3] == tag && ga[cl][1][1] == tag && ga[cl][1][3] == tag;
        if (__all(ok)) break;
        __builtin_amdgcn_s_sleep(KV_SLEEP);
      }
#pragma unroll
      for (int cl = 0; cl < 2; cl++) *(u32x4*)(sm + P_U + cl * U_CH + 480 + 16 * t) = u32x4{ga[cl][0][0], ga[cl][0][2], ga[cl][1][0], ga[cl][1][2]};
    }
    lds_arrive(lf + W_CD, lane);
    lds_wait(lf + W_HRDY, 4u);
    lds_wait(lf + W_CD, 4u);
  } else {
    const int s = t - 256;
    u32x4 ha[2], hb[2];
    const __amdgpu_buffer_rsrc_t rh_ = mk_rsrc(HK + (size_t)chp * S, 2 * S * 2);
#pragma unroll
    for (int cl = 0; cl < 2; cl++) { ha[cl] = bload16(rh_, (u32)(cl * S + 8 * s) * 2 - 16); hb[cl] = bload16(rh_, (u32)(cl * S + 8 * s) * 2); }
    if (w == 7) {
      const char* p;
      if (lane < 24) p = ws + (size_t)lane * (1 << 20);
      else if (lane < 27) p = (const char*)OUT + (size_t)(lane - 24) * ((1 << 20) - 64);
      else if (lane < 30) p = (const char*)X + (size_t)(lane - 27) * ((1 << 20) - 64);
      else if (lane < 32) p = (const char*)W1 + (size_t)(lane - 30) * (D * F * 2 - 64);
      else if (lane < 34) p = (const char*)W2 + (size_t)(lane - 32) * (D * F * 2 - 64);
      else p = ws + WS_F2 + (size_t)(lane - 34) * 4096;
      asm volatile("s_mov_b32 m0, %0\n\ts_nop 0\n\tglobal_load_lds_dword %1, off" ::"s"((u32)(size_t)(sm + L_ST + 1024)), "v"(p) : "memory");
    }
    code_prefetch((b >> 3) * 4 + (w - 4), 128, lane, (u32)(size_t)(sm + L_ST));
    SCHED_FENCE();
#pragma unroll
    for (int cl = 0; cl < 2; cl++) {
      if (s == 0) ha[cl] = u32x4{0, 0, 0, 0};
      char* hbase = sm + P_H + cl * HC_CH;
      const u32 W[8] = {ha[cl][0], ha[cl][1], ha[cl][2], ha[cl][3], hb[cl][0], hb[cl][1], hb[cl][2], hb[cl][3]};
#pragma unroll
      for (int cc = 0; cc < 8; cc++) {
        u32x4 o;
#pragma unroll
        for (int a = 0; a < 4; a++) o[a] = (cc & 1) ? __builtin_amdgcn_alignbit(W[(cc >> 1) + a + 1], W[(cc >> 1) + a], 16) : W[(cc >> 1) + a];
        *(u32x4*)(hbase + cc * HC_STRIDE + 16 * (s + 1 + hoff(cc))) = o;
      }
      if (s < 8) *(u32x4*)(hbase + s * HC_STRIDE + 16 * hoff_rt(s)) = u32x4{0, 0, 0, 0};
      if (s == 255) *(u32x4*)(hbase + 16 * (257 + hoff(0))) = hb[cl];
    }
    lds_arrive(lf + W_HRDY, lane);
    lds_wait(lf + W_CB, 4u);
    {
      const int lc = lane & 15, lg = lane >> 4;
      if (unit < 176) {
        const __amdgpu_buffer_rsrc_t rw = mk_rsrc(W1, D * F * 2);
        const int blk = unit >> 4, ks = unit & 15;
        const int col = 128 * blk + 8 * lc;
        const u32 o0 = col < F ? (u32)((32 * ks + 8 * lg) * F + col) * 2 : 0x80000000u;
#pragma unroll
        for (int j = 0; j < 8; j++) wl[j] = bload16(rw, o0 + j * F * 2);
      } else if (unit < 528) {
        const __amdgpu_buffer_rsrc_t rw = mk_rsrc(W2, D * F * 2);
        const int u2 = unit - 176, ks = u2 >> 3, c2 = u2 & 7;
#pragma unroll
        for (int j = 0; j < 8; j++) {
          const u32 o = (u32)(fcol(ks, lg, j) * D + 64 * c2 + 8 * (lc >> 2) + 2 * (lc & 3)) * 2;
          wl[j][0] = __builtin_amdgcn_raw_buffer_load_b32(rw, o, 0, 0);
          wl[j][1] = __builtin_amdgcn_raw_buffer_load_b32(rw, o + 64, 0, 0);
        }
      }
    }
  }

  u32x4 xf[2][4];
  auto load_x = [&]() {
#pragma unroll
    for (int kk = 0; kk < 2; kk++)
#pragma unroll
      for (int m = 0; m < 4; m++) xf[kk][m] = *(const u32x4*)(X + (size_t)(row0 + 16 * m + n) * D + 32 * (2 * w + kk) + 8 * g);
  };
  auto ln1_partials = [&]() {
    const u32x4 ones = {ONES, ONES, ONES, ONES};
#pragma unroll
    for (int m = 0; m < 4; m++) {
      f32x4 q0 = {0.f, 0.f, 0.f, 0.f}, q1 = {0.f, 0.f, 0.f, 0.f};
#pragma unroll
      for (int kk = 0; kk < 2; kk++) { q0 = mfma(xf[kk][m], xf[kk][m], q0); q1 = mfma(ones, xf[kk][m], q1); }
      const float lo = (n & 1) ? q0[1] : q0[0], hi = (n & 1) ? q0[3] : q0[2];
      if (g == (n >> 2)) *(f32x2*)(sm + L_X1 + ((w * 4 + m) * 16 + n) * 8) = f32x2{q1[0], (n & 2) ? hi : lo};
    }
  };
  constexpr int NT = 3, NW1 = 48, PRE = W1_PRE * NT;
  u32x4 w1[NW1];
  const __amdgpu_buffer_rsrc_t rw1 = mk_rsrc(ws + WS_W1T + (size_t)(c * 11 + 3 * (w & 3)) * 16 * 1024, 48 * 1024);
  auto w1load = [&](int k) { w1[k] = bload16s(rw1, lane * 16, (k % NT) * 16384 + (k / NT) * 1024); };
  if (w < 4) {
    const int wcl = (w >> 1) & 1, rh = w & 1;
    const u32 dvr = dvp >> (16 * wcl);
    f32x4 acc[8];
#pragma unroll
    for (int i = 0; i < 8; i++) acc[i] = f32x4{0.f, 0.f, 0.f, 0.f};
    const int idx0 = 25 + n - 8 * g;
    const int cc = idx0 & 7;
    const char* fbase = sm + P_H + wcl * HC_CH + cc * HC_STRIDE + 16 * ((idx0 >> 3) + hoff_rt(cc)) + 64 * 4 * rh;
    const char* gbase = sm + P_U + wcl * U_CH + 2 * (16 * n + 8 * g + 224) - 64 * 4 * rh;
    u32x4 Ff[2][8], Gf[2][8];
#pragma unroll
    for (int a = 0; a < 8; a++) { Gf[0][a] = *(const u32x4*)(gbase + 64 * (8 * a)); Ff[0][a] = *(const u32x4*)(fbase + 64 * (8 * a)); }
    SCHED_FENCE();
#pragma unroll
    for (int rr = 0; rr < 4; rr++) {
      const int cur = rr & 1, nxt = cur ^ 1;
      int m = 0;
#pragma unroll
      for (int bb = 0; bb < 8; bb++) {
#pragma unroll
        for (int a = 0; a + bb < 8; a++) {
          acc[a + bb] = mfma(Ff[cur][a], Gf[cur][bb], acc[a + bb]);
          if (rr < 3 && (m & 1) == 1 && m < 32) {
            const int q = m >> 2;
            SCHED_FENCE();
            if (m & 2) Ff[nxt][q] = *(const u32x4*)(fbase + 64 * (8 * q + rr + 1));
            else Gf[nxt][q] = *(const u32x4*)(gbase + 64 * (8 * q - rr - 1));
            SCHED_FENCE();
          }
          m++;
        }
      }
      SCHED_FENCE();
    }
    f32x4* rbuf = (f32x4*)(sm + P_R) + w * 256;
    if (rh) {
#pragma unroll
      for (int i = 0; i < 4; i++) rbuf[i * 64 + lane] = acc[i];
#pragma unroll
      for (int i = 0; i < 4; i++) acc[i] = acc[4 + i];
    } else {
#pragma unroll
      for (int i = 0; i < 4; i++) rbuf[i * 64 + lane] = acc[4 + i];
    }
    lds_set(lf + W_PART + w * 4, 1u, lane);
    load_x();
    const int ch = chp + wcl;
    const char* ub = sm + P_U + wcl * U_CH;
    u32x2 uq[4];
#pragma unroll
    for (int i = 0; i < 4; i++) uq[i] = *(const u32x2*)(ub + 2 * (240 + 256 * (4 * rh + i) + 16 * n + 8 * (g >> 1) + ((g & 1) ? 0 : 4)));
    lds_wait(lf + W_PART + (w ^ 1) * 4, 1u);
    const float dv = bf_lo(dvr);
    const f32x4* pbuf = (const f32x4*)(sm + P_R) + (w ^ 1) * 256;
#pragma unroll
    for (int i = 0; i < 4; i++) {
      const int T = 4 * rh + i;
      const f32x4 a = acc[i] + pbuf[i * 64 + lane];
      const int pos = 256 * T + 16 * n + 4 * g;
      const float y0 = rb(a[0]) + rb(bf_hi(uq[i][1]) * dv), y1 = rb(a[1]) + rb(bf_lo(uq[i][1]) * dv);
      const float y2 = rb(a[2]) + rb(bf_hi(uq[i][0]) * dv), y3 = rb(a[3]) + rb(bf_lo(uq[i][0]) * dv);
      gstore8<16>(ws, WS_YT + ((size_t)ch * S + pos) * 2, u32x2{pack_bf(y0, y1), pack_bf(y2, y3)});
    }
    __builtin_amdgcn_s_waitcnt(0x0f70);
    asm volatile("s_waitcnt vmcnt(0)" ::: "memory");
    lds_arrive(lf + W_CE, lane);
    if (w == 0) {
      lds_wait(lf + W_CE, 4u);
      flag_store(ws + WS_F2, b, ep, lane);
    }
    ln1_partials();
    lds_wait(lf + W_FWOK, 1u);
#pragma unroll
    for (int i = 0; i < PRE; i++) w1load(i);
    SCHED_FENCE();
    if (w == 0) {
      poll_flags<4>(ws + WS_F2, lane, 64, ep);
      if (b == 0 && lane == 0) __hip_atomic_store((GAS u64*)(ws + WS_EP), ep, RLX_AGENT);
    }
  } else {
    if (unit < 176) {
      const int blk = unit >> 4, ks = unit & 15;
      float csel[2] = {0.f, 0.f};
#pragma unroll
      for (int i = 0; i < 8; i++) {
        u32x4 o;
#pragma unroll
        for (int a = 0; a < 4; a++) o[a] = (i & 1) ? perm_hi(wl[2 * a][i >> 1], wl[2 * a + 1][i >> 1]) : perm_lo(wl[2 * a][i >> 1], wl[2 * a + 1][i >> 1]);
        gstore16<16>(ws, WS_W1T + ((size_t)((i * 11 + blk) * 16 + ks) * 64 + lane) * 16, o);
        const float cs = sum_g(dot2(o[0], ONES, dot2(o[1], ONES, dot2(o[2], ONES, dot2(o[3], ONES, 0.f)))));
        csel[i >> 2] = g == (i & 3) ? cs : csel[i >> 2];
      }
#pragma unroll
      for (int hh = 0; hh < 2; hh++)
        __hip_atomic_store((GAS u32*)(ws + WS_CSP + (size_t)(ks * 11 + blk) * 512 + hh * 256 + lane * 4), __float_as_uint(csel[hh]), RLX_AGENT);
    } else if (unit < 528) {
      const int u2 = unit - 176, ks = u2 >> 3, c2 = u2 & 7;
#pragma unroll
      for (int i = 0; i < 4; i++) {
        u32x4 o;
#pragma unroll
        for (int a = 0; a < 4; a++) o[a] = (i & 1) ? perm_hi(wl[2 * a][i >> 1], wl[2 * a + 1][i >> 1]) : perm_lo(wl[2 * a][i >> 1], wl[2 * a + 1][i >> 1]);
        gstore16<16>(ws, WS_W2T + ((size_t)(((c2 * 2 + (i >> 1)) * 44 + ks) * 2 + (i & 1)) * 64 + lane) * 16, o);
      }
    }
    __builtin_amdgcn_s_waitcnt(0x0f70);
    asm volatile("s_waitcnt vmcnt(0)" ::: "memory");
    lds_arrive(lf + W_WDONE, lane);
    if (w == 4) {
      lds_wait(lf + W_WDONE, 4u);
      flag_store(ws + WS_FW, b, ep, lane);
    }
    load_x();
    ln1_partials();
    if (w == 4) {
      poll_flags<4>(ws + WS_FW, lane, 64, ep);
      lds_set(lf + W_FWOK, 1u, lane);
    }
  }
  asm volatile("s_waitcnt lgkmcnt(0)" ::: "memory");
  __builtin_amdgcn_s_barrier();
  {
    const __amdgpu_buffer_rsrc_t rh = mk_rsrc(ws + WS_YT + ((size_t)(64 * w) * S + row0) * 2, 64 * S * 2);
    const int f0 = 2 * ((lane >> 4) & 1);
    const u32 v0 = (u32)(lane >> 3) * S * 2 + (((lane & 7) ^ f0) << 4), v1 = (u32)(lane >> 3) * S * 2 + (((lane & 7) ^ (f0 + 4)) << 4);
#pragma unroll
    for (int i = 0; i < 8; i++) glds16<true>(rh, (i & 1) ? v1 : v0, i * 8 * S * 2, (u32)(size_t)(sm + L_Y) + (64 * w + 8 * i) * 128);
  }
  const __amdgpu_buffer_rsrc_t rw2 = mk_rsrc(ws + WS_W2T + (size_t)((c * 2 + (w & 1)) * 44 + 11 * (w >> 1)) * 2048, 22 * 1024);
  float csp[16];
  const int cj = (w & 3) * 64 + lane;
  float r1[4], c1[4];
#pragma unroll
  for (int m = 0; m < 4; m++) {
    float a = 0.f, q = 0.f;
#pragma unroll
    for (int ww = 0; ww < 8; ww++) { const f32x2 v = *(const f32x2*)(sm + L_X1 + ((ww * 4 + m) * 16 + n) * 8); a += v[0]; q += v[1]; }
    const float mu = a * (1.f / D);
    const float var = fmaxf(q * (1.f / D) - mu * mu, 0.f);
    r1[m] = __builtin_amdgcn_rcpf(rb(__builtin_amdgcn_sqrtf(rb(rb(var) + 1e-5f))));
    c1[m] = -rb(mu) * r1[m];
  }
  asm volatile("s_waitcnt vmcnt(0)" ::: "memory");
  __builtin_amdgcn_s_barrier();
  const int q4 = (lane & 15) >> 2, p4 = lane & 3;
  {
    const int fsw = 2 * ((q4 >> 1) & 1) + 4 * (g & 1);
    u32x2 ya[2][4][2];
#pragma unroll
    for (int kk = 0; kk < 2; kk++)
#pragma unroll
      for (int m = 0; m < 4; m++) {
        const char* p = sm + L_Y + (8 * g + q4) * 128 + (((2 * m + (p4 >> 1)) ^ fsw) << 4) + 8 * (p4 & 1) + (2 * w + kk) * 4096;
        ya[kk][m][0] = tr_read(p); ya[kk][m][1] = tr_read(p + 512);
      }
    SCHED_FENCE();
#pragma unroll
    for (int kk = 0; kk < 2; kk++)
#pragma unroll
      for (int m = 0; m < 4; m++) {
        f32x2 xv[4], yv[4], qv[4];
#pragma unroll
        for (int a = 0; a < 4; a++) {
          const u32 xp = xf[kk][m][a], yp = ya[kk][m][a >> 1][a & 1];
          xv[a] = f32x2{bf_lo(xp), bf_hi(xp)}; yv[a] = f32x2{bf_lo(yp), bf_hi(yp)};
        }
        SCHED_FENCE();
#pragma unroll
        for (int a = 0; a < 4; a++) qv[a] = __builtin_elementwise_fma(xv[a], f32x2{r1[m], r1[m]}, f32x2{c1[m], c1[m]});
        SCHED_FENCE();
#pragma unroll
        for (int a = 0; a < 4; a++) qv[a] = __builtin_elementwise_fma(qv[a], yv[a], xv[a]);
        SCHED_FENCE();
        u32x4 hf;
#pragma unroll
        for (int a = 0; a < 4; a++) hf[a] = pack_bf(qv[a][0], qv[a][1]);
        *(u32x4*)(sm + L_H + ((m * 16 + 2 * w + kk) * 64 + lane) * 16) = hf;
      }
  }
  if (w >= 4) {
    const __amdgpu_buffer_rsrc_t rc = mk_rsrc(ws + WS_CSP, 16 * 11 * 512);
    const int cjc = cj < 176 ? cj : 175;
    const u32 co = (u32)((cjc >> 4) * 512 + c * 64 + (cjc & 15) * 4);
#pragma unroll
    for (int ks = 0; ks < 16; ks++) csp[ks] = __uint_as_float(__builtin_amdgcn_raw_buffer_load_b32(rc, co, ks * 11 * 512, 16));
  }
  asm volatile("s_waitcnt lgkmcnt(0)" ::: "memory");
  __builtin_amdgcn_s_barrier();
  auto save_residual = [&]() {
    *(u32x4*)(sm + L_RES + (w * 64 + lane) * 16) = *(const u32x4*)(sm + L_H + (((w >> 1) * 16 + 2 * c + (w & 1)) * 64 + lane) * 16);
  };
  if (w >= 4) {
    __builtin_amdgcn_s_setprio(3);
    u32x4 fa[16];
#pragma unroll
    for (int ks = 0; ks < 16; ks++) fa[ks] = *(const u32x4*)(sm + L_H + (((w - 4) * 16 + ks) * 64 + lane) * 16);
    f32x4 q0 = {0.f, 0.f, 0.f, 0.f}, q1 = {0.f, 0.f, 0.f, 0.f};
    const u32x4 ones = {ONES, ONES, ONES, ONES};
#pragma unroll
    for (int ks = 0; ks < 16; ks++) {
      q0 = mfma(fa[ks], fa[ks], q0);
      q1 = mfma(ones, fa[ks], q1);
    }
    {
      const float lo = (n & 1) ? q0[1] : q0[0], hi = (n & 1) ? q0[3] : q0[2];
      const float q = (n & 2) ? hi : lo;
      const float mu = q1[0] * (1.f / D);
      const float var = fmaxf(q * (1.f / D) - mu * mu, 0.f);
      const float rstd = __builtin_amdgcn_rcpf(rb(__builtin_amdgcn_sqrtf(rb(rb(var) + 1e-5f))));
      if (g == (n >> 2)) *(f32x2*)(sm + L_ST + ((w - 4) * 16 + n) * 8) = f32x2{-1.44269504089f * rstd, rb(mu) * rstd * 1.44269504089f};
    }
    {
      float s4 = 0.f;
#pragma unroll
      for (int ks = 0; ks < 16; ks++) s4 += csp[ks];
      if (cj < 192) *(float*)(sm + L_CS + cj * 4) = s4;
    }
    save_residual();
    lds_arrive(lf + W_LN2, lane);
    __builtin_amdgcn_s_setprio(0);
    lds_wait(lf + W_W1S, 1u);
  } else {
    auto issued = [&](int ks) constexpr { const int v = PRE + NT * (ks + 1); return v < NW1 ? v : NW1; };
    f32x4 acc[NT][4];
    u32x4 hb[3][4];
#pragma unroll
    for (int i = 0; i < NT; i++)
#pragma unroll
      for (int m = 0; m < 4; m++) acc[i][m] = f32x4{0.f, 0.f, 0.f, 0.f};
    const char* hbase = sm + L_H + lane * 16;
#pragma unroll
    for (int d = 0; d < 2; d++)
#pragma unroll
      for (int m = 0; m < 4; m++) hb[d][m] = *(const u32x4*)(hbase + (m * 16 + d) * 1024);
#pragma unroll
    for (int ks = 0; ks < 16; ks++) {
#pragma unroll
      for (int m = 0; m < 4; m++) {
        acc[0][m] = mfma(w1[ks * NT], hb[ks % 3][m], acc[0][m]);
        SCHED_FENCE();
        if (ks + 2 < 16) hb[(ks + 2) % 3][m] = *(const u32x4*)(hbase + (m * 16 + ks + 2) * 1024);
        SCHED_FENCE();
        acc[1][m] = mfma(w1[ks * NT + 1], hb[ks % 3][m], acc[1][m]);
        SCHED_FENCE();
        if (m < NT && issued(ks - 1) + m < issued(ks)) w1load(issued(ks - 1) + m);
        SCHED_FENCE();
        acc[2][m] = mfma(w1[ks * NT + 2], hb[ks % 3][m], acc[2][m]);
        SCHED_FENCE();
      }
      if (ks == 15 - W1_PRE && w == 0 && lane == 0) *(volatile LAS u32*)(lf + W_W1S) = 1u;
    }
    save_residual();
    lds_wait(lf + W_LN2, 4u);
    float rs[4], mr[4];
    f32x4 cs[NT];
#pragma unroll
    for (int m = 0; m < 4; m++) { const f32x2 v = *(const f32x2*)(sm + L_ST + (m * 16 + n) * 8); rs[m] = v[0]; mr[m] = v[1]; }
#pragma unroll
    for (int i = 0; i < NT; i++) cs[i] = *(const f32x4*)(sm + L_CS + ((3 * w + i) * 16 + 4 * g) * 4);
    const __amdgpu_buffer_rsrc_t hrs = mk_rsrc(ws + WS_HF + (size_t)rt * 44 * 4096, 44 * 4096);
    u32 o_t[NT];
#pragma unroll
    for (int i = 0; i < NT; i++) {
      const int tl = 3 * w + i;
      const int ks2 = tl < 10 ? 5 * c + (tl >> 1) : 40 + (c >> 1), par = tl < 10 ? (tl & 1) : (c & 1);
      o_t[i] = (tl < 11 && (g & 1) == par) ? (u32)(ks2 * 4096 + lane * 16) : 0x40000000u;
    }
#pragma unroll
    for (int m = 0; m < 4; m++) {
#pragma unroll
      for (int i = 0; i < NT; i++) {
        float h1[4];
#pragma unroll
        for (int r = 0; r < 4; r++) h1[r] = silu_val(acc[i][m][r], rs[m], mr[m] * cs[i][r]);
        const u32 p0 = pack_bf(h1[0], h1[1]), p1 = pack_bf(h1[2], h1[3]);
        const auto s0 = __builtin_amdgcn_permlane16_swap(p0, p0, false, false), s1 = __builtin_amdgcn_permlane16_swap(p1, p1, false, false);
        __builtin_amdgcn_raw_buffer_store_b128(u32x4{s0[0], s1[0], s0[1], s1[1]}, hrs, o_t[i] + m * 1024, 0, 16);
      }
      SCHED_FENCE();
    }
  }
  u32x4 w2[22];
#pragma unroll
  for (int i = 0; i < 8; i++) bload16_asm_sc1(w2[i], rw2, lane * 16, i * 1024);
  load_w2_tail_support(w2, rw2, lane * 16, w);
  asm volatile("s_cmp_lt_u32 %0, 4\n\ts_cbranch_scc0 1f\n\ts_waitcnt vmcnt(0)\n1:\n\ts_waitcnt lgkmcnt(0)" ::"s"(w) : "memory", "scc");
  __builtin_amdgcn_s_barrier();
  if (w == 7) {
    flag_store(ws + WS_F3, b, ep, lane);
    poll_flags<1>(ws + WS_F3, (b & 3) + (b & 56) + 4 * ((lane >> 2) & 1) + 64 * (lane & 3), 0, ep);
  }
  if (w < 4) __builtin_amdgcn_s_sleep(2);
  load_w2_tail_main(w2, rw2, lane * 16, w);
  __builtin_amdgcn_s_barrier();

  const int dh = w & 1, kq = w >> 1;
  const __amdgpu_buffer_rsrc_t rhf = mk_rsrc(ws + WS_HF + (size_t)rt * 44 * 4096 + (size_t)(11 * kq * 4 + 2 * dh) * 1024, 11 * 4096);
  const u32 ldst = (u32)(size_t)sm + (kq * 4 + 2 * dh) * 1024;
  auto stage1 = [&](int s, int e) {
    const int slot = s < 9 ? s : s - 9;
    glds16<true>(rhf, lane * 16, s * 4096 + e * 1024, ldst + slot * 16384 + e * 1024);
  };
  auto stage = [&](int s) { stage1(s, 0); stage1(s, 1); };
  stage(0); stage(1); stage(2); stage(3);
  f32x4 acc2[2][4];
#pragma unroll
  for (int i = 0; i < 2; i++)
#pragma unroll
    for (int m = 0; m < 4; m++) acc2[i][m] = f32x4{0.f, 0.f, 0.f, 0.f};
  asm volatile("s_waitcnt vmcnt(4)"
               : "+v"(w2[0]), "+v"(w2[1]), "+v"(w2[2]), "+v"(w2[3]), "+v"(w2[4]), "+v"(w2[5]), "+v"(w2[6]), "+v"(w2[7]), "+v"(w2[8]), "+v"(w2[9]), "+v"(w2[10]),
                 "+v"(w2[11]), "+v"(w2[12]), "+v"(w2[13]), "+v"(w2[14]), "+v"(w2[15]), "+v"(w2[16]), "+v"(w2[17]), "+v"(w2[18]), "+v"(w2[19]), "+v"(w2[20]), "+v"(w2[21])
               ::"memory");
  __builtin_amdgcn_s_barrier();
  {
    const char* base = sm + kq * 4096 + lane * 16;
    u32x4 hb[2][4];
#pragma unroll
    for (int m = 0; m < 4; m++) hb[0][m] = *(const u32x4*)(base + m * 1024);
#pragma unroll
    for (int s = 0; s < 11; s++) {
      if (s == 1) { asm volatile("s_waitcnt vmcnt(4)" ::: "memory"); __builtin_amdgcn_s_barrier(); }
      else if (s == 3 || s == 5) { asm volatile("s_waitcnt vmcnt(6)" ::: "memory"); __builtin_amdgcn_s_barrier(); }
      else if (s == 7) { asm volatile("s_waitcnt vmcnt(2)" ::: "memory"); __builtin_amdgcn_s_barrier(); }
      else if (s == 9) { asm volatile("s_waitcnt vmcnt(0)" ::: "memory"); __builtin_amdgcn_s_barrier(); }
      const int slot = s + 1 < 9 ? s + 1 : s + 1 - 9;
#pragma unroll
      for (int m = 0; m < 4; m++) {
        acc2[0][m] = mfma(w2[2 * s], hb[s & 1][m], acc2[0][m]);
        SCHED_FENCE();
        if (s < 10) hb[(s + 1) & 1][m] = *(const u32x4*)(base + slot * 16384 + m * 1024);
        SCHED_FENCE();
        acc2[1][m] = mfma(w2[2 * s + 1], hb[s & 1][m], acc2[1][m]);
        SCHED_FENCE();
        if (s < 2) stage1(4 + 2 * s + (m >> 1), m & 1);
        else if (s < 5 && (m & 1)) stage1(s + 6, m >> 1);
        SCHED_FENCE();
      }
    }
  }
  asm volatile("s_waitcnt lgkmcnt(0)" ::: "memory");
  __builtin_amdgcn_s_barrier();
  {
    char* red = sm + L_RED + dh * 32768;
    f32x4 o0, o1;
    auto scatter = [&](auto KQ) {
      constexpr int k = decltype(KQ)::value;
#pragma unroll
      for (int m = 0; m < 4; m++) {
        if (m != k) {
#pragma unroll
          for (int i = 0; i < 2; i++) *(f32x4*)(red + (((k * 4 + m) * 2 + i) * 64 + lane) * 16) = acc2[i][m];
        }
      }
      o0 = acc2[0][k]; o1 = acc2[1][k];
      asm volatile("" : "+v"(o0), "+v"(o1));
    };
    if (kq == 0) scatter(IntC<0>{}); else if (kq == 1) scatter(IntC<1>{}); else if (kq == 2) scatter(IntC<2>{}); else scatter(IntC<3>{});
    __syncthreads();
#pragma unroll
    for (int j = 1; j < 4; j++) {
      const int src = (kq + j) & 3;
      o0 += *(const f32x4*)(red + (((src * 4 + kq) * 2 + 0) * 64 + lane) * 16);
      o1 += *(const f32x4*)(red + (((src * 4 + kq) * 2 + 1) * 64 + lane) * 16);
    }
    const u32x4 hres = *(const u32x4*)(sm + L_RES + (w * 64 + lane) * 16);
    u32x4 res;
#pragma unroll
    for (int r = 0; r < 4; r++) res[r] = pack_bf(bf_lo(hres[r]) + rb(o0[r]), bf_hi(hres[r]) + rb(o1[r]));
    *(u32x4*)(OUT + (size_t)(row0 + 16 * kq + n) * D + 64 * c + 32 * dh + 8 * g) = res;
  }
  asm volatile("ffn_text_end:");
}

extern "C" void kernel_launch(void* const* d_in, const int* in_sizes, int n_in, void* d_out, int out_size, void* d_ws,
                              size_t ws_size, hipStream_t stream) {
  fused_block<<<256, 512, 0, stream>>>((const u16*)d_in[0], (const u16*)d_in[1], (const u16*)d_in[2], (const u16*)d_in[3], (const u16*)d_in[4],
                                       (const u16*)d_in[5], (const u16*)d_in[7], (const u16*)d_in[8], (char*)d_ws, (u16*)d_out);
}
```

```cpp
#include <hip/hip_runtime.h>
#include <stdint.h>
#include <stdio.h>

struct Stamps { unsigned long long t[32]; };
#define STAMP_DECL Stamps stp; _Pragma("unroll") for (int _i = 0; _i < 32; _i++) stp.t[_i] = 0
#define STAMP(ws, kidx, i) do { __builtin_amdgcn_sched_barrier(0); stp.t[(kidx) * 16 + (i)] = __builtin_amdgcn_s_memrealtime(); __builtin_amdgcn_s_waitcnt(0xc07f); __builtin_amdgcn_sched_barrier(0); } while (0)
#define DT(i) (stp.t[i] ? (int)(stp.t[i] - stp.t[0]) : -1)
#define STAMP_FLUSH(ws, kidx) do { if ((kidx) == 1 && (threadIdx.x == 0 || threadIdx.x == 256) && (blockIdx.x % 37 == 0)) { \
  printf("b%d t%d abs %d | A: %d %d %d %d %d %d %d %d | B0 %d y %d h %d g1 %d s2 %d ep %d dr %d s6 %d fl %d H0 %d g2 %d end %d\n", (int)blockIdx.x, (int)threadIdx.x, (int)(stp.t[0] % 1000000ull), \
   DT(1), DT(2), DT(3), DT(4), DT(5), DT(6), DT(7), DT(8), DT(16), DT(17), DT(29), DT(28), DT(18), DT(20), DT(21), DT(22), DT(23), DT(24), DT(25), DT(26)); } } while (0)

typedef unsigned short u16;
typedef unsigned int u32;
typedef unsigned long long u64;
typedef __bf16 bf16x8 __attribute__((ext_vector_type(8)));
typedef __bf16 bf16x2 __attribute__((ext_vector_type(2)));
typedef short s16x4 __attribute__((ext_vector_type(4)));
typedef float f32x4 __attribute__((ext_vector_type(4)));
typedef float f32x2 __attribute__((ext_vector_type(2)));
typedef u32 u32x4 __attribute__((ext_vector_type(4)));
typedef u32 u32x2 __attribute__((ext_vector_type(2)));
#define GAS __attribute__((address_space(1)))
#define LAS __attribute__((address_space(3)))
#define RLX_AGENT __ATOMIC_RELAXED, __HIP_MEMORY_SCOPE_AGENT
#define SCHED_FENCE() __builtin_amdgcn_sched_barrier(0)

constexpr int S = 2048, D = 512, F = 1376;

constexpr size_t WS_EP   = 0;
constexpr size_t WS_F2   = 4096;
constexpr size_t WS_F3   = WS_F2 + 32768;
constexpr size_t WS_FW   = WS_F3 + 32768;
constexpr size_t WS_KVT  = 1 << 17;
constexpr size_t WS_YT   = WS_KVT + (size_t)D * S * 4;
constexpr size_t WS_W1T  = WS_YT + (size_t)D * S * 2;
constexpr size_t WS_W2T  = WS_W1T + 8 * 11 * 16 * 1024;
constexpr size_t WS_CSP  = WS_W2T + 8 * 4 * 44 * 1024;
constexpr size_t WS_HF   = WS_CSP + 16 * 8 * 11 * 16 * 4;
constexpr size_t WS_END  = WS_HF + (size_t)32 * 44 * 4 * 1024;

__device__ __forceinline__ float bf_lo(u32 v) { return __uint_as_float(v << 16); }
__device__ __forceinline__ float bf_hi(u32 v) { return __uint_as_float(v & 0xffff0000u); }
__device__ __forceinline__ u32 pack_bf(float a, float b) {
  f32x2 z = {a, b};
  return __builtin_bit_cast(u32, __builtin_convertvector(z, bf16x2));
}
__device__ __forceinline__ float rb(float a) { return bf_lo(pack_bf(a, 0.f)); }
__device__ __forceinline__ float dot2(u32 a, u32 b, float c) {
  return __builtin_amdgcn_fdot2_f32_bf16(__builtin_bit_cast(bf16x2, a), __builtin_bit_cast(bf16x2, b), c, false);
}
__device__ __forceinline__ u32 perm_lo(u32 a, u32 b) { return __builtin_amdgcn_perm(b, a, 0x05040100u); }
__device__ __forceinline__ u32 perm_hi(u32 a, u32 b) { return __builtin_amdgcn_perm(b, a, 0x07060302u); }
__device__ __forceinline__ f32x4 mfma(u32x4 a, u32x4 b, f32x4 c) {
  return __builtin_amdgcn_mfma_f32_16x16x32_bf16(__builtin_bit_cast(bf16x8, a), __builtin_bit_cast(bf16x8, b), c, 0, 0, 0);
}
constexpr u32 ONES = 0x3F803F80u;
template <int V> struct IntC { static constexpr int value = V; };
__device__ __forceinline__ float sum_g(float x) {
  auto s = __builtin_amdgcn_permlane16_swap(__float_as_uint(x), __float_as_uint(x), false, false);
  x = __uint_as_float(s[0]) + __uint_as_float(s[1]);
  auto t = __builtin_amdgcn_permlane32_swap(__float_as_uint(x), __float_as_uint(x), false, false);
  return __uint_as_float(t[0]) + __uint_as_float(t[1]);
}
template <int CTRL>
__device__ __forceinline__ float dpp_add(float x) {
  return x + __uint_as_float(__builtin_amdgcn_update_dpp(0u, __float_as_uint(x), CTRL, 0xf, 0xf, true));
}
__device__ __forceinline__ float wave_sum(float x) {
  x = dpp_add<0xB1>(x);
  x = dpp_add<0x4E>(x);
  x = dpp_add<0x141>(x);
  x = dpp_add<0x140>(x);
  return sum_g(x);
}
__device__ __forceinline__ __amdgpu_buffer_rsrc_t mk_rsrc(const void* p, u32 bytes) {
  return __builtin_amdgcn_make_buffer_rsrc((void*)p, 0, bytes, 0x00020000);
}
__device__ __forceinline__ u32x4 bload16(__amdgpu_buffer_rsrc_t r, u32 off) { return __builtin_amdgcn_raw_buffer_load_b128(r, off, 0, 0); }
__device__ __forceinline__ u32x2 bload8(__amdgpu_buffer_rsrc_t r, u32 off) { return __builtin_amdgcn_raw_buffer_load_b64(r, off, 0, 0); }
template <bool SC1>
__device__ __forceinline__ void glds16(__amdgpu_buffer_rsrc_t r, u32 voff, u32 soff, u32 lds_dst) {
  if (SC1)
    asm volatile("s_mov_b32 m0, %0\n\ts_nop 0\n\tbuffer_load_dwordx4 %1, %2, %3 offen sc1 lds" ::"s"(lds_dst), "v"(voff), "s"(r), "s"(soff) : "memory");
  else
    asm volatile("s_mov_b32 m0, %0\n\ts_nop 0\n\tbuffer_load_dwordx4 %1, %2, %3 offen lds" ::"s"(lds_dst), "v"(voff), "s"(r), "s"(soff) : "memory");
}
__device__ __forceinline__ void bload16_asm(u32x4& d, __amdgpu_buffer_rsrc_t r, u32 voff, u32 soff) {
  asm volatile("buffer_load_dwordx4 %0, %1, %2, %3 offen" : "=v"(d) : "v"(voff), "s"(r), "s"(soff) : "memory");
}
__device__ __forceinline__ void bload16_asm_sc1(u32x4& d, __amdgpu_buffer_rsrc_t r, u32 voff, u32 soff) {
  asm volatile("buffer_load_dwordx4 %0, %1, %2, %3 offen sc1" : "=v"(d) : "v"(voff), "s"(r), "s"(soff) : "memory");
}
template <int AUX>
__device__ __forceinline__ void gstore16(char* base, size_t off, u32x4 v) {
  const __amdgpu_buffer_rsrc_t r = mk_rsrc(base, 0x7fffffffu);
  __builtin_amdgcn_raw_buffer_store_b128(v, r, (u32)off, 0, AUX);
}
template <int AUX>
__device__ __forceinline__ void gstore8(char* base, size_t off, u32x2 v) {
  const __amdgpu_buffer_rsrc_t r = mk_rsrc(base, 0x7fffffffu);
  __builtin_amdgcn_raw_buffer_store_b64(v, r, (u32)off, 0, AUX);
}
__device__ __forceinline__ u32x4 bload16s(__amdgpu_buffer_rsrc_t r, u32 voff, u32 soff) { return __builtin_amdgcn_raw_buffer_load_b128(r, voff, soff, 16); }
__device__ __forceinline__ u32x2 tr_read(const char* p) {
  s16x4 r = __builtin_amdgcn_ds_read_tr16_b64_v4i16((LAS s16x4*)p);
  return __builtin_bit_cast(u32x2, r);
}

__device__ __forceinline__ void code_prefetch(int part, int nparts, int lane, u32 lds_dump) {
  u64 pc; u32 len;
  asm volatile("s_getpc_b64 %0\n\ts_mov_b32 %1, ffn_text_end-." : "=s"(pc), "=s"(len));
  const __amdgpu_buffer_rsrc_t r = mk_rsrc((const void*)pc, len);
  if ((u32)part * 1024 < len) glds16<false>(r, lane * 16, part * 1024, lds_dump);
}
__device__ __forceinline__ int fcol(int ks, int g, int j) {
  const int slot = 8 * (g >> 1) + j;
  if (ks < 40) { const int ct = ks / 5, p = ks - 5 * ct; return 128 * (2 * p + (g & 1)) + 8 * slot + ct; }
  return 1280 + 8 * slot + 2 * (ks - 40) + (g & 1);
}

constexpr int HC_STRIDE = 4608;
constexpr int HC_CH = 8 * HC_STRIDE;
constexpr int U_CH = (240 + 2048) * 2;
constexpr int P_H = 0, P_U = 2 * HC_CH, P_R = P_U + 2 * U_CH + 32;
constexpr int P_T = P_R + 4 * 4096;
constexpr int P_END = P_T + 8192;
constexpr int L_H = 0;
constexpr int L_Y = 65536;
constexpr int L_X1 = 131072;
constexpr int L_ST = 147456;
constexpr int L_CS = L_ST + 512;
constexpr int L_RES = L_CS + 768;
constexpr int L_RED = 0;
constexpr int L_FLG = L_RES + 8192;
constexpr int L_TOT = L_FLG + 128;
enum { W_CA = 12, W_CB = 16, W_HRDY = 24, W_CD = 28, W_PART = 32, W_WDONE = 56, W_CE = 60, W_FWOK = 20, W_LN2 = 64, W_W1S = 68 };
static_assert(P_END <= L_X1 && L_TOT <= 163840, "LDS map");
#define KV_SLEEP 8
#define KV_FIRST 32
#define W1_PRE 6
__device__ __forceinline__ constexpr int hoff(int c) {
  return c == 0 ? 4 : c == 1 ? 2 : c == 2 ? 8 : c == 3 ? 3 : c == 4 ? 7 : c == 5 ? 0 : c == 6 ? 15 : 12;
}
__device__ __forceinline__ int hoff_rt(int c) { return (0xCF073824u >> (4 * c)) & 15; }
__device__ __forceinline__ void lds_wait(const char* p, u32 want) {
  for (u32 spins = 0; spins < (1u << 22); spins++) {
    if (__builtin_amdgcn_readfirstlane(*(volatile LAS u32*)p) == want) break;
    __builtin_amdgcn_s_sleep(1);
  }
  asm volatile("" ::: "memory");
}
__device__ __forceinline__ void lds_arrive(char* p, int lane) {
  asm volatile("s_waitcnt lgkmcnt(0)" ::: "memory");
  if (lane == 0) __hip_atomic_fetch_add((LAS u32*)p, 1u, __ATOMIC_RELAXED, __HIP_MEMORY_SCOPE_WORKGROUP);
}
__device__ __forceinline__ void lds_set(char* p, u32 v, int lane) {
  asm volatile("s_waitcnt lgkmcnt(0)" ::: "memory");
  if (lane == 0) *(volatile LAS u32*)p = v;
}
__device__ __forceinline__ void flag_store(char* base, int idx, u64 ep, int lane) {
  if (lane < 16) __hip_atomic_store((GAS u64*)(base + (size_t)idx * 128 + lane * 8), ep, RLX_AGENT);
}
template <int N>
__device__ __forceinline__ void poll_flags(const char* base, int first, int stride, u64 ep) {
  for (u32 spins = 0; spins < (1u << 20); spins++) {
    bool ok = true;
#pragma unroll
    for (int i = 0; i < N; i++) ok &= __hip_atomic_load((GAS u64*)(base + (size_t)(first + i * stride) * 128), RLX_AGENT) == ep;
    if (__all(ok)) break;
    __builtin_amdgcn_s_sleep(1);
  }
  asm volatile("" ::: "memory");
}

#define W2_LOADS                                                                                                                   \
  "buffer_load_dwordx4 %0, %14, %15, %16 offen sc1\n\tbuffer_load_dwordx4 %1, %14, %15, %16 offen offset:1024 sc1\n\t"                      \
  "buffer_load_dwordx4 %2, %14, %15, %16 offen offset:2048 sc1\n\tbuffer_load_dwordx4 %3, %14, %15, %16 offen offset:3072 sc1\n\t"          \
  "buffer_load_dwordx4 %4, %14, %15, %17 offen sc1\n\tbuffer_load_dwordx4 %5, %14, %15, %17 offen offset:1024 sc1\n\t"                      \
  "buffer_load_dwordx4 %6, %14, %15, %17 offen offset:2048 sc1\n\tbuffer_load_dwordx4 %7, %14, %15, %17 offen offset:3072 sc1\n\t"          \
  "buffer_load_dwordx4 %8, %14, %15, %18 offen sc1\n\tbuffer_load_dwordx4 %9, %14, %15, %18 offen offset:1024 sc1\n\t"                      \
  "buffer_load_dwordx4 %10, %14, %15, %18 offen offset:2048 sc1\n\tbuffer_load_dwordx4 %11, %14, %15, %18 offen offset:3072 sc1\n\t"        \
  "buffer_load_dwordx4 %12, %14, %15, %19 offen sc1\n\tbuffer_load_dwordx4 %13, %14, %15, %19 offen offset:1024 sc1\n"
__device__ __forceinline__ void load_w2_tail_support(u32x4 (&w2)[22], __amdgpu_buffer_rsrc_t r, u32 voff, int w) {
  asm volatile("s_cmp_lt_u32 %20, 4\n\ts_cbranch_scc1 1f\n\t" W2_LOADS "1:"
               : "=&v"(w2[8]), "=&v"(w2[9]), "=&v"(w2[10]), "=&v"(w2[11]), "=&v"(w2[12]), "=&v"(w2[13]), "=&v"(w2[14]), "=&v"(w2[15]), "=&v"(w2[16]),
                 "=&v"(w2[17]), "=&v"(w2[18]), "=&v"(w2[19]), "=&v"(w2[20]), "=&v"(w2[21])
               : "v"(voff), "s"(r), "s"(8192), "s"(12288), "s"(16384), "s"(20480), "s"(w) : "memory", "scc");
}
__device__ __forceinline__ void load_w2_tail_main(u32x4 (&w2)[22], __amdgpu_buffer_rsrc_t r, u32 voff, int w) {
  asm volatile("s_cmp_lt_u32 %20, 4\n\ts_cbranch_scc0 1f\n\t" W2_LOADS "1:"
               : "+v"(w2[8]), "+v"(w2[9]), "+v"(w2[10]), "+v"(w2[11]), "+v"(w2[12]), "+v"(w2[13]), "+v"(w2[14]), "+v"(w2[15]), "+v"(w2[16]),
                 "+v"(w2[17]), "+v"(w2[18]), "+v"(w2[19]), "+v"(w2[20]), "+v"(w2[21])
               : "v"(voff), "s"(r), "s"(8192), "s"(12288), "s"(16384), "s"(20480), "s"(w) : "memory", "scc");
}
__device__ __forceinline__ float silu_val(float acc, float rs, float bias) {
  const float tt = fmaf(acc, rs, bias);
  const float e = __builtin_amdgcn_exp2f(tt);
  return tt * __builtin_amdgcn_rcpf(fmaf(e, -1.44269504089f, -1.44269504089f));
}

__global__ __launch_bounds__(512, 2) void fused_block(const u16* __restrict__ X, const u16* __restrict__ K, const u16* __restrict__ V,
                                                      const u16* __restrict__ SK, const u16* __restrict__ HK, const u16* __restrict__ DV,
                                                      const u16* __restrict__ W1, const u16* __restrict__ W2, char* __restrict__ ws,
                                                      u16* __restrict__ OUT) {
  __shared__ __attribute__((aligned(16))) char sm[L_TOT];
  const int b = blockIdx.x, t = threadIdx.x, lane = t & 63;
  const int w = __builtin_amdgcn_readfirstlane(t >> 6);
  const int n = lane & 15, g = lane >> 4;
  char* lf = sm + L_FLG;
  STAMP_DECL;
  STAMP(ws, 0, 0);
#pragma unroll
  for (int i = 0; i < 4; i++) code_prefetch(w + 8 * i, 32, lane, (u32)(size_t)(sm + L_ST));
  if (t < 32) *(volatile LAS u32*)(lf + t * 4) = 0u;
  asm volatile("s_waitcnt lgkmcnt(0)" ::: "memory");
  __builtin_amdgcn_s_barrier();
  const int chp = 64 * (b & 7) + 2 * (b >> 3);
  const int rt = 8 * (b & 3) + ((b >> 3) & 7), c = 4 * ((b >> 2) & 1) + (b >> 6);
  const int row0 = 64 * rt;
  const u64 ep = *(const u64*)(ws + WS_EP) + 1;
  const u32 dvp = *(const u32*)(DV + chp);

  u32x4 wl[8];
  const int unit = w >= 4 ? b + 256 * (w - 4) : 1 << 20;
  if (w < 4) {
    const int dt = b & 7, st = b >> 3;
    const int rp = t >> 3, c8 = t & 7;
    const int s = st * 64 + 2 * rp, ch0 = dt * 64 + c8 * 8;
    const __amdgpu_buffer_rsrc_t rk = mk_rsrc(K, S * D * 2);
    const u32 off = (u32)(s * D + ch0) * 2;
    u32x4 kk[5];
#pragma unroll
    for (int i = 0; i < 5; i++) kk[i] = bload16(rk, off + (1 - i) * 1024);
    const u32x4 v0 = *(const u32x4*)((const char*)V + off), v1 = *(const u32x4*)((const char*)V + off + 1024);
    u32x4 wa[4];
#pragma unroll
    for (int i = 0; i < 4; i++) wa[i] = *(const u32x4*)(SK + ch0 * 4 + i * 8);
    SCHED_FENCE();
    STAMP(ws, 0, 1);
    char* pk = sm + P_T + (8 * c8) * 128 + (((rp >> 2) ^ c8) << 4) + ((3 - (rp & 3)) << 2);
#pragma unroll
    for (int i = 0; i < 4; i++) {
      const u32 l01 = perm_lo(kk[0][i], kk[1][i]), l12 = perm_lo(kk[1][i], kk[2][i]), l23 = perm_lo(kk[2][i], kk[3][i]), l34 = perm_lo(kk[3][i], kk[4][i]);
      const u32 h01 = perm_hi(kk[0][i], kk[1][i]), h12 = perm_hi(kk[1][i], kk[2][i]), h23 = perm_hi(kk[2][i], kk[3][i]), h34 = perm_hi(kk[3][i], kk[4][i]);
      const float a1 = dot2(l01, wa[i][0], dot2(l23, wa[i][1], 0.f)) * bf_lo(v1[i]);
      const float a0 = dot2(l12, wa[i][0], dot2(l34, wa[i][1], 0.f)) * bf_lo(v0[i]);
      const float b1 = dot2(h01, wa[i][2], dot2(h23, wa[i][3], 0.f)) * bf_hi(v1[i]);
      const float b0 = dot2(h12, wa[i][2], dot2(h34, wa[i][3], 0.f)) * bf_hi(v0[i]);
      *(u32*)(pk + (2 * i) * 128) = pack_bf(a1, a0);
      *(u32*)(pk + (2 * i + 1) * 128) = pack_bf(b1, b0);
    }
    lds_arrive(lf + W_CA, lane);
    lds_wait(lf + W_CA, 4u);
    STAMP(ws, 0, 2);
    const u32 tag = (u32)ep;
#pragma unroll
    for (int i = 0; i < 2; i++) {
      const int u = t + 256 * i, ch = u >> 3, cc = u & 7;
#pragma unroll
      for (int hh = 0; hh < 2; hh++) {
        const int grp = 4 * hh + (cc >> 1);
        const u32x2 d = *(const u32x2*)(sm + P_T + ch * 128 + ((grp ^ ((ch >> 3) & 7)) << 4) + (cc & 1) * 8);
        gstore16<16>(ws, WS_KVT + (size_t)(dt * 64 + ch) * 8192 + (st * 8 + 4 * hh) * 32 + cc * 16, u32x4{d[0], tag, d[1], tag});
      }
    }
    lds_arrive(lf + W_CB, lane);
    STAMP(ws, 0, 3);
    {
      const __amdgpu_buffer_rsrc_t ru = mk_rsrc(ws + WS_KVT + (size_t)chp * 8192, 2 * 8192);
      if (t < 60) *(u32x4*)(sm + P_U + (t < 30 ? 0 : U_CH - 480) + 16 * t) = u32x4{0, 0, 0, 0};
      u32x4 ga[2][2];
      __builtin_amdgcn_s_sleep(KV_FIRST);
      for (u32 spins = 0; spins < (1u << 18); spins++) {
#pragma unroll
        for (int cl = 0; cl < 2; cl++) { ga[cl][0] = bload16s(ru, t * 32, cl * 8192); ga[cl][1] = bload16s(ru, t * 32 + 16, cl * 8192); }
        bool ok = true;
#pragma unroll
        for (int cl = 0; cl < 2; cl++) ok &= ga[cl][0][1] == tag && ga[cl][0][3] == tag && ga[cl][1][1] == tag && ga[cl][1][3] == tag;
        if (__all(ok)) break;
        __builtin_amdgcn_s_sleep(KV_SLEEP);
      }
      STAMP(ws, 0, 4);
#pragma unroll
      for (int cl = 0; cl < 2; cl++) *(u32x4*)(sm + P_U + cl * U_CH + 480 + 16 * t) = u32x4{ga[cl][0][0], ga[cl][0][2], ga[cl][1][0], ga[cl][1][2]};
    }
    lds_arrive(lf + W_CD, lane);
    lds_wait(lf + W_HRDY, 4u);
    lds_wait(lf + W_CD, 4u);
  } else {
    const int s = t - 256;
    u32x4 ha[2], hb[2];
    const __amdgpu_buffer_rsrc_t rh_ = mk_rsrc(HK + (size_t)chp * S, 2 * S * 2);
#pragma unroll
    for (int cl = 0; cl < 2; cl++) { ha[cl] = bload16(rh_, (u32)(cl * S + 8 * s) * 2 - 16); hb[cl] = bload16(rh_, (u32)(cl * S + 8 * s) * 2); }
    if (w == 7) {
      const char* p;
      if (lane < 24) p = ws + (size_t)lane * (1 << 20);
      else if (lane < 27) p = (const char*)OUT + (size_t)(lane - 24) * ((1 << 20) - 64);
      else if (lane < 30) p = (const char*)X + (size_t)(lane - 27) * ((1 << 20) - 64);
      else if (lane < 32) p = (const char*)W1 + (size_t)(lane - 30) * (D * F * 2 - 64);
      else if (lane < 34) p = (const char*)W2 + (size_t)(lane - 32) * (D * F * 2 - 64);
      else p = ws + WS_F2 + (size_t)(lane - 34) * 4096;
      asm volatile("s_mov_b32 m0, %0\n\ts_nop 0\n\tglobal_load_lds_dword %1, off" ::"s"((u32)(size_t)(sm + L_ST + 1024)), "v"(p) : "memory");
    }

    SCHED_FENCE();
#pragma unroll
    for (int cl = 0; cl < 2; cl++) {
      if (s == 0) ha[cl] = u32x4{0, 0, 0, 0};
      char* hbase = sm + P_H + cl * HC_CH;
      const u32 W[8] = {ha[cl][0], ha[cl][1], ha[cl][2], ha[cl][3], hb[cl][0], hb[cl][1], hb[cl][2], hb[cl][3]};
#pragma unroll
      for (int cc = 0; cc < 8; cc++) {
        u32x4 o;
#pragma unroll
        for (int a = 0; a < 4; a++) o[a] = (cc & 1) ? __builtin_amdgcn_alignbit(W[(cc >> 1) + a + 1], W[(cc >> 1) + a], 16) : W[(cc >> 1) + a];
        *(u32x4*)(hbase + cc * HC_STRIDE + 16 * (s + 1 + hoff(cc))) = o;
      }
      if (s < 8) *(u32x4*)(hbase + s * HC_STRIDE + 16 * hoff_rt(s)) = u32x4{0, 0, 0, 0};
      if (s == 255) *(u32x4*)(hbase + 16 * (257 + hoff(0))) = hb[cl];
    }
    lds_arrive(lf + W_HRDY, lane);
    STAMP(ws, 0, 1);
    lds_wait(lf + W_CB, 4u);
    {
      const int lc = lane & 15, lg = lane >> 4;
      if (unit < 176) {
        const __amdgpu_buffer_rsrc_t rw = mk_rsrc(W1, D * F * 2);
        const int blk = unit >> 4, ks = unit & 15;
        const int col = 128 * blk + 8 * lc;
        const u32 o0 = col < F ? (u32)((32 * ks + 8 * lg) * F + col) * 2 : 0x80000000u;
#pragma unroll
        for (int j = 0; j < 8; j++) wl[j] = bload16(rw, o0 + j * F * 2);
      } else if (unit < 528) {
        const __amdgpu_buffer_rsrc_t rw = mk_rsrc(W2, D * F * 2);
        const int u2 = unit - 176, ks = u2 >> 3, c2 = u2 & 7;
#pragma unroll
        for (int j = 0; j < 8; j++) {
          const u32 o = (u32)(fcol(ks, lg, j) * D + 64 * c2 + 8 * (lc >> 2) + 2 * (lc & 3)) * 2;
          wl[j][0] = __builtin_amdgcn_raw_buffer_load_b32(rw, o, 0, 0);
          wl[j][1] = __builtin_amdgcn_raw_buffer_load_b32(rw, o + 64, 0, 0);
        }
      }
    }
  }

  u32x4 xf[2][4];
  auto load_x = [&]() {
#pragma unroll
    for (int kk = 0; kk < 2; kk++)
#pragma unroll
      for (int m = 0; m < 4; m++) xf[kk][m] = *(const u32x4*)(X + (size_t)(row0 + 16 * m + n) * D + 32 * (2 * w + kk) + 8 * g);
  };
  auto ln1_partials = [&]() {
    const u32x4 ones = {ONES, ONES, ONES, ONES};
#pragma unroll
    for (int m = 0; m < 4; m++) {
      f32x4 q0 = {0.f, 0.f, 0.f, 0.f}, q1 = {0.f, 0.f, 0.f, 0.f};
#pragma unroll
      for (int kk = 0; kk < 2; kk++) { q0 = mfma(xf[kk][m], xf[kk][m], q0); q1 = mfma(ones, xf[kk][m], q1); }
      const float lo = (n & 1) ? q0[1] : q0[0], hi = (n & 1) ? q0[3] : q0[2];
      if (g == (n >> 2)) *(f32x2*)(sm + L_X1 + ((w * 4 + m) * 16 + n) * 8) = f32x2{q1[0], (n & 2) ? hi : lo};
    }
  };
  constexpr int NT = 3, NW1 = 48, PRE = W1_PRE * NT;
  u32x4 w1[NW1];
  const __amdgpu_buffer_rsrc_t rw1 = mk_rsrc(ws + WS_W1T + (size_t)(c * 11 + 3 * (w & 3)) * 16 * 1024, 48 * 1024);
  auto w1load = [&](int k) { w1[k] = bload16s(rw1, lane * 16, (k % NT) * 16384 + (k / NT) * 1024); };
  if (w < 4) {
    const int wcl = (w >> 1) & 1, rh = w & 1;
    const u32 dvr = dvp >> (16 * wcl);
    STAMP(ws, 0, 5);
    f32x4 acc[8];
#pragma unroll
    for (int i = 0; i < 8; i++) acc[i] = f32x4{0.f, 0.f, 0.f, 0.f};
    const int idx0 = 25 + n - 8 * g;
    const int cc = idx0 & 7;
    const char* fbase = sm + P_H + wcl * HC_CH + cc * HC_STRIDE + 16 * ((idx0 >> 3) + hoff_rt(cc)) + 64 * 4 * rh;
    const char* gbase = sm + P_U + wcl * U_CH + 2 * (16 * n + 8 * g + 224) - 64 * 4 * rh;
    u32x4 Ff[2][8], Gf[2][8];
#pragma unroll
    for (int a = 0; a < 8; a++) { Gf[0][a] = *(const u32x4*)(gbase + 64 * (8 * a)); Ff[0][a] = *(const u32x4*)(fbase + 64 * (8 * a)); }
    SCHED_FENCE();
#pragma unroll
    for (int rr = 0; rr < 4; rr++) {
      const int cur = rr & 1, nxt = cur ^ 1;
      int m = 0;
#pragma unroll
      for (int bb = 0; bb < 8; bb++) {
#pragma unroll
        for (int a = 0; a + bb < 8; a++) {
          acc[a + bb] = mfma(Ff[cur][a], Gf[cur][bb], acc[a + bb]);
          if (rr < 3 && (m & 1) == 1 && m < 32) {
            const int q = m >> 2;
            SCHED_FENCE();
            if (m & 2) Ff[nxt][q] = *(const u32x4*)(fbase + 64 * (8 * q + rr + 1));
            else Gf[nxt][q] = *(const u32x4*)(gbase + 64 * (8 * q - rr - 1));
            SCHED_FENCE();
          }
          m++;
        }
      }
      SCHED_FENCE();
    }
    STAMP(ws, 0, 6);
    f32x4* rbuf = (f32x4*)(sm + P_R) + w * 256;
    if (rh) {
#pragma unroll
      for (int i = 0; i < 4; i++) rbuf[i * 64 + lane] = acc[i];
#pragma unroll
      for (int i = 0; i < 4; i++) acc[i] = acc[4 + i];
    } else {
#pragma unroll
      for (int i = 0; i < 4; i++) rbuf[i * 64 + lane] = acc[4 + i];
    }
    lds_set(lf + W_PART + w * 4, 1u, lane);
    load_x();
    const int ch = chp + wcl;
    const char* ub = sm + P_U + wcl * U_CH;
    u32x2 uq[4];
#pragma unroll
    for (int i = 0; i < 4; i++) uq[i] = *(const u32x2*)(ub + 2 * (240 + 256 * (4 * rh + i) + 16 * n + 8 * (g >> 1) + ((g & 1) ? 0 : 4)));
    lds_wait(lf + W_PART + (w ^ 1) * 4, 1u);
    const float dv = bf_lo(dvr);
    const f32x4* pbuf = (const f32x4*)(sm + P_R) + (w ^ 1) * 256;
#pragma unroll
    for (int i = 0; i < 4; i++) {
      const int T = 4 * rh + i;
      const f32x4 a = acc[i] + pbuf[i * 64 + lane];
      const int pos = 256 * T + 16 * n + 4 * g;
      const float y0 = rb(a[0]) + rb(bf_hi(uq[i][1]) * dv), y1 = rb(a[1]) + rb(bf_lo(uq[i][1]) * dv);
      const float y2 = rb(a[2]) + rb(bf_hi(uq[i][0]) * dv), y3 = rb(a[3]) + rb(bf_lo(uq[i][0]) * dv);
      gstore8<16>(ws, WS_YT + ((size_t)ch * S + pos) * 2, u32x2{pack_bf(y0, y1), pack_bf(y2, y3)});
    }
    STAMP(ws, 0, 7);
    __builtin_amdgcn_s_waitcnt(0x0f70);
    asm volatile("s_waitcnt vmcnt(0)" ::: "memory");
    lds_arrive(lf + W_CE, lane);
    if (w == 0) {
      lds_wait(lf + W_CE, 4u);
      flag_store(ws + WS_F2, b, ep, lane);
      STAMP(ws, 0, 8);
    }
    ln1_partials();
    lds_wait(lf + W_FWOK, 1u);
#pragma unroll
    for (int i = 0; i < PRE; i++) w1load(i);
    SCHED_FENCE();
    if (w == 0) {
      poll_flags<4>(ws + WS_F2, lane, 64, ep);
      if (b == 0 && lane == 0) __hip_atomic_store((GAS u64*)(ws + WS_EP), ep, RLX_AGENT);
    }
  } else {
    if (unit < 176) {
      const int blk = unit >> 4, ks = unit & 15;
      float csel[2] = {0.f, 0.f};
#pragma unroll
      for (int i = 0; i < 8; i++) {
        u32x4 o;
#pragma unroll
        for (int a = 0; a < 4; a++) o[a] = (i & 1) ? perm_hi(wl[2 * a][i >> 1], wl[2 * a + 1][i >> 1]) : perm_lo(wl[2 * a][i >> 1], wl[2 * a + 1][i >> 1]);
        gstore16<16>(ws, WS_W1T + ((size_t)((i * 11 + blk) * 16 + ks) * 64 + lane) * 16, o);
        const float cs = sum_g(dot2(o[0], ONES, dot2(o[1], ONES, dot2(o[2], ONES, dot2(o[3], ONES, 0.f)))));
        csel[i >> 2] = g == (i & 3) ? cs : csel[i >> 2];
      }
#pragma unroll
      for (int hh = 0; hh < 2; hh++)
        __hip_atomic_store((GAS u32*)(ws + WS_CSP + (size_t)(ks * 11 + blk) * 512 + hh * 256 + lane * 4), __float_as_uint(csel[hh]), RLX_AGENT);
    } else if (unit < 528) {
      const int u2 = unit - 176, ks = u2 >> 3, c2 = u2 & 7;
#pragma unroll
      for (int i = 0; i < 4; i++) {
        u32x4 o;
#pragma unroll
        for (int a = 0; a < 4; a++) o[a] = (i & 1) ? perm_hi(wl[2 * a][i >> 1], wl[2 * a + 1][i >> 1]) : perm_lo(wl[2 * a][i >> 1], wl[2 * a + 1][i >> 1]);
        gstore16<16>(ws, WS_W2T + ((size_t)(((c2 * 2 + (i >> 1)) * 44 + ks) * 2 + (i & 1)) * 64 + lane) * 16, o);
      }
    }
    STAMP(ws, 0, 5);
    __builtin_amdgcn_s_waitcnt(0x0f70);
    asm volatile("s_waitcnt vmcnt(0)" ::: "memory");
    lds_arrive(lf + W_WDONE, lane);
    if (w == 4) {
      lds_wait(lf + W_WDONE, 4u);
      flag_store(ws + WS_FW, b, ep, lane);
    }
    load_x();
    ln1_partials();
    if (w == 4) {
      poll_flags<4>(ws + WS_FW, lane, 64, ep);
      lds_set(lf + W_FWOK, 1u, lane);
    }
    STAMP(ws, 0, 4);
  }
  asm volatile("s_waitcnt lgkmcnt(0)" ::: "memory");
  __builtin_amdgcn_s_barrier();
  STAMP_FLUSH(ws, 0);
  STAMP(ws, 1, 0);
  {
    const __amdgpu_buffer_rsrc_t rh = mk_rsrc(ws + WS_YT + ((size_t)(64 * w) * S + row0) * 2, 64 * S * 2);
    const int f0 = 2 * ((lane >> 4) & 1);
    const u32 v0 = (u32)(lane >> 3) * S * 2 + (((lane & 7) ^ f0) << 4), v1 = (u32)(lane >> 3) * S * 2 + (((lane & 7) ^ (f0 + 4)) << 4);
#pragma unroll
    for (int i = 0; i < 8; i++) glds16<true>(rh, (i & 1) ? v1 : v0, i * 8 * S * 2, (u32)(size_t)(sm + L_Y) + (64 * w + 8 * i) * 128);
  }
  const __amdgpu_buffer_rsrc_t rw2 = mk_rsrc(ws + WS_W2T + (size_t)((c * 2 + (w & 1)) * 44 + 11 * (w >> 1)) * 2048, 22 * 1024);
  float csp[16];
  const int cj = (w & 3) * 64 + lane;
  float r1[4], c1[4];
#pragma unroll
  for (int m = 0; m < 4; m++) {
    float a = 0.f, q = 0.f;
#pragma unroll
    for (int ww = 0; ww < 8; ww++) { const f32x2 v = *(const f32x2*)(sm + L_X1 + ((ww * 4 + m) * 16 + n) * 8); a += v[0]; q += v[1]; }
    const float mu = a * (1.f / D);
    const float var = fmaxf(q * (1.f / D) - mu * mu, 0.f);
    r1[m] = __builtin_amdgcn_rcpf(rb(__builtin_amdgcn_sqrtf(rb(rb(var) + 1e-5f))));
    c1[m] = -rb(mu) * r1[m];
  }
  asm volatile("s_waitcnt vmcnt(0)" ::: "memory");
  __builtin_amdgcn_s_barrier();
  STAMP(ws, 1, 1);
  const int q4 = (lane & 15) >> 2, p4 = lane & 3;
  {
    const int fsw = 2 * ((q4 >> 1) & 1) + 4 * (g & 1);
    u32x2 ya[2][4][2];
#pragma unroll
    for (int kk = 0; kk < 2; kk++)
#pragma unroll
      for (int m = 0; m < 4; m++) {
        const char* p = sm + L_Y + (8 * g + q4) * 128 + (((2 * m + (p4 >> 1)) ^ fsw) << 4) + 8 * (p4 & 1) + (2 * w + kk) * 4096;
        ya[kk][m][0] = tr_read(p); ya[kk][m][1] = tr_read(p + 512);
      }
    SCHED_FENCE();
#pragma unroll
    for (int kk = 0; kk < 2; kk++)
#pragma unroll
      for (int m = 0; m < 4; m++) {
        f32x2 xv[4], yv[4], qv[4];
#pragma unroll
        for (int a = 0; a < 4; a++) {
          const u32 xp = xf[kk][m][a], yp = ya[kk][m][a >> 1][a & 1];
          xv[a] = f32x2{bf_lo(xp), bf_hi(xp)}; yv[a] = f32x2{bf_lo(yp), bf_hi(yp)};
        }
        SCHED_FENCE();
#pragma unroll
        for (int a = 0; a < 4; a++) qv[a] = __builtin_elementwise_fma(xv[a], f32x2{r1[m], r1[m]}, f32x2{c1[m], c1[m]});
        SCHED_FENCE();
#pragma unroll
        for (int a = 0; a < 4; a++) qv[a] = __builtin_elementwise_fma(qv[a], yv[a], xv[a]);
        SCHED_FENCE();
        u32x4 hf;
#pragma unroll
        for (int a = 0; a < 4; a++) hf[a] = pack_bf(qv[a][0], qv[a][1]);
        *(u32x4*)(sm + L_H + ((m * 16 + 2 * w + kk) * 64 + lane) * 16) = hf;
      }
  }
  if (w >= 4) {
    const __amdgpu_buffer_rsrc_t rc = mk_rsrc(ws + WS_CSP, 16 * 11 * 512);
    const int cjc = cj < 176 ? cj : 175;
    const u32 co = (u32)((cjc >> 4) * 512 + c * 64 + (cjc & 15) * 4);
#pragma unroll
    for (int ks = 0; ks < 16; ks++) csp[ks] = __uint_as_float(__builtin_amdgcn_raw_buffer_load_b32(rc, co, ks * 11 * 512, 16));
  }
  asm volatile("s_waitcnt lgkmcnt(0)" ::: "memory");
  __builtin_amdgcn_s_barrier();
  STAMP(ws, 1, 13);
  auto save_residual = [&]() {
    *(u32x4*)(sm + L_RES + (w * 64 + lane) * 16) = *(const u32x4*)(sm + L_H + (((w >> 1) * 16 + 2 * c + (w & 1)) * 64 + lane) * 16);
  };
  if (w >= 4) {
    __builtin_amdgcn_s_setprio(3);
    u32x4 fa[16];
#pragma unroll
    for (int ks = 0; ks < 16; ks++) fa[ks] = *(const u32x4*)(sm + L_H + (((w - 4) * 16 + ks) * 64 + lane) * 16);
    f32x4 q0 = {0.f, 0.f, 0.f, 0.f}, q1 = {0.f, 0.f, 0.f, 0.f};
    const u32x4 ones = {ONES, ONES, ONES, ONES};
#pragma unroll
    for (int ks = 0; ks < 16; ks++) {
      q0 = mfma(fa[ks], fa[ks], q0);
      q1 = mfma(ones, fa[ks], q1);
    }
    {
      const float lo = (n & 1) ? q0[1] : q0[0], hi = (n & 1) ? q0[3] : q0[2];
      const float q = (n & 2) ? hi : lo;
      const float mu = q1[0] * (1.f / D);
      const float var = fmaxf(q * (1.f / D) - mu * mu, 0.f);
      const float rstd = __builtin_amdgcn_rcpf(rb(__builtin_amdgcn_sqrtf(rb(rb(var) + 1e-5f))));
      if (g == (n >> 2)) *(f32x2*)(sm + L_ST + ((w - 4) * 16 + n) * 8) = f32x2{-1.44269504089f * rstd, rb(mu) * rstd * 1.44269504089f};
    }
    {
      float s4 = 0.f;
#pragma unroll
      for (int ks = 0; ks < 16; ks++) s4 += csp[ks];
      if (cj < 192) *(float*)(sm + L_CS + cj * 4) = s4;
    }
    save_residual();
    lds_arrive(lf + W_LN2, lane);
    __builtin_amdgcn_s_setprio(0);
    STAMP(ws, 1, 12);
    lds_wait(lf + W_W1S, 1u);
  } else {
    auto issued = [&](int ks) constexpr { const int v = PRE + NT * (ks + 1); return v < NW1 ? v : NW1; };
    f32x4 acc[NT][4];
    u32x4 hb[3][4];
#pragma unroll
    for (int i = 0; i < NT; i++)
#pragma unroll
      for (int m = 0; m < 4; m++) acc[i][m] = f32x4{0.f, 0.f, 0.f, 0.f};
    const char* hbase = sm + L_H + lane * 16;
#pragma unroll
    for (int d = 0; d < 2; d++)
#pragma unroll
      for (int m = 0; m < 4; m++) hb[d][m] = *(const u32x4*)(hbase + (m * 16 + d) * 1024);
#pragma unroll
    for (int ks = 0; ks < 16; ks++) {
#pragma unroll
      for (int m = 0; m < 4; m++) {
        acc[0][m] = mfma(w1[ks * NT], hb[ks % 3][m], acc[0][m]);
        SCHED_FENCE();
        if (ks + 2 < 16) hb[(ks + 2) % 3][m] = *(const u32x4*)(hbase + (m * 16 + ks + 2) * 1024);
        SCHED_FENCE();
        acc[1][m] = mfma(w1[ks * NT + 1], hb[ks % 3][m], acc[1][m]);
        SCHED_FENCE();
        if (m < NT && issued(ks - 1) + m < issued(ks)) w1load(issued(ks - 1) + m);
        SCHED_FENCE();
        acc[2][m] = mfma(w1[ks * NT + 2], hb[ks % 3][m], acc[2][m]);
        SCHED_FENCE();
      }
      if (ks == 15 - W1_PRE && w == 0 && lane == 0) *(volatile LAS u32*)(lf + W_W1S) = 1u;
    }
    STAMP(ws, 1, 12);
    save_residual();
    lds_wait(lf + W_LN2, 4u);
    STAMP(ws, 1, 2);
    float rs[4], mr[4];
    f32x4 cs[NT];
#pragma unroll
    for (int m = 0; m < 4; m++) { const f32x2 v = *(const f32x2*)(sm + L_ST + (m * 16 + n) * 8); rs[m] = v[0]; mr[m] = v[1]; }
#pragma unroll
    for (int i = 0; i < NT; i++) cs[i] = *(const f32x4*)(sm + L_CS + ((3 * w + i) * 16 + 4 * g) * 4);
    const __amdgpu_buffer_rsrc_t hrs = mk_rsrc(ws + WS_HF + (size_t)rt * 44 * 4096, 44 * 4096);
    u32 o_t[NT];
#pragma unroll
    for (int i = 0; i < NT; i++) {
      const int tl = 3 * w + i;
      const int ks2 = tl < 10 ? 5 * c + (tl >> 1) : 40 + (c >> 1), par = tl < 10 ? (tl & 1) : (c & 1);
      o_t[i] = (tl < 11 && (g & 1) == par) ? (u32)(ks2 * 4096 + lane * 16) : 0x40000000u;
    }
#pragma unroll
    for (int m = 0; m < 4; m++) {
#pragma unroll
      for (int i = 0; i < NT; i++) {
        float h1[4];
#pragma unroll
        for (int r = 0; r < 4; r++) h1[r] = silu_val(acc[i][m][r], rs[m], mr[m] * cs[i][r]);
        const u32 p0 = pack_bf(h1[0], h1[1]), p1 = pack_bf(h1[2], h1[3]);
        const auto s0 = __builtin_amdgcn_permlane16_swap(p0, p0, false, false), s1 = __builtin_amdgcn_permlane16_swap(p1, p1, false, false);
        __builtin_amdgcn_raw_buffer_store_b128(u32x4{s0[0], s1[0], s0[1], s1[1]}, hrs, o_t[i] + m * 1024, 0, 16);
      }
      SCHED_FENCE();
    }
    STAMP(ws, 1, 4);
  }
  u32x4 w2[22];
#pragma unroll
  for (int i = 0; i < 8; i++) bload16_asm_sc1(w2[i], rw2, lane * 16, i * 1024);
  load_w2_tail_support(w2, rw2, lane * 16, w);
  asm volatile("s_cmp_lt_u32 %0, 4\n\ts_cbranch_scc0 1f\n\ts_waitcnt vmcnt(0)\n1:\n\ts_waitcnt lgkmcnt(0)" ::"s"(w) : "memory", "scc");
  STAMP(ws, 1, 5);
  __builtin_amdgcn_s_barrier();
  STAMP(ws, 1, 6);
  if (w == 7) {
    flag_store(ws + WS_F3, b, ep, lane);
    poll_flags<1>(ws + WS_F3, (b & 3) + (b & 56) + 4 * ((lane >> 2) & 1) + 64 * (lane & 3), 0, ep);
  }
  if (w < 4) __builtin_amdgcn_s_sleep(2);
  load_w2_tail_main(w2, rw2, lane * 16, w);
  __builtin_amdgcn_s_barrier();
  STAMP(ws, 1, 7);

  const int dh = w & 1, kq = w >> 1;
  const __amdgpu_buffer_rsrc_t rhf = mk_rsrc(ws + WS_HF + (size_t)rt * 44 * 4096 + (size_t)(11 * kq * 4 + 2 * dh) * 1024, 11 * 4096);
  const u32 ldst = (u32)(size_t)sm + (kq * 4 + 2 * dh) * 1024;
  auto stage1 = [&](int s, int e) {
    const int slot = s < 9 ? s : s - 9;
    glds16<true>(rhf, lane * 16, s * 4096 + e * 1024, ldst + slot * 16384 + e * 1024);
  };
  auto stage = [&](int s) { stage1(s, 0); stage1(s, 1); };
  stage(0); stage(1); stage(2); stage(3);
  f32x4 acc2[2][4];
#pragma unroll
  for (int i = 0; i < 2; i++)
#pragma unroll
    for (int m = 0; m < 4; m++) acc2[i][m] = f32x4{0.f, 0.f, 0.f, 0.f};
  asm volatile("s_waitcnt vmcnt(4)"
               : "+v"(w2[0]), "+v"(w2[1]), "+v"(w2[2]), "+v"(w2[3]), "+v"(w2[4]), "+v"(w2[5]), "+v"(w2[6]), "+v"(w2[7]), "+v"(w2[8]), "+v"(w2[9]), "+v"(w2[10]),
                 "+v"(w2[11]), "+v"(w2[12]), "+v"(w2[13]), "+v"(w2[14]), "+v"(w2[15]), "+v"(w2[16]), "+v"(w2[17]), "+v"(w2[18]), "+v"(w2[19]), "+v"(w2[20]), "+v"(w2[21])
               ::"memory");
  __builtin_amdgcn_s_barrier();
  STAMP(ws, 1, 8);
  {
    const char* base = sm + kq * 4096 + lane * 16;
    u32x4 hb[2][4];
#pragma unroll
    for (int m = 0; m < 4; m++) hb[0][m] = *(const u32x4*)(base + m * 1024);
#pragma unroll
    for (int s = 0; s < 11; s++) {
      if (s == 1) { asm volatile("s_waitcnt vmcnt(4)" ::: "memory"); __builtin_amdgcn_s_barrier(); }
      else if (s == 3 || s == 5) { asm volatile("s_waitcnt vmcnt(6)" ::: "memory"); __builtin_amdgcn_s_barrier(); }
      else if (s == 7) { asm volatile("s_waitcnt vmcnt(2)" ::: "memory"); __builtin_amdgcn_s_barrier(); }
      else if (s == 9) { asm volatile("s_waitcnt vmcnt(0)" ::: "memory"); __builtin_amdgcn_s_barrier(); }
      const int slot = s + 1 < 9 ? s + 1 : s + 1 - 9;
#pragma unroll
      for (int m = 0; m < 4; m++) {
        acc2[0][m] = mfma(w2[2 * s], hb[s & 1][m], acc2[0][m]);
        SCHED_FENCE();
        if (s < 10) hb[(s + 1) & 1][m] = *(const u32x4*)(base + slot * 16384 + m * 1024);
        SCHED_FENCE();
        acc2[1][m] = mfma(w2[2 * s + 1], hb[s & 1][m], acc2[1][m]);
        SCHED_FENCE();
        if (s < 2) stage1(4 + 2 * s + (m >> 1), m & 1);
        else if (s < 5 && (m & 1)) stage1(s + 6, m >> 1);
        SCHED_FENCE();
      }
      if (s == 2) STAMP(ws, 1, 3);
      if (s == 5) STAMP(ws, 1, 11);
      if (s == 8) STAMP(ws, 1, 15);
    }
  }
  asm volatile("s_waitcnt lgkmcnt(0)" ::: "memory");
  __builtin_amdgcn_s_barrier();
  STAMP(ws, 1, 9);
  {
    char* red = sm + L_RED + dh * 32768;
    f32x4 o0, o1;
    auto scatter = [&](auto KQ) {
      constexpr int k = decltype(KQ)::value;
#pragma unroll
      for (int m = 0; m < 4; m++) {
        if (m != k) {
#pragma unroll
          for (int i = 0; i < 2; i++) *(f32x4*)(red + (((k * 4 + m) * 2 + i) * 64 + lane) * 16) = acc2[i][m];
        }
      }
      o0 = acc2[0][k]; o1 = acc2[1][k];
      asm volatile("" : "+v"(o0), "+v"(o1));
    };
    if (kq == 0) scatter(IntC<0>{}); else if (kq == 1) scatter(IntC<1>{}); else if (kq == 2) scatter(IntC<2>{}); else scatter(IntC<3>{});
    __syncthreads();
#pragma unroll
    for (int j = 1; j < 4; j++) {
      const int src = (kq + j) & 3;
      o0 += *(const f32x4*)(red + (((src * 4 + kq) * 2 + 0) * 64 + lane) * 16);
      o1 += *(const f32x4*)(red + (((src * 4 + kq) * 2 + 1) * 64 + lane) * 16);
    }
    const u32x4 hres = *(const u32x4*)(sm + L_RES + (w * 64 + lane) * 16);
    u32x4 res;
#pragma unroll
    for (int r = 0; r < 4; r++) res[r] = pack_bf(bf_lo(hres[r]) + rb(o0[r]), bf_hi(hres[r]) + rb(o1[r]));
    *(u32x4*)(OUT + (size_t)(row0 + 16 * kq + n) * D + 64 * c + 32 * dh + 8 * g) = res;
  }
  STAMP(ws, 1, 10);
  STAMP_FLUSH(ws, 1);
  asm volatile("ffn_text_end:");
}

extern "C" void kernel_launch(void* const* d_in, const int* in_sizes, int n_in, void* d_out, int out_size, void* d_ws,
                              size_t ws_size, hipStream_t stream) {
  fused_block<<<256, 512, 0, stream>>>((const u16*)d_in[0], (const u16*)d_in[1], (const u16*)d_in[2], (const u16*)d_in[3], (const u16*)d_in[4],
                                       (const u16*)d_in[5], (const u16*)d_in[7], (const u16*)d_in[8], (char*)d_ws, (u16*)d_out);
}
```

```cpp
#include <hip/hip_runtime.h>
#include <stdint.h>

typedef unsigned short u16;
typedef unsigned int u32;
typedef unsigned long long u64;
typedef __bf16 bf16x8 __attribute__((ext_vector_type(8)));
typedef __bf16 bf16x2 __attribute__((ext_vector_type(2)));
typedef short s16x4 __attribute__((ext_vector_type(4)));
typedef float f32x4 __attribute__((ext_vector_type(4)));
typedef float f32x2 __attribute__((ext_vector_type(2)));
typedef u32 u32x4 __attribute__((ext_vector_type(4)));
typedef u32 u32x2 __attribute__((ext_vector_type(2)));
#define GAS __attribute__((address_space(1)))
#define LAS __attribute__((address_space(3)))
#define RLX_AGENT __ATOMIC_RELAXED, __HIP_MEMORY_SCOPE_AGENT
#define SCHED_FENCE() __builtin_amdgcn_sched_barrier(0)

constexpr int S = 2048, D = 512, F = 1376;

constexpr size_t WS_EP   = 0;
constexpr size_t WS_F2   = 4096;
constexpr size_t WS_F3   = WS_F2 + 32768;
constexpr size_t WS_FW   = WS_F3 + 32768;
constexpr size_t WS_KVT  = 1 << 17;
constexpr size_t WS_YT   = WS_KVT + (size_t)D * S * 4;
constexpr size_t WS_W1T  = WS_YT + (size_t)D * S * 2;
constexpr size_t WS_W2T  = WS_W1T + 8 * 11 * 16 * 1024;
constexpr size_t WS_CSP  = WS_W2T + 8 * 4 * 44 * 1024;
constexpr size_t WS_HF   = WS_CSP + 16 * 8 * 11 * 16 * 4;
constexpr size_t WS_END  = WS_HF + (size_t)32 * 44 * 4 * 1024;

__device__ __forceinline__ float bf_lo(u32 v) { return __uint_as_float(v << 16); }
__device__ __forceinline__ float bf_hi(u32 v) { return __uint_as_float(v & 0xffff0000u); }
__device__ __forceinline__ u32 pack_bf(float a, float b) {
  f32x2 z = {a, b};
  return __builtin_bit_cast(u32, __builtin_convertvector(z, bf16x2));
}
__device__ __forceinline__ float rb(float a) { return bf_lo(pack_bf(a, 0.f)); }
__device__ __forceinline__ float dot2(u32 a, u32 b, float c) {
  return __builtin_amdgcn_fdot2_f32_bf16(__builtin_bit_cast(bf16x2, a), __builtin_bit_cast(bf16x2, b), c, false);
}
__device__ __forceinline__ u32 perm_lo(u32 a, u32 b) { return __builtin_amdgcn_perm(b, a, 0x05040100u); }
__device__ __forceinline__ u32 perm_hi(u32 a, u32 b) { return __builtin_amdgcn_perm(b, a, 0x07060302u); }
__device__ __forceinline__ f32x4 mfma(u32x4 a, u32x4 b, f32x4 c) {
  return __builtin_amdgcn_mfma_f32_16x16x32_bf16(__builtin_bit_cast(bf16x8, a), __builtin_bit_cast(bf16x8, b), c, 0, 0, 0);
}
constexpr u32 ONES = 0x3F803F80u;
template <int V> struct IntC { static constexpr int value = V; };
__device__ __forceinline__ float sum_g(float x) {
  auto s = __builtin_amdgcn_permlane16_swap(__float_as_uint(x), __float_as_uint(x), false, false);
  x = __uint_as_float(s[0]) + __uint_as_float(s[1]);
  auto t = __builtin_amdgcn_permlane32_swap(__float_as_uint(x), __float_as_uint(x), false, false);
  return __uint_as_float(t[0]) + __uint_as_float(t[1]);
}
template <int CTRL>
__device__ __forceinline__ float dpp_add(float x) {
  return x + __uint_as_float(__builtin_amdgcn_update_dpp(0u, __float_as_uint(x), CTRL, 0xf, 0xf, true));
}
__device__ __forceinline__ float wave_sum(float x) {
  x = dpp_add<0xB1>(x);
  x = dpp_add<0x4E>(x);
  x = dpp_add<0x141>(x);
  x = dpp_add<0x140>(x);
  return sum_g(x);
}
__device__ __forceinline__ __amdgpu_buffer_rsrc_t mk_rsrc(const void* p, u32 bytes) {
  return __builtin_amdgcn_make_buffer_rsrc((void*)p, 0, bytes, 0x00020000);
}
__device__ __forceinline__ u32x4 bload16(__amdgpu_buffer_rsrc_t r, u32 off) { return __builtin_amdgcn_raw_buffer_load_b128(r, off, 0, 0); }
__device__ __forceinline__ u32x2 bload8(__amdgpu_buffer_rsrc_t r, u32 off) { return __builtin_amdgcn_raw_buffer_load_b64(r, off, 0, 0); }
template <bool SC1>
__device__ __forceinline__ void glds16(__amdgpu_buffer_rsrc_t r, u32 voff, u32 soff, u32 lds_dst) {
  if (SC1)
    asm volatile("s_mov_b32 m0, %0\n\ts_nop 0\n\tbuffer_load_dwordx4 %1, %2, %3 offen sc1 lds" ::"s"(lds_dst), "v"(voff), "s"(r), "s"(soff) : "memory");
  else
    asm volatile("s_mov_b32 m0, %0\n\ts_nop 0\n\tbuffer_load_dwordx4 %1, %2, %3 offen lds" ::"s"(lds_dst), "v"(voff), "s"(r), "s"(soff) : "memory");
}
__device__ __forceinline__ void bload16_asm(u32x4& d, __amdgpu_buffer_rsrc_t r, u32 voff, u32 soff) {
  asm volatile("buffer_load_dwordx4 %0, %1, %2, %3 offen" : "=v"(d) : "v"(voff), "s"(r), "s"(soff) : "memory");
}
__device__ __forceinline__ void bload16_asm_sc1(u32x4& d, __amdgpu_buffer_rsrc_t r, u32 voff, u32 soff) {
  asm volatile("buffer_load_dwordx4 %0, %1, %2, %3 offen sc1" : "=v"(d) : "v"(voff), "s"(r), "s"(soff) : "memory");
}
template <int AUX>
__device__ __forceinline__ void gstore16(char* base, size_t off, u32x4 v) {
  const __amdgpu_buffer_rsrc_t r = mk_rsrc(base, 0x7fffffffu);
  __builtin_amdgcn_raw_buffer_store_b128(v, r, (u32)off, 0, AUX);
}
template <int AUX>
__device__ __forceinline__ void gstore8(char* base, size_t off, u32x2 v) {
  const __amdgpu_buffer_rsrc_t r = mk_rsrc(base, 0x7fffffffu);
  __builtin_amdgcn_raw_buffer_store_b64(v, r, (u32)off, 0, AUX);
}
__device__ __forceinline__ u32x4 bload16s(__amdgpu_buffer_rsrc_t r, u32 voff, u32 soff) { return __builtin_amdgcn_raw_buffer_load_b128(r, voff, soff, 16); }
__device__ __forceinline__ u32x2 tr_read(const char* p) {
  s16x4 r = __builtin_amdgcn_ds_read_tr16_b64_v4i16((LAS s16x4*)p);
  return __builtin_bit_cast(u32x2, r);
}

__device__ __forceinline__ void code_prefetch(int part, int nparts, int lane, u32 lds_dump) {
  u64 pc; u32 len;
  asm volatile("s_getpc_b64 %0\n\ts_mov_b32 %1, ffn_text_end-." : "=s"(pc), "=s"(len));
  const __amdgpu_buffer_rsrc_t r = mk_rsrc((const void*)pc, len);
  if ((u32)part * 1024 < len) glds16<false>(r, lane * 16, part * 1024, lds_dump);
}
__device__ __forceinline__ int fcol(int ks, int g, int j) {
  const int slot = 8 * (g >> 1) + j;
  if (ks < 40) { const int ct = ks / 5, p = ks - 5 * ct; return 128 * (2 * p + (g & 1)) + 8 * slot + ct; }
  return 1280 + 8 * slot + 2 * (ks - 40) + (g & 1);
}

constexpr int HC_STRIDE = 4608;
constexpr int HC_CH = 8 * HC_STRIDE;
constexpr int U_CH = (240 + 2048) * 2;
constexpr int P_H = 0, P_U = 2 * HC_CH, P_R = P_U + 2 * U_CH + 32;
constexpr int P_T = P_R + 4 * 4096;
constexpr int P_END = P_T + 8192;
constexpr int L_H = 0;
constexpr int L_Y = 65536;
constexpr int L_X1 = 131072;
constexpr int L_ST = 147456;
constexpr int L_CS = L_ST + 512;
constexpr int L_RES = L_CS + 768;
constexpr int L_RED = 0;
constexpr int L_FLG = L_RES + 8192;
constexpr int L_TOT = L_FLG + 128;
enum { W_CA = 12, W_CB = 16, W_HRDY = 24, W_CD = 28, W_PART = 32, W_WDONE = 56, W_CE = 60, W_FWOK = 20, W_LN2 = 64, W_W1S = 68 };
static_assert(P_END <= L_X1 && L_TOT <= 163840, "LDS map");
#define KV_SLEEP 8
#define KV_FIRST 32
#define W1_PRE 6
__device__ __forceinline__ constexpr int hoff(int c) {
  return c == 0 ? 4 : c == 1 ? 2 : c == 2 ? 8 : c == 3 ? 3 : c == 4 ? 7 : c == 5 ? 0 : c == 6 ? 15 : 12;
}
__device__ __forceinline__ int hoff_rt(int c) { return (0xCF073824u >> (4 * c)) & 15; }
__device__ __forceinline__ void lds_wait(const char* p, u32 want) {
  for (u32 spins = 0; spins < (1u << 22); spins++) {
    if (__builtin_amdgcn_readfirstlane(*(volatile LAS u32*)p) == want) break;
    __builtin_amdgcn_s_sleep(1);
  }
  asm volatile("" ::: "memory");
}
__device__ __forceinline__ void lds_arrive(char* p, int lane) {
  asm volatile("s_waitcnt lgkmcnt(0)" ::: "memory");
  if (lane == 0) __hip_atomic_fetch_add((LAS u32*)p, 1u, __ATOMIC_RELAXED, __HIP_MEMORY_SCOPE_WORKGROUP);
}
__device__ __forceinline__ void lds_set(char* p, u32 v, int lane) {
  asm volatile("s_waitcnt lgkmcnt(0)" ::: "memory");
  if (lane == 0) *(volatile LAS u32*)p = v;
}
__device__ __forceinline__ void flag_store(char* base, int idx, u64 ep, int lane) {
  if (lane < 16) __hip_atomic_store((GAS u64*)(base + (size_t)idx * 128 + lane * 8), ep, RLX_AGENT);
}
template <int N>
__device__ __forceinline__ void poll_flags(const char* base, int first, int stride, u64 ep) {
  for (u32 spins = 0; spins < (1u << 20); spins++) {
    bool ok = true;
#pragma unroll
    for (int i = 0; i < N; i++) ok &= __hip_atomic_load((GAS u64*)(base + (size_t)(first + i * stride) * 128), RLX_AGENT) == ep;
    if (__all(ok)) break;
    __builtin_amdgcn_s_sleep(1);
  }
  asm volatile("" ::: "memory");
}

#define W2_LOADS                                                                                                                   \
  "buffer_load_dwordx4 %0, %14, %15, %16 offen sc1\n\tbuffer_load_dwordx4 %1, %14, %15, %16 offen offset:1024 sc1\n\t"                      \
  "buffer_load_dwordx4 %2, %14, %15, %16 offen offset:2048 sc1\n\tbuffer_load_dwordx4 %3, %14, %15, %16 offen offset:3072 sc1\n\t"          \
  "buffer_load_dwordx4 %4, %14, %15, %17 offen sc1\n\tbuffer_load_dwordx4 %5, %14, %15, %17 offen offset:1024 sc1\n\t"                      \
  "buffer_load_dwordx4 %6, %14, %15, %17 offen offset:2048 sc1\n\tbuffer_load_dwordx4 %7, %14, %15, %17 offen offset:3072 sc1\n\t"          \
  "buffer_load_dwordx4 %8, %14, %15, %18 offen sc1\n\tbuffer_load_dwordx4 %9, %14, %15, %18 offen offset:1024 sc1\n\t"                      \
  "buffer_load_dwordx4 %10, %14, %15, %18 offen offset:2048 sc1\n\tbuffer_load_dwordx4 %11, %14, %15, %18 offen offset:3072 sc1\n\t"        \
  "buffer_load_dwordx4 %12, %14, %15, %19 offen sc1\n\tbuffer_load_dwordx4 %13, %14, %15, %19 offen offset:1024 sc1\n"
__device__ __forceinline__ void load_w2_tail_support(u32x4 (&w2)[22], __amdgpu_buffer_rsrc_t r, u32 voff, int w) {
  asm volatile("s_cmp_lt_u32 %20, 4\n\ts_cbranch_scc1 1f\n\t" W2_LOADS "1:"
               : "=&v"(w2[8]), "=&v"(w2[9]), "=&v"(w2[10]), "=&v"(w2[11]), "=&v"(w2[12]), "=&v"(w2[13]), "=&v"(w2[14]), "=&v"(w2[15]), "=&v"(w2[16]),
                 "=&v"(w2[17]), "=&v"(w2[18]), "=&v"(w2[19]), "=&v"(w2[20]), "=&v"(w2[21])
               : "v"(voff), "s"(r), "s"(8192), "s"(12288), "s"(16384), "s"(20480), "s"(w) : "memory", "scc");
}
__device__ __forceinline__ void load_w2_tail_main(u32x4 (&w2)[22], __amdgpu_buffer_rsrc_t r, u32 voff, int w) {
  asm volatile("s_cmp_lt_u32 %20, 4\n\ts_cbranch_scc0 1f\n\t" W2_LOADS "1:"
               : "+v"(w2[8]), "+v"(w2[9]), "+v"(w2[10]), "+v"(w2[11]), "+v"(w2[12]), "+v"(w2[13]), "+v"(w2[14]), "+v"(w2[15]), "+v"(w2[16]),
                 "+v"(w2[17]), "+v"(w2[18]), "+v"(w2[19]), "+v"(w2[20]), "+v"(w2[21])
               : "v"(voff), "s"(r), "s"(8192), "s"(12288), "s"(16384), "s"(20480), "s"(w) : "memory", "scc");
}
__device__ __forceinline__ float silu_val(float acc, float rs, float bias) {
  const float tt = fmaf(acc, rs, bias);
  const float e = __builtin_amdgcn_exp2f(tt);
  return tt * __builtin_amdgcn_rcpf(fmaf(e, -1.44269504089f, -1.44269504089f));
}

__global__ __launch_bounds__(512, 2) void fused_block(const u16* __restrict__ X, const u16* __restrict__ K, const u16* __restrict__ V,
                                                      const u16* __restrict__ SK, const u16* __restrict__ HK, const u16* __restrict__ DV,
                                                      const u16* __restrict__ W1, const u16* __restrict__ W2, char* __restrict__ ws,
                                                      u16* __restrict__ OUT) {
  __shared__ __attribute__((aligned(16))) char sm[L_TOT];
  const int b = blockIdx.x, t = threadIdx.x, lane = t & 63;
  const int w = __builtin_amdgcn_readfirstlane(t >> 6);
  const int n = lane & 15, g = lane >> 4;
  char* lf = sm + L_FLG;
#pragma unroll
  for (int i = 0; i < 4; i++) code_prefetch(w + 8 * i, 32, lane, (u32)(size_t)(sm + L_ST));
  if (t < 32) *(volatile LAS u32*)(lf + t * 4) = 0u;
  asm volatile("s_waitcnt lgkmcnt(0)" ::: "memory");
  __builtin_amdgcn_s_barrier();
  const int chp = 64 * (b & 7) + 2 * (b >> 3);
  const int rt = 8 * (b & 3) + ((b >> 3) & 7), c = 4 * ((b >> 2) & 1) + (b >> 6);
  const int row0 = 64 * rt;
  const u64 ep = *(const u64*)(ws + WS_EP) + 1;
  const u32 dvp = *(const u32*)(DV + chp);

  u32x4 wl[8];
  const int unit = w >= 4 ? b + 256 * (w - 4) : 1 << 20;
  if (w < 4) {
    const int dt = b & 7, st = b >> 3;
    const int rp = t >> 3, c8 = t & 7;
    const int s = st * 64 + 2 * rp, ch0 = dt * 64 + c8 * 8;
    const __amdgpu_buffer_rsrc_t rk = mk_rsrc(K, S * D * 2);
    const u32 off = (u32)(s * D + ch0) * 2;
    u32x4 kk[5];
#pragma unroll
    for (int i = 0; i < 5; i++) kk[i] = bload16(rk, off + (1 - i) * 1024);
    const u32x4 v0 = *(const u32x4*)((const char*)V + off), v1 = *(const u32x4*)((const char*)V + off + 1024);
    u32x4 wa[4];
#pragma unroll
    for (int i = 0; i < 4; i++) wa[i] = *(const u32x4*)(SK + ch0 * 4 + i * 8);
    SCHED_FENCE();
    char* pk = sm + P_T + (8 * c8) * 128 + (((rp >> 2) ^ c8) << 4) + ((3 - (rp & 3)) << 2);
#pragma unroll
    for (int i = 0; i < 4; i++) {
      const u32 l01 = perm_lo(kk[0][i], kk[1][i]), l12 = perm_lo(kk[1][i], kk[2][i]), l23 = perm_lo(kk[2][i], kk[3][i]), l34 = perm_lo(kk[3][i], kk[4][i]);
      const u32 h01 = perm_hi(kk[0][i], kk[1][i]), h12 = perm_hi(kk[1][i], kk[2][i]), h23 = perm_hi(kk[2][i], kk[3][i]), h34 = perm_hi(kk[3][i], kk[4][i]);
      const float a1 = dot2(l01, wa[i][0], dot2(l23, wa[i][1], 0.f)) * bf_lo(v1[i]);
      const float a0 = dot2(l12, wa[i][0], dot2(l34, wa[i][1], 0.f)) * bf_lo(v0[i]);
      const float b1 = dot2(h01, wa[i][2], dot2(h23, wa[i][3], 0.f)) * bf_hi(v1[i]);
      const float b0 = dot2(h12, wa[i][2], dot2(h34, wa[i][3], 0.f)) * bf_hi(v0[i]);
      *(u32*)(pk + (2 * i) * 128) = pack_bf(a1, a0);
      *(u32*)(pk + (2 * i + 1) * 128) = pack_bf(b1, b0);
    }
    lds_arrive(lf + W_CA, lane);
    lds_wait(lf + W_CA, 4u);
    const u32 tag = (u32)ep;
#pragma unroll
    for (int i = 0; i < 2; i++) {
      const int u = t + 256 * i, ch = u >> 3, cc = u & 7;
#pragma unroll
      for (int hh = 0; hh < 2; hh++) {
        const int grp = 4 * hh + (cc >> 1);
        const u32x2 d = *(const u32x2*)(sm + P_T + ch * 128 + ((grp ^ ((ch >> 3) & 7)) << 4) + (cc & 1) * 8);
        gstore16<16>(ws, WS_KVT + (size_t)(dt * 64 + ch) * 8192 + (st * 8 + 4 * hh) * 32 + cc * 16, u32x4{d[0], tag, d[1], tag});
      }
    }
    lds_arrive(lf + W_CB, lane);
    {
      const __amdgpu_buffer_rsrc_t ru = mk_rsrc(ws + WS_KVT + (size_t)chp * 8192, 2 * 8192);
      if (t < 60) *(u32x4*)(sm + P_U + (t < 30 ? 0 : U_CH - 480) + 16 * t) = u32x4{0, 0, 0, 0};
      u32x4 ga[2][2];
      __builtin_amdgcn_s_sleep(KV_FIRST);
      for (u32 spins = 0; spins < (1u << 18); spins++) {
#pragma unroll
        for (int cl = 0; cl < 2; cl++) { ga[cl][0] = bload16s(ru, t * 32, cl * 8192); ga[cl][1] = bload16s(ru, t * 32 + 16, cl * 8192); }
        bool ok = true;
#pragma unroll
        for (int cl = 0; cl < 2; cl++) ok &= ga[cl][0][1] == tag && ga[cl][0][3] == tag && ga[cl][1][1] == tag && ga[cl][1][3] == tag;
        if (__all(ok)) break;
        __builtin_amdgcn_s_sleep(KV_SLEEP);
      }
#pragma unroll
      for (int cl = 0; cl < 2; cl++) *(u32x4*)(sm + P_U + cl * U_CH + 480 + 16 * t) = u32x4{ga[cl][0][0], ga[cl][0][2], ga[cl][1][0], ga[cl][1][2]};
    }
    lds_arrive(lf + W_CD, lane);
    lds_wait(lf + W_HRDY, 4u);
    lds_wait(lf + W_CD, 4u);
  } else {
    const int s = t - 256;
    u32x4 ha[2], hb[2];
    const __amdgpu_buffer_rsrc_t rh_ = mk_rsrc(HK + (size_t)chp * S, 2 * S * 2);
#pragma unroll
    for (int cl = 0; cl < 2; cl++) { ha[cl] = bload16(rh_, (u32)(cl * S + 8 * s) * 2 - 16); hb[cl] = bload16(rh_, (u32)(cl * S + 8 * s) * 2); }
    if (w == 7) {
      const char* p;
      if (lane < 24) p = ws + (size_t)lane * (1 << 20);
      else if (lane < 27) p = (const char*)OUT + (size_t)(lane - 24) * ((1 << 20) - 64);
      else if (lane < 30) p = (const char*)X + (size_t)(lane - 27) * ((1 << 20) - 64);
      else if (lane < 32) p = (const char*)W1 + (size_t)(lane - 30) * (D * F * 2 - 64);
      else if (lane < 34) p = (const char*)W2 + (size_t)(lane - 32) * (D * F * 2 - 64);
      else p = ws + WS_F2 + (size_t)(lane - 34) * 4096;
      asm volatile("s_mov_b32 m0, %0\n\ts_nop 0\n\tglobal_load_lds_dword %1, off" ::"s"((u32)(size_t)(sm + L_ST + 1024)), "v"(p) : "memory");
    }

    SCHED_FENCE();
#pragma unroll
    for (int cl = 0; cl < 2; cl++) {
      if (s == 0) ha[cl] = u32x4{0, 0, 0, 0};
      char* hbase = sm + P_H + cl * HC_CH;
      const u32 W[8] = {ha[cl][0], ha[cl][1], ha[cl][2], ha[cl][3], hb[cl][0], hb[cl][1], hb[cl][2], hb[cl][3]};
#pragma unroll
      for (int cc = 0; cc < 8; cc++) {
        u32x4 o;
#pragma unroll
        for (int a = 0; a < 4; a++) o[a] = (cc & 1) ? __builtin_amdgcn_alignbit(W[(cc >> 1) + a + 1], W[(cc >> 1) + a], 16) : W[(cc >> 1) + a];
        *(u32x4*)(hbase + cc * HC_STRIDE + 16 * (s + 1 + hoff(cc))) = o;
      }
      if (s < 8) *(u32x4*)(hbase + s * HC_STRIDE + 16 * hoff_rt(s)) = u32x4{0, 0, 0, 0};
      if (s == 255) *(u32x4*)(hbase + 16 * (257 + hoff(0))) = hb[cl];
    }
    lds_arrive(lf + W_HRDY, lane);
    lds_wait(lf + W_CB, 4u);
    {
      const int lc = lane & 15, lg = lane >> 4;
      if (unit < 176) {
        const __amdgpu_buffer_rsrc_t rw = mk_rsrc(W1, D * F * 2);
        const int blk = unit >> 4, ks = unit & 15;
        const int col = 128 * blk + 8 * lc;
        const u32 o0 = col < F ? (u32)((32 * ks + 8 * lg) * F + col) * 2 : 0x80000000u;
#pragma unroll
        for (int j = 0; j < 8; j++) wl[j] = bload16(rw, o0 + j * F * 2);
      } else if (unit < 528) {
        const __amdgpu_buffer_rsrc_t rw = mk_rsrc(W2, D * F * 2);
        const int u2 = unit - 176, ks = u2 >> 3, c2 = u2 & 7;
#pragma unroll
        for (int j = 0; j < 8; j++) {
          const u32 o = (u32)(fcol(ks, lg, j) * D + 64 * c2 + 8 * (lc >> 2) + 2 * (lc & 3)) * 2;
          wl[j][0] = __builtin_amdgcn_raw_buffer_load_b32(rw, o, 0, 0);
          wl[j][1] = __builtin_amdgcn_raw_buffer_load_b32(rw, o + 64, 0, 0);
        }
      }
    }
  }

  u32x4 xf[2][4];
  auto load_x = [&]() {
#pragma unroll
    for (int kk = 0; kk < 2; kk++)
#pragma unroll
      for (int m = 0; m < 4; m++) xf[kk][m] = *(const u32x4*)(X + (size_t)(row0 + 16 * m + n) * D + 32 * (2 * w + kk) + 8 * g);
  };
  auto ln1_partials = [&]() {
    const u32x4 ones = {ONES, ONES, ONES, ONES};
#pragma unroll
    for (int m = 0; m < 4; m++) {
      f32x4 q0 = {0.f, 0.f, 0.f, 0.f}, q1 = {0.f, 0.f, 0.f, 0.f};
#pragma unroll
      for (int kk = 0; kk < 2; kk++) { q0 = mfma(xf[kk][m], xf[kk][m], q0); q1 = mfma(ones, xf[kk][m], q1); }
      const float lo = (n & 1) ? q0[1] : q0[0], hi = (n & 1) ? q0[3] : q0[2];
      if (g == (n >> 2)) *(f32x2*)(sm + L_X1 + ((w * 4 + m) * 16 + n) * 8) = f32x2{q1[0], (n & 2) ? hi : lo};
    }
  };
  constexpr int NT = 3, NW1 = 48, PRE = W1_PRE * NT;
  u32x4 w1[NW1];
  const __amdgpu_buffer_rsrc_t rw1 = mk_rsrc(ws + WS_W1T + (size_t)(c * 11 + 3 * (w & 3)) * 16 * 1024, 48 * 1024);
  auto w1load = [&](int k) { w1[k] = bload16s(rw1, lane * 16, (k % NT) * 16384 + (k / NT) * 1024); };
  if (w < 4) {
    const int wcl = (w >> 1) & 1, rh = w & 1;
    const u32 dvr = dvp >> (16 * wcl);
    f32x4 acc[8];
#pragma unroll
    for (int i = 0; i < 8; i++) acc[i] = f32x4{0.f, 0.f, 0.f, 0.f};
    const int idx0 = 25 + n - 8 * g;
    const int cc = idx0 & 7;
    const char* fbase = sm + P_H + wcl * HC_CH + cc * HC_STRIDE + 16 * ((idx0 >> 3) + hoff_rt(cc)) + 64 * 4 * rh;
    const char* gbase = sm + P_U + wcl * U_CH + 2 * (16 * n + 8 * g + 224) - 64 * 4 * rh;
    u32x4 Ff[2][8], Gf[2][8];
#pragma unroll
    for (int a = 0; a < 8; a++) { Gf[0][a] = *(const u32x4*)(gbase + 64 * (8 * a)); Ff[0][a] = *(const u32x4*)(fbase + 64 * (8 * a)); }
    SCHED_FENCE();
#pragma unroll
    for (int rr = 0; rr < 4; rr++) {
      const int cur = rr & 1, nxt = cur ^ 1;
      int m = 0;
#pragma unroll
      for (int bb = 0; bb < 8; bb++) {
#pragma unroll
        for (int a = 0; a + bb < 8; a++) {
          acc[a + bb] = mfma(Ff[cur][a], Gf[cur][bb], acc[a + bb]);
          if (rr < 3 && (m & 1) == 1 && m < 32) {
            const int q = m >> 2;
            SCHED_FENCE();
            if (m & 2) Ff[nxt][q] = *(const u32x4*)(fbase + 64 * (8 * q + rr + 1));
            else Gf[nxt][q] = *(const u32x4*)(gbase + 64 * (8 * q - rr - 1));
            SCHED_FENCE();
          }
          m++;
        }
      }
      SCHED_FENCE();
    }
    f32x4* rbuf = (f32x4*)(sm + P_R) + w * 256;
    if (rh) {
#pragma unroll
      for (int i = 0; i < 4; i++) rbuf[i * 64 + lane] = acc[i];
#pragma unroll
      for (int i = 0; i < 4; i++) acc[i] = acc[4 + i];
    } else {
#pragma unroll
      for (int i = 0; i < 4; i++) rbuf[i * 64 + lane] = acc[4 + i];
    }
    lds_set(lf + W_PART + w * 4, 1u, lane);
    load_x();
    const int ch = chp + wcl;
    const char* ub = sm + P_U + wcl * U_CH;
    u32x2 uq[4];
#pragma unroll
    for (int i = 0; i < 4; i++) uq[i] = *(const u32x2*)(ub + 2 * (240 + 256 * (4 * rh + i) + 16 * n + 8 * (g >> 1) + ((g & 1) ? 0 : 4)));
    lds_wait(lf + W_PART + (w ^ 1) * 4, 1u);
    const float dv = bf_lo(dvr);
    const f32x4* pbuf = (const f32x4*)(sm + P_R) + (w ^ 1) * 256;
#pragma unroll
    for (int i = 0; i < 4; i++) {
      const int T = 4 * rh + i;
      const f32x4 a = acc[i] + pbuf[i * 64 + lane];
      const int pos = 256 * T + 16 * n + 4 * g;
      const float y0 = rb(a[0]) + rb(bf_hi(uq[i][1]) * dv), y1 = rb(a[1]) + rb(bf_lo(uq[i][1]) * dv);
      const float y2 = rb(a[2]) + rb(bf_hi(uq[i][0]) * dv), y3 = rb(a[3]) + rb(bf_lo(uq[i][0]) * dv);
      gstore8<16>(ws, WS_YT + ((size_t)ch * S + pos) * 2, u32x2{pack_bf(y0, y1), pack_bf(y2, y3)});
    }
    __builtin_amdgcn_s_waitcnt(0x0f70);
    asm volatile("s_waitcnt vmcnt(0)" ::: "memory");
    lds_arrive(lf + W_CE, lane);
    if (w == 0) {
      lds_wait(lf + W_CE, 4u);
      flag_store(ws + WS_F2, b, ep, lane);
    }
    ln1_partials();
    lds_wait(lf + W_FWOK, 1u);
#pragma unroll
    for (int i = 0; i < PRE; i++) w1load(i);
    SCHED_FENCE();
    if (w == 0) {
      poll_flags<4>(ws + WS_F2, lane, 64, ep);
      if (b == 0 && lane == 0) __hip_atomic_store((GAS u64*)(ws + WS_EP), ep, RLX_AGENT);
    }
  } else {
    if (unit < 176) {
      const int blk = unit >> 4, ks = unit & 15;
      float csel[2] = {0.f, 0.f};
#pragma unroll
      for (int i = 0; i < 8; i++) {
        u32x4 o;
#pragma unroll
        for (int a = 0; a < 4; a++) o[a] = (i & 1) ? perm_hi(wl[2 * a][i >> 1], wl[2 * a + 1][i >> 1]) : perm_lo(wl[2 * a][i >> 1], wl[2 * a + 1][i >> 1]);
        gstore16<16>(ws, WS_W1T + ((size_t)((i * 11 + blk) * 16 + ks) * 64 + lane) * 16, o);
        const float cs = sum_g(dot2(o[0], ONES, dot2(o[1], ONES, dot2(o[2], ONES, dot2(o[3], ONES, 0.f)))));
        csel[i >> 2] = g == (i & 3) ? cs : csel[i >> 2];
      }
#pragma unroll
      for (int hh = 0; hh < 2; hh++)
        __hip_atomic_store((GAS u32*)(ws + WS_CSP + (size_t)(ks * 11 + blk) * 512 + hh * 256 + lane * 4), __float_as_uint(csel[hh]), RLX_AGENT);
    } else if (unit < 528) {
      const int u2 = unit - 176, ks = u2 >> 3, c2 = u2 & 7;
#pragma unroll
      for (int i = 0; i < 4; i++) {
        u32x4 o;
#pragma unroll
        for (int a = 0; a < 4; a++) o[a] = (i & 1) ? perm_hi(wl[2 * a][i >> 1], wl[2 * a + 1][i >> 1]) : perm_lo(wl[2 * a][i >> 1], wl[2 * a + 1][i >> 1]);
        gstore16<16>(ws, WS_W2T + ((size_t)(((c2 * 2 + (i >> 1)) * 44 + ks) * 2 + (i & 1)) * 64 + lane) * 16, o);
      }
    }
    __builtin_amdgcn_s_waitcnt(0x0f70);
    asm volatile("s_waitcnt vmcnt(0)" ::: "memory");
    lds_arrive(lf + W_WDONE, lane);
    if (w == 4) {
      lds_wait(lf + W_WDONE, 4u);
      flag_store(ws + WS_FW, b, ep, lane);
    }
    load_x();
    ln1_partials();
    if (w == 4) {
      poll_flags<4>(ws + WS_FW, lane, 64, ep);
      lds_set(lf + W_FWOK, 1u, lane);
    }
  }
  asm volatile("s_waitcnt lgkmcnt(0)" ::: "memory");
  __builtin_amdgcn_s_barrier();
  {
    const __amdgpu_buffer_rsrc_t rh = mk_rsrc(ws + WS_YT + ((size_t)(64 * w) * S + row0) * 2, 64 * S * 2);
    const int f0 = 2 * ((lane >> 4) & 1);
    const u32 v0 = (u32)(lane >> 3) * S * 2 + (((lane & 7) ^ f0) << 4), v1 = (u32)(lane >> 3) * S * 2 + (((lane & 7) ^ (f0 + 4)) << 4);
#pragma unroll
    for (int i = 0; i < 8; i++) glds16<true>(rh, (i & 1) ? v1 : v0, i * 8 * S * 2, (u32)(size_t)(sm + L_Y) + (64 * w + 8 * i) * 128);
  }
  const __amdgpu_buffer_rsrc_t rw2 = mk_rsrc(ws + WS_W2T + (size_t)((c * 2 + (w & 1)) * 44 + 11 * (w >> 1)) * 2048, 22 * 1024);
  float csp[16];
  const int cj = (w & 3) * 64 + lane;
  float r1[4], c1[4];
#pragma unroll
  for (int m = 0; m < 4; m++) {
    float a = 0.f, q = 0.f;
#pragma unroll
    for (int ww = 0; ww < 8; ww++) { const f32x2 v = *(const f32x2*)(sm + L_X1 + ((ww * 4 + m) * 16 + n) * 8); a += v[0]; q += v[1]; }
    const float mu = a * (1.f / D);
    const float var = fmaxf(q * (1.f / D) - mu * mu, 0.f);
    r1[m] = __builtin_amdgcn_rcpf(rb(__builtin_amdgcn_sqrtf(rb(rb(var) + 1e-5f))));
    c1[m] = -rb(mu) * r1[m];
  }
  asm volatile("s_waitcnt vmcnt(0)" ::: "memory");
  __builtin_amdgcn_s_barrier();
  const int q4 = (lane & 15) >> 2, p4 = lane & 3;
  {
    const int fsw = 2 * ((q4 >> 1) & 1) + 4 * (g & 1);
    u32x2 ya[2][4][2];
#pragma unroll
    for (int kk = 0; kk < 2; kk++)
#pragma unroll
      for (int m = 0; m < 4; m++) {
        const char* p = sm + L_Y + (8 * g + q4) * 128 + (((2 * m + (p4 >> 1)) ^ fsw) << 4) + 8 * (p4 & 1) + (2 * w + kk) * 4096;
        ya[kk][m][0] = tr_read(p); ya[kk][m][1] = tr_read(p + 512);
      }
    SCHED_FENCE();
#pragma unroll
    for (int kk = 0; kk < 2; kk++)
#pragma unroll
      for (int m = 0; m < 4; m++) {
        f32x2 xv[4], yv[4], qv[4];
#pragma unroll
        for (int a = 0; a < 4; a++) {
          const u32 xp = xf[kk][m][a], yp = ya[kk][m][a >> 1][a & 1];
          xv[a] = f32x2{bf_lo(xp), bf_hi(xp)}; yv[a] = f32x2{bf_lo(yp), bf_hi(yp)};
        }
        SCHED_FENCE();
#pragma unroll
        for (int a = 0; a < 4; a++) qv[a] = __builtin_elementwise_fma(xv[a], f32x2{r1[m], r1[m]}, f32x2{c1[m], c1[m]});
        SCHED_FENCE();
#pragma unroll
        for (int a = 0; a < 4; a++) qv[a] = __builtin_elementwise_fma(qv[a], yv[a], xv[a]);
        SCHED_FENCE();
        u32x4 hf;
#pragma unroll
        for (int a = 0; a < 4; a++) hf[a] = pack_bf(qv[a][0], qv[a][1]);
        *(u32x4*)(sm + L_H + ((m * 16 + 2 * w + kk) * 64 + lane) * 16) = hf;
      }
  }
  if (w >= 4) {
    const __amdgpu_buffer_rsrc_t rc = mk_rsrc(ws + WS_CSP, 16 * 11 * 512);
    const int cjc = cj < 176 ? cj : 175;
    const u32 co = (u32)((cjc >> 4) * 512 + c * 64 + (cjc & 15) * 4);
#pragma unroll
    for (int ks = 0; ks < 16; ks++) csp[ks] = __uint_as_float(__builtin_amdgcn_raw_buffer_load_b32(rc, co, ks * 11 * 512, 16));
  }
  asm volatile("s_waitcnt lgkmcnt(0)" ::: "memory");
  __builtin_amdgcn_s_barrier();
  auto save_residual = [&]() {
    *(u32x4*)(sm + L_RES + (w * 64 + lane) * 16) = *(const u32x4*)(sm + L_H + (((w >> 1) * 16 + 2 * c + (w & 1)) * 64 + lane) * 16);
  };
  if (w >= 4) {
    __builtin_amdgcn_s_setprio(3);
    u32x4 fa[16];
#pragma unroll
    for (int ks = 0; ks < 16; ks++) fa[ks] = *(const u32x4*)(sm + L_H + (((w - 4) * 16 + ks) * 64 + lane) * 16);
    f32x4 q0 = {0.f, 0.f, 0.f, 0.f}, q1 = {0.f, 0.f, 0.f, 0.f};
    const u32x4 ones = {ONES, ONES, ONES, ONES};
#pragma unroll
    for (int ks = 0; ks < 16; ks++) {
      q0 = mfma(fa[ks], fa[ks], q0);
      q1 = mfma(ones, fa[ks], q1);
    }
    {
      const float lo = (n & 1) ? q0[1] : q0[0], hi = (n & 1) ? q0[3] : q0[2];
      const float q = (n & 2) ? hi : lo;
      const float mu = q1[0] * (1.f / D);
      const float var = fmaxf(q * (1.f / D) - mu * mu, 0.f);
      const float rstd = __builtin_amdgcn_rcpf(rb(__builtin_amdgcn_sqrtf(rb(rb(var) + 1e-5f))));
      if (g == (n >> 2)) *(f32x2*)(sm + L_ST + ((w - 4) * 16 + n) * 8) = f32x2{-1.44269504089f * rstd, rb(mu) * rstd * 1.44269504089f};
    }
    {
      float s4 = 0.f;
#pragma unroll
      for (int ks = 0; ks < 16; ks++) s4 += csp[ks];
      if (cj < 192) *(float*)(sm + L_CS + cj * 4) = s4;
    }
    save_residual();
    lds_arrive(lf + W_LN2, lane);
    __builtin_amdgcn_s_setprio(0);
    lds_wait(lf + W_W1S, 1u);
  } else {
    auto issued = [&](int ks) constexpr { const int v = PRE + NT * (ks + 1); return v < NW1 ? v : NW1; };
    f32x4 acc[NT][4];
    u32x4 hb[3][4];
#pragma unroll
    for (int i = 0; i < NT; i++)
#pragma unroll
      for (int m = 0; m < 4; m++) acc[i][m] = f32x4{0.f, 0.f, 0.f, 0.f};
    const char* hbase = sm + L_H + lane * 16;
#pragma unroll
    for (int d = 0; d < 2; d++)
#pragma unroll
      for (int m = 0; m < 4; m++) hb[d][m] = *(const u32x4*)(hbase + (m * 16 + d) * 1024);
#pragma unroll
    for (int ks = 0; ks < 16; ks++) {
#pragma unroll
      for (int m = 0; m < 4; m++) {
        acc[0][m] = mfma(w1[ks * NT], hb[ks % 3][m], acc[0][m]);
        SCHED_FENCE();
        if (ks + 2 < 16) hb[(ks + 2) % 3][m] = *(const u32x4*)(hbase + (m * 16 + ks + 2) * 1024);
        SCHED_FENCE();
        acc[1][m] = mfma(w1[ks * NT + 1], hb[ks % 3][m], acc[1][m]);
        SCHED_FENCE();
        if (m < NT && issued(ks - 1) + m < issued(ks)) w1load(issued(ks - 1) + m);
        SCHED_FENCE();
        acc[2][m] = mfma(w1[ks * NT + 2], hb[ks % 3][m], acc[2][m]);
        SCHED_FENCE();
      }
      if (ks == 15 - W1_PRE && w == 0 && lane == 0) *(volatile LAS u32*)(lf + W_W1S) = 1u;
    }
    save_residual();
    lds_wait(lf + W_LN2, 4u);
    float rs[4], mr[4];
    f32x4 cs[NT];
#pragma unroll
    for (int m = 0; m < 4; m++) { const f32x2 v = *(const f32x2*)(sm + L_ST + (m * 16 + n) * 8); rs[m] = v[0]; mr[m] = v[1]; }
#pragma unroll
    for (int i = 0; i < NT; i++) cs[i] = *(const f32x4*)(sm + L_CS + ((3 * w + i) * 16 + 4 * g) * 4);
    const __amdgpu_buffer_rsrc_t hrs = mk_rsrc(ws + WS_HF + (size_t)rt * 44 * 4096, 44 * 4096);
    u32 o_t[NT];
#pragma unroll
    for (int i = 0; i < NT; i++) {
      const int tl = 3 * w + i;
      const int ks2 = tl < 10 ? 5 * c + (tl >> 1) : 40 + (c >> 1), par = tl < 10 ? (tl & 1) : (c & 1);
      o_t[i] = (tl < 11 && (g & 1) == par) ? (u32)(ks2 * 4096 + lane * 16) : 0x40000000u;
    }
#pragma unroll
    for (int m = 0; m < 4; m++) {
#pragma unroll
      for (int i = 0; i < NT; i++) {
        float h1[4];
#pragma unroll
        for (int r = 0; r < 4; r++) h1[r] = silu_val(acc[i][m][r], rs[m], mr[m] * cs[i][r]);
        const u32 p0 = pack_bf(h1[0], h1[1]), p1 = pack_bf(h1[2], h1[3]);
        const auto s0 = __builtin_amdgcn_permlane16_swap(p0, p0, false, false), s1 = __builtin_amdgcn_permlane16_swap(p1, p1, false, false);
        __builtin_amdgcn_raw_buffer_store_b128(u32x4{s0[0], s1[0], s0[1], s1[1]}, hrs, o_t[i] + m * 1024, 0, 16);
      }
      SCHED_FENCE();
    }
  }
  u32x4 w2[22];
#pragma unroll
  for (int i = 0; i < 8; i++) bload16_asm_sc1(w2[i], rw2, lane * 16, i * 1024);
  load_w2_tail_support(w2, rw2, lane * 16, w);
  asm volatile("s_cmp_lt_u32 %0, 4\n\ts_cbranch_scc0 1f\n\ts_waitcnt vmcnt(0)\n1:\n\ts_waitcnt lgkmcnt(0)" ::"s"(w) : "memory", "scc");
  __builtin_amdgcn_s_barrier();
  if (w == 7) {
    flag_store(ws + WS_F3, b, ep, lane);
    poll_flags<1>(ws + WS_F3, (b & 3) + (b & 56) + 4 * ((lane >> 2) & 1) + 64 * (lane & 3), 0, ep);
  }
  if (w < 4) __builtin_amdgcn_s_sleep(2);
  load_w2_tail_main(w2, rw2, lane * 16, w);
  __builtin_amdgcn_s_barrier();

  const int dh = w & 1, kq = w >> 1;
  const __amdgpu_buffer_rsrc_t rhf = mk_rsrc(ws + WS_HF + (size_t)rt * 44 * 4096 + (size_t)(11 * kq * 4 + 2 * dh) * 1024, 11 * 4096);
  const u32 ldst = (u32)(size_t)sm + (kq * 4 + 2 * dh) * 1024;
  auto stage1 = [&](int s, int e) {
    const int slot = s < 9 ? s : s - 9;
    glds16<true>(rhf, lane * 16, s * 4096 + e * 1024, ldst + slot * 16384 + e * 1024);
  };
  auto stage = [&](int s) { stage1(s, 0); stage1(s, 1); };
  stage(0); stage(1); stage(2); stage(3);
  f32x4 acc2[2][4];
#pragma unroll
  for (int i = 0; i < 2; i++)
#pragma unroll
    for (int m = 0; m < 4; m++) acc2[i][m] = f32x4{0.f, 0.f, 0.f, 0.f};
  asm volatile("s_waitcnt vmcnt(4)"
               : "+v"(w2[0]), "+v"(w2[1]), "+v"(w2[2]), "+v"(w2[3]), "+v"(w2[4]), "+v"(w2[5]), "+v"(w2[6]), "+v"(w2[7]), "+v"(w2[8]), "+v"(w2[9]), "+v"(w2[10]),
                 "+v"(w2[11]), "+v"(w2[12]), "+v"(w2[13]), "+v"(w2[14]), "+v"(w2[15]), "+v"(w2[16]), "+v"(w2[17]), "+v"(w2[18]), "+v"(w2[19]), "+v"(w2[20]), "+v"(w2[21])
               ::"memory");
  __builtin_amdgcn_s_barrier();
  {
    const char* base = sm + kq * 4096 + lane * 16;
    u32x4 hb[2][4];
#pragma unroll
    for (int m = 0; m < 4; m++) hb[0][m] = *(const u32x4*)(base + m * 1024);
#pragma unroll
    for (int s = 0; s < 11; s++) {
      if (s == 1) { asm volatile("s_waitcnt vmcnt(4)" ::: "memory"); __builtin_amdgcn_s_barrier(); }
      else if (s == 3 || s == 5) { asm volatile("s_waitcnt vmcnt(6)" ::: "memory"); __builtin_amdgcn_s_barrier(); }
      else if (s == 7) { asm volatile("s_waitcnt vmcnt(2)" ::: "memory"); __builtin_amdgcn_s_barrier(); }
      else if (s == 9) { asm volatile("s_waitcnt vmcnt(0)" ::: "memory"); __builtin_amdgcn_s_barrier(); }
      const int slot = s + 1 < 9 ? s + 1 : s + 1 - 9;
#pragma unroll
      for (int m = 0; m < 4; m++) {
        acc2[0][m] = mfma(w2[2 * s], hb[s & 1][m], acc2[0][m]);
        SCHED_FENCE();
        if (s < 10) hb[(s + 1) & 1][m] = *(const u32x4*)(base + slot * 16384 + m * 1024);
        SCHED_FENCE();
        acc2[1][m] = mfma(w2[2 * s + 1], hb[s & 1][m], acc2[1][m]);
        SCHED_FENCE();
        if (s < 2) stage1(4 + 2 * s + (m >> 1), m & 1);
        else if (s < 5 && (m & 1)) stage1(s + 6, m >> 1);
        SCHED_FENCE();
      }
    }
  }
  asm volatile("s_waitcnt lgkmcnt(0)" ::: "memory");
  __builtin_amdgcn_s_barrier();
  {
    char* red = sm + L_RED + dh * 32768;
    f32x4 o0, o1;
    auto scatter = [&](auto KQ) {
      constexpr int k = decltype(KQ)::value;
#pragma unroll
      for (int m = 0; m < 4; m++) {
        if (m != k) {
#pragma unroll
          for (int i = 0; i < 2; i++) *(f32x4*)(red + (((k * 4 + m) * 2 + i) * 64 + lane) * 16) = acc2[i][m];
        }
      }
      o0 = acc2[0][k]; o1 = acc2[1][k];
      asm volatile("" : "+v"(o0), "+v"(o1));
    };
    if (kq == 0) scatter(IntC<0>{}); else if (kq == 1) scatter(IntC<1>{}); else if (kq == 2) scatter(IntC<2>{}); else scatter(IntC<3>{});
    __syncthreads();
#pragma unroll
    for (int j = 1; j < 4; j++) {
      const int src = (kq + j) & 3;
      o0 += *(const f32x4*)(red + (((src * 4 + kq) * 2 + 0) * 64 + lane) * 16);
      o1 += *(const f32x4*)(red + (((src * 4 + kq) * 2 + 1) * 64 + lane) * 16);
    }
    const u32x4 hres = *(const u32x4*)(sm + L_RES + (w * 64 + lane) * 16);
    u32x4 res;
#pragma unroll
    for (int r = 0; r < 4; r++) res[r] = pack_bf(bf_lo(hres[r]) + rb(o0[r]), bf_hi(hres[r]) + rb(o1[r]));
    __builtin_amdgcn_raw_buffer_store_b128(res, mk_rsrc(OUT, S * D * 2), (u32)((row0 + 16 * kq + n) * D + 64 * c + 32 * dh + 8 * g) * 2, 0, 2);
  }
  asm volatile("ffn_text_end:");
}

extern "C" void kernel_launch(void* const* d_in, const int* in_sizes, int n_in, void* d_out, int out_size, void* d_ws,
                              size_t ws_size, hipStream_t stream) {
  fused_block<<<256, 512, 0, stream>>>((const u16*)d_in[0], (const u16*)d_in[1], (const u16*)d_in[2], (const u16*)d_in[3], (const u16*)d_in[4],
                                       (const u16*)d_in[5], (const u16*)d_in[7], (const u16*)d_in[8], (char*)d_ws, (u16*)d_out);
}
```

```cpp
#include <hip/hip_runtime.h>
#include <stdint.h>

typedef unsigned short u16;
typedef unsigned int u32;
typedef unsigned long long u64;
typedef __bf16 bf16x8 __attribute__((ext_vector_type(8)));
typedef __bf16 bf16x2 __attribute__((ext_vector_type(2)));
typedef short s16x4 __attribute__((ext_vector_type(4)));
typedef float f32x4 __attribute__((ext_vector_type(4)));
typedef float f32x2 __attribute__((ext_vector_type(2)));
typedef u32 u32x4 __attribute__((ext_vector_type(4)));
typedef u32 u32x2 __attribute__((ext_vector_type(2)));
#define GAS __attribute__((address_space(1)))
#define LAS __attribute__((address_space(3)))
#define RLX_AGENT __ATOMIC_RELAXED, __HIP_MEMORY_SCOPE_AGENT
#define SCHED_FENCE() __builtin_amdgcn_sched_barrier(0)

constexpr int S = 2048, D = 512, F = 1376;

constexpr size_t WS_FLAG = 0;
constexpr size_t WS_KVT  = 1 << 16;
constexpr size_t WS_XT   = WS_KVT + (size_t)D * S * 2;
constexpr size_t WS_HT   = WS_XT + (size_t)D * S * 2;
constexpr size_t WS_W1T  = WS_HT + (size_t)D * S * 2;
constexpr size_t WS_W2T  = WS_W1T + 8 * 11 * 16 * 1024;
constexpr size_t WS_CSP  = WS_W2T + 8 * 4 * 44 * 1024;
constexpr size_t WS_ST1  = WS_CSP + 16 * 8 * 11 * 16 * 4;
constexpr size_t WS_HF   = WS_ST1 + S * 8;
constexpr size_t WS_END  = WS_HF + (size_t)32 * 44 * 4 * 1024;

__device__ __forceinline__ float bf_lo(u32 v) { return __uint_as_float(v << 16); }
__device__ __forceinline__ float bf_hi(u32 v) { return __uint_as_float(v & 0xffff0000u); }
__device__ __forceinline__ u32 pack_bf(float a, float b) {
  f32x2 z = {a, b};
  return __builtin_bit_cast(u32, __builtin_convertvector(z, bf16x2));
}
__device__ __forceinline__ float rb(float a) { return bf_lo(pack_bf(a, 0.f)); }
__device__ __forceinline__ float dot2(u32 a, u32 b, float c) {
  return __builtin_amdgcn_fdot2_f32_bf16(__builtin_bit_cast(bf16x2, a), __builtin_bit_cast(bf16x2, b), c, false);
}
__device__ __forceinline__ u32 perm_lo(u32 a, u32 b) { return __builtin_amdgcn_perm(b, a, 0x05040100u); }
__device__ __forceinline__ u32 perm_hi(u32 a, u32 b) { return __builtin_amdgcn_perm(b, a, 0x07060302u); }
__device__ __forceinline__ f32x4 mfma(u32x4 a, u32x4 b, f32x4 c) {
  return __builtin_amdgcn_mfma_f32_16x16x32_bf16(__builtin_bit_cast(bf16x8, a), __builtin_bit_cast(bf16x8, b), c, 0, 0, 0);
}
constexpr u32 ONES = 0x3F803F80u;
template <int V> struct IntC { static constexpr int value = V; };
__device__ __forceinline__ float sum_g(float x) {
  auto s = __builtin_amdgcn_permlane16_swap(__float_as_uint(x), __float_as_uint(x), false, false);
  x = __uint_as_float(s[0]) + __uint_as_float(s[1]);
  auto t = __builtin_amdgcn_permlane32_swap(__float_as_uint(x), __float_as_uint(x), false, false);
  return __uint_as_float(t[0]) + __uint_as_float(t[1]);
}
template <int CTRL>
__device__ __forceinline__ float dpp_add(float x) {
  return x + __uint_as_float(__builtin_amdgcn_update_dpp(0u, __float_as_uint(x), CTRL, 0xf, 0xf, true));
}
__device__ __forceinline__ float wave_sum(float x) {
  x = dpp_add<0xB1>(x);
  x = dpp_add<0x4E>(x);
  x = dpp_add<0x141>(x);
  x = dpp_add<0x140>(x);
  return sum_g(x);
}
__device__ __forceinline__ __amdgpu_buffer_rsrc_t mk_rsrc(const void* p, u32 bytes) {
  return __builtin_amdgcn_make_buffer_rsrc((void*)p, 0, bytes, 0x00020000);
}
__device__ __forceinline__ u32x4 bload16(__amdgpu_buffer_rsrc_t r, u32 off) { return __builtin_amdgcn_raw_buffer_load_b128(r, off, 0, 0); }
__device__ __forceinline__ u32x2 bload8(__amdgpu_buffer_rsrc_t r, u32 off) { return __builtin_amdgcn_raw_buffer_load_b64(r, off, 0, 0); }
template <bool SC1>
__device__ __forceinline__ void glds16(__amdgpu_buffer_rsrc_t r, u32 voff, u32 soff, u32 lds_dst) {
  if (SC1)
    asm volatile("s_mov_b32 m0, %0\n\ts_nop 0\n\tbuffer_load_dwordx4 %1, %2, %3 offen sc1 lds" ::"s"(lds_dst), "v"(voff), "s"(r), "s"(soff) : "memory");
  else
    asm volatile("s_mov_b32 m0, %0\n\ts_nop 0\n\tbuffer_load_dwordx4 %1, %2, %3 offen lds" ::"s"(lds_dst), "v"(voff), "s"(r), "s"(soff) : "memory");
}
__device__ __forceinline__ void bload16_asm(u32x4& d, __amdgpu_buffer_rsrc_t r, u32 voff, u32 soff) {
  asm volatile("buffer_load_dwordx4 %0, %1, %2, %3 offen" : "=v"(d) : "v"(voff), "s"(r), "s"(soff) : "memory");
}
#define K1_AUX 0
#define WT_AUX 16
#define K2_AUX 16
template <int AUX>
__device__ __forceinline__ void gstore16(char* base, size_t off, u32x4 v) {
  const __amdgpu_buffer_rsrc_t r = mk_rsrc(base, 0x7fffffffu);
  __builtin_amdgcn_raw_buffer_store_b128(v, r, (u32)off, 0, AUX);
}
template <int AUX>
__device__ __forceinline__ void gstore8(char* base, size_t off, u32x2 v) {
  const __amdgpu_buffer_rsrc_t r = mk_rsrc(base, 0x7fffffffu);
  __builtin_amdgcn_raw_buffer_store_b64(v, r, (u32)off, 0, AUX);
}
__device__ __forceinline__ u32x4 bload16s(__amdgpu_buffer_rsrc_t r, u32 voff, u32 soff) { return __builtin_amdgcn_raw_buffer_load_b128(r, voff, soff, 0); }
__device__ __forceinline__ u32x2 tr_read(const char* p) {
  s16x4 r = __builtin_amdgcn_ds_read_tr16_b64_v4i16((LAS s16x4*)p);
  return __builtin_bit_cast(u32x2, r);
}

__device__ __forceinline__ void code_prefetch(int part, int nparts, int lane) {
  u64 pc; u32 len;
  asm volatile("s_getpc_b64 %0\n\ts_mov_b32 %1, ffn_text_end-." : "=s"(pc), "=s"(len));
  const __amdgpu_buffer_rsrc_t r = mk_rsrc((const void*)pc, len);
  u32 acc = 0;
  for (u32 o = part * 1024; o < len; o += nparts * 1024) acc |= __builtin_amdgcn_raw_buffer_load_b128(r, o + lane * 16, 0, 0)[0];
  asm volatile("" ::"v"(acc));
}
__device__ __forceinline__ int fcol(int ks, int g, int j) {
  const int slot = 8 * (g >> 1) + j;
  if (ks < 40) { const int ct = ks / 5, p = ks - 5 * ct; return 128 * (2 * p + (g & 1)) + 8 * slot + ct; }
  return 1280 + 8 * slot + 2 * (ks - 40) + (g & 1);
}

__global__ __launch_bounds__(512, 2) void k1_prep(const u16* __restrict__ X, const u16* __restrict__ K, const u16* __restrict__ V,
                                                  const u16* __restrict__ SK, const u16* __restrict__ HK, char* __restrict__ ws) {
  __shared__ __attribute__((aligned(16))) char sm[16384 + 4096];
  const int b = blockIdx.x, t = threadIdx.x, lane = t & 63;
  const int w = __builtin_amdgcn_readfirstlane(t >> 6);
  const int dt = b & 7, st = b >> 3;
  if (w < 4) {
    const int rp = t >> 3, c = t & 7;
    const int s = st * 64 + 2 * rp, ch0 = dt * 64 + c * 8;
    const __amdgpu_buffer_rsrc_t rk = mk_rsrc(K, S * D * 2);
    const u32 off = (u32)(s * D + ch0) * 2;
    u32x4 kk[5];
#pragma unroll
    for (int i = 0; i < 5; i++) kk[i] = bload16(rk, off + (1 - i) * 1024);
    const u32x4 v0 = *(const u32x4*)((const char*)V + off), v1 = *(const u32x4*)((const char*)V + off + 1024);
    const u32x4 x0 = *(const u32x4*)((const char*)X + off), x1 = *(const u32x4*)((const char*)X + off + 1024);
    u32x4 wa[4];
#pragma unroll
    for (int i = 0; i < 4; i++) wa[i] = *(const u32x4*)(SK + ch0 * 4 + i * 8);
    SCHED_FENCE();
    const int swz = ((rp >> 2) ^ c) << 4;
    char* pk = sm + (8 * c) * 128 + swz + ((3 - (rp & 3)) << 2);
    char* px = sm + 8192 + (8 * c) * 128 + swz + ((rp & 3) << 2);
#pragma unroll
    for (int i = 0; i < 4; i++) {
      const u32 l01 = perm_lo(kk[0][i], kk[1][i]), l12 = perm_lo(kk[1][i], kk[2][i]), l23 = perm_lo(kk[2][i], kk[3][i]), l34 = perm_lo(kk[3][i], kk[4][i]);
      const u32 h01 = perm_hi(kk[0][i], kk[1][i]), h12 = perm_hi(kk[1][i], kk[2][i]), h23 = perm_hi(kk[2][i], kk[3][i]), h34 = perm_hi(kk[3][i], kk[4][i]);
      const float a1 = dot2(l01, wa[i][0], dot2(l23, wa[i][1], 0.f)) * bf_lo(v1[i]);
      const float a0 = dot2(l12, wa[i][0], dot2(l34, wa[i][1], 0.f)) * bf_lo(v0[i]);
      const float b1 = dot2(h01, wa[i][2], dot2(h23, wa[i][3], 0.f)) * bf_hi(v1[i]);
      const float b0 = dot2(h12, wa[i][2], dot2(h34, wa[i][3], 0.f)) * bf_hi(v0[i]);
      *(u32*)(pk + (2 * i) * 128) = pack_bf(a1, a0);
      *(u32*)(pk + (2 * i + 1) * 128) = pack_bf(b1, b0);
      *(u32*)(px + (2 * i) * 128) = perm_lo(x0[i], x1[i]);
      *(u32*)(px + (2 * i + 1) * 128) = perm_hi(x0[i], x1[i]);
    }
    __syncthreads();
#pragma unroll
    for (int i = 0; i < 2; i++) {
      const int u = t + 256 * i, ch = u >> 3, cc = u & 7;
      const u32x4 o = *(const u32x4*)(sm + ch * 128 + cc * 16);
      const int grp = cc ^ ((ch >> 3) & 7);
      const size_t go = (size_t)(dt * 64 + ch) * S + st * 64 + grp * 8;
      gstore16<K1_AUX>(ws, WS_KVT + go * 2, o);
      gstore16<K1_AUX>(ws, WS_XT + go * 2, *(const u32x4*)(sm + 8192 + ch * 128 + cc * 16));
    }
  } else {
    const int lrow = 256 * (b & 7) + 8 * (b >> 3) + 2 * (w - 4);
    u32x4 xr[2];
#pragma unroll
    for (int i = 0; i < 2; i++) xr[i] = *(const u32x4*)(X + (size_t)(lrow + i) * D + lane * 8);
    if (w == 4 && lane < 32) __hip_atomic_store((GAS u32*)(ws + WS_FLAG) + b * 64 + lane, 0u, RLX_AGENT);
    code_prefetch((b >> 3) * 4 + (w - 4), 128, lane);
    __builtin_amdgcn_s_barrier();
    {
      const u32 dump = (u32)(size_t)(sm + 16384) + (w - 4) * 1024;
      const int chp = 64 * (b & 7) + 2 * (b >> 3);
      const __amdgpu_buffer_rsrc_t rh = mk_rsrc(HK + (size_t)chp * S, 2 * S * 2);
      glds16<false>(rh, lane * 16, (w - 4) * 2048, dump);
      glds16<false>(rh, lane * 16, (w - 4) * 2048 + 1024, dump);
    }
#pragma unroll
    for (int q = 0; q < 2; q++) {
      float sum = 0.f;
#pragma unroll
      for (int i = 0; i < 4; i++) sum = dot2(xr[q][i], ONES, sum);
      const float mean = wave_sum(sum) * (1.f / D);
      float sq = 0.f;
#pragma unroll
      for (int i = 0; i < 4; i++) { const float d0 = bf_lo(xr[q][i]) - mean, d1 = bf_hi(xr[q][i]) - mean; sq += d0 * d0 + d1 * d1; }
      sq = wave_sum(sq);
      const float sd = rb(__builtin_amdgcn_sqrtf(rb(rb(sq * (1.f / D)) + 1e-5f)));
      const float r1 = __builtin_amdgcn_rcpf(sd);
      if (lane == 0) { f32x2 o2 = {r1, -rb(mean) * r1}; *(f32x2*)(ws + WS_ST1 + (size_t)(lrow + q) * 8) = o2; }
    }
    asm volatile("s_waitcnt vmcnt(0)" ::: "memory");
  }
}

constexpr int HC_STRIDE = 4608;
constexpr int HC_CH = 8 * HC_STRIDE;
constexpr int U_CH = (240 + 2048) * 2;
constexpr int K2_H = 0, K2_U = 2 * HC_CH, K2_R = K2_U + 2 * U_CH + 32, K2_F = K2_R + 4 * 4096, K2_END = K2_F + 32;
__device__ __forceinline__ constexpr int hoff(int c) {
  return c == 0 ? 4 : c == 1 ? 2 : c == 2 ? 8 : c == 3 ? 3 : c == 4 ? 7 : c == 5 ? 0 : c == 6 ? 15 : 12;
}
__device__ __forceinline__ int hoff_rt(int c) { return (0xCF073824u >> (4 * c)) & 15; }
__device__ __forceinline__ void lds_wait(const char* p, u32 want) {
  for (u32 spins = 0; spins < (1u << 22); spins++) {
    if (__builtin_amdgcn_readfirstlane(*(volatile LAS u32*)p) == want) break;
    __builtin_amdgcn_s_sleep(1);
  }
  asm volatile("" ::: "memory");
}

__global__ __launch_bounds__(512, 2) void k2_conv(const u16* __restrict__ HK, const u16* __restrict__ DV,
                                                  const u16* __restrict__ W1, const u16* __restrict__ W2, char* __restrict__ ws) {
  __shared__ __attribute__((aligned(16))) char sm[K2_END];
  const int b = blockIdx.x, t = threadIdx.x, lane = t & 63;
  const int w = __builtin_amdgcn_readfirstlane(t >> 6);
  const int chp = 64 * (b & 7) + 2 * (b >> 3);
  const int wcl = (w >> 1) & 1, rh = w & 1;
  const int n = lane & 15, g = lane >> 4;
  {
    const int cl = t >> 8, j = t & 255;
    const __amdgpu_buffer_rsrc_t rh_ = mk_rsrc(HK + (size_t)chp * S, 2 * S * 2);
    const u32 ho = (u32)(cl * S + 8 * j) * 2;
    u32x4 ha, hb, uu;
    const __amdgpu_buffer_rsrc_t ru = mk_rsrc(ws + WS_KVT + (size_t)chp * S * 2, 2 * S * 2);
    bload16_asm(ha, rh_, ho - 16, 0);
    bload16_asm(hb, rh_, ho, 0);
    bload16_asm(uu, ru, ho, 0);
    asm volatile("s_waitcnt vmcnt(0)" : "+v"(ha), "+v"(hb), "+v"(uu)::"memory");
    if (t < 8) *(volatile LAS u32*)(sm + K2_F + t * 4) = 0u;
    SCHED_FENCE();
    if (j == 0) ha = u32x4{0, 0, 0, 0};
    char* hbase = sm + K2_H + cl * HC_CH;
    const u32 W[8] = {ha[0], ha[1], ha[2], ha[3], hb[0], hb[1], hb[2], hb[3]};
#pragma unroll
    for (int c = 0; c < 8; c++) {
      u32x4 o;
#pragma unroll
      for (int a = 0; a < 4; a++) o[a] = (c & 1) ? __builtin_amdgcn_alignbit(W[(c >> 1) + a + 1], W[(c >> 1) + a], 16) : W[(c >> 1) + a];
      *(u32x4*)(hbase + c * HC_STRIDE + 16 * (j + 1 + hoff(c))) = o;
    }
    const u32x4 z4 = {0, 0, 0, 0};
    if (j < 8) *(u32x4*)(hbase + j * HC_STRIDE + 16 * hoff_rt(j)) = z4;
    if (j == 255) *(u32x4*)(hbase + 16 * (257 + hoff(0))) = hb;
    char* ub = sm + K2_U + cl * U_CH;
    *(u32x4*)(ub + 480 + 16 * j) = uu;
    if (j < 30) *(u32x4*)(ub + 16 * j) = z4;
  }
  u32x2 xq[4];
  f32x4 sa[4], sb[4];
  u32 dvr = 0;
  if (w < 4) {
    const __amdgpu_buffer_rsrc_t rs1 = mk_rsrc(ws + WS_ST1, S * 8);
    const __amdgpu_buffer_rsrc_t rx = mk_rsrc(ws + WS_XT + (size_t)(chp + wcl) * S * 2, S * 2);
    const u32 p0 = 1024 * rh + 16 * n + 4 * g;
#pragma unroll
    for (int i = 0; i < 4; i++) {
      asm volatile("buffer_load_dwordx2 %0, %1, %2, %3 offen" : "=v"(xq[i]) : "v"(p0 * 2), "s"(rx), "s"(i * 512) : "memory");
      asm volatile("buffer_load_dwordx4 %0, %1, %2, %3 offen" : "=v"(sa[i]) : "v"(p0 * 8), "s"(rs1), "s"(i * 2048) : "memory");
      asm volatile("buffer_load_dwordx4 %0, %1, %2, %3 offen offset:16" : "=v"(sb[i]) : "v"(p0 * 8), "s"(rs1), "s"(i * 2048) : "memory");
    }
    dvr = *(const u32*)(DV + chp) >> (16 * wcl);
  }
  __syncthreads();
  if (w < 4) {
    f32x4 acc[8];
#pragma unroll
    for (int i = 0; i < 8; i++) acc[i] = f32x4{0.f, 0.f, 0.f, 0.f};
    const int idx0 = 25 + n - 8 * g;
    const int c = idx0 & 7;
    const char* fbase = sm + K2_H + wcl * HC_CH + c * HC_STRIDE + 16 * ((idx0 >> 3) + hoff_rt(c)) + 64 * 4 * rh;
    const char* gbase = sm + K2_U + wcl * U_CH + 2 * (16 * n + 8 * g + 224) - 64 * 4 * rh;
    u32x4 Ff[2][8], Gf[2][8];
#pragma unroll
    for (int a = 0; a < 8; a++) { Gf[0][a] = *(const u32x4*)(gbase + 64 * (8 * a)); Ff[0][a] = *(const u32x4*)(fbase + 64 * (8 * a)); }
    SCHED_FENCE();
#pragma unroll
    for (int rr = 0; rr < 4; rr++) {
      const int cur = rr & 1, nxt = cur ^ 1;
      int m = 0;
#pragma unroll
      for (int bb = 0; bb < 8; bb++) {
#pragma unroll
        for (int a = 0; a + bb < 8; a++) {
          acc[a + bb] = mfma(Ff[cur][a], Gf[cur][bb], acc[a + bb]);
          if (rr < 3 && (m & 1) == 1 && m < 32) {
            const int q = m >> 2;
            SCHED_FENCE();
            if (m & 2) Ff[nxt][q] = *(const u32x4*)(fbase + 64 * (8 * q + rr + 1));
            else Gf[nxt][q] = *(const u32x4*)(gbase + 64 * (8 * q - rr - 1));
            SCHED_FENCE();
          }
          m++;
        }
      }
      SCHED_FENCE();
    }
    f32x4* rbuf = (f32x4*)(sm + K2_R) + w * 256;
    if (rh) {
#pragma unroll
      for (int i = 0; i < 4; i++) rbuf[i * 64 + lane] = acc[i];
#pragma unroll
      for (int i = 0; i < 4; i++) acc[i] = acc[4 + i];
    } else {
#pragma unroll
      for (int i = 0; i < 4; i++) rbuf[i * 64 + lane] = acc[4 + i];
    }
    asm volatile("s_waitcnt lgkmcnt(0)" ::: "memory");
    if (lane == 0) *(volatile LAS u32*)(sm + K2_F + w * 4) = 1u;
    const int ch = chp + wcl;
    const char* ub = sm + K2_U + wcl * U_CH;
    u32x2 uq[4];
#pragma unroll
    for (int i = 0; i < 4; i++) uq[i] = *(const u32x2*)(ub + 2 * (240 + 256 * (4 * rh + i) + 16 * n + 8 * (g >> 1) + ((g & 1) ? 0 : 4)));
    asm volatile("s_waitcnt vmcnt(0)" : "+v"(xq[0]), "+v"(xq[1]), "+v"(xq[2]), "+v"(xq[3]), "+v"(sa[0]), "+v"(sa[1]), "+v"(sa[2]), "+v"(sa[3]),
                 "+v"(sb[0]), "+v"(sb[1]), "+v"(sb[2]), "+v"(sb[3])::"memory");
    lds_wait(sm + K2_F + (w ^ 1) * 4, 1u);
    const float dv = bf_lo(dvr);
    const f32x4* pbuf = (const f32x4*)(sm + K2_R) + (w ^ 1) * 256;
#pragma unroll
    for (int i = 0; i < 4; i++) {
      const int T = 4 * rh + i;
      const f32x4 a = acc[i] + pbuf[i * 64 + lane];
      const int pos = 256 * T + 16 * n + 4 * g;
      const float y0 = fmaf(bf_hi(uq[i][1]), dv, a[0]), y1 = fmaf(bf_lo(uq[i][1]), dv, a[1]), y2 = fmaf(bf_hi(uq[i][0]), dv, a[2]), y3 = fmaf(bf_lo(uq[i][0]), dv, a[3]);
      const float x0 = bf_lo(xq[i][0]), x1 = bf_hi(xq[i][0]), x2 = bf_lo(xq[i][1]), x3 = bf_hi(xq[i][1]);
      const float h0 = fmaf(fmaf(x0, sa[i][0], sa[i][1]), y0, x0), h1 = fmaf(fmaf(x1, sa[i][2], sa[i][3]), y1, x1);
      const float h2 = fmaf(fmaf(x2, sb[i][0], sb[i][1]), y2, x2), h3 = fmaf(fmaf(x3, sb[i][2], sb[i][3]), y3, x3);
      const u32x2 o = {pack_bf(h0, h1), pack_bf(h2, h3)};
      gstore8<K2_AUX>(ws, WS_HT + ((size_t)ch * S + pos) * 2, o);
    }
  } else {
    const int unit = b + 256 * (w - 4);
    const int lc = lane & 15, lg = lane >> 4;
    if (unit < 176) {
      const __amdgpu_buffer_rsrc_t rw = mk_rsrc(W1, D * F * 2);
      const int blk = unit >> 4, ks = unit & 15;
      const int col = 128 * blk + 8 * lc;
      const u32 o0 = col < F ? (u32)((32 * ks + 8 * lg) * F + col) * 2 : 0x80000000u;
      u32x4 wl[8];
#pragma unroll
      for (int j = 0; j < 8; j++) wl[j] = bload16(rw, o0 + j * F * 2);
#pragma unroll
      for (int i = 0; i < 8; i++) {
        u32x4 o;
#pragma unroll
        for (int a = 0; a < 4; a++) o[a] = (i & 1) ? perm_hi(wl[2 * a][i >> 1], wl[2 * a + 1][i >> 1]) : perm_lo(wl[2 * a][i >> 1], wl[2 * a + 1][i >> 1]);
        gstore16<WT_AUX>(ws, WS_W1T + ((size_t)((i * 11 + blk) * 16 + ks) * 64 + lane) * 16, o);
        const float cs = sum_g(dot2(o[0], ONES, dot2(o[1], ONES, dot2(o[2], ONES, dot2(o[3], ONES, 0.f)))));
        if (lane < 16) *(float*)(ws + WS_CSP + ((size_t)(i * 176 + blk * 16 + lane) * 16 + ks) * 4) = cs;
      }
    } else if (unit < 528) {
      const __amdgpu_buffer_rsrc_t rw = mk_rsrc(W2, D * F * 2);
      const int u2 = unit - 176, ks = u2 >> 3, c2 = u2 & 7;
      u32 wx[8], wy[8];
#pragma unroll
      for (int j = 0; j < 8; j++) {
        const u32 o = (u32)(fcol(ks, lg, j) * D + 64 * c2 + 8 * (lc >> 2) + 2 * (lc & 3)) * 2;
        wx[j] = __builtin_amdgcn_raw_buffer_load_b32(rw, o, 0, 0);
        wy[j] = __builtin_amdgcn_raw_buffer_load_b32(rw, o + 64, 0, 0);
      }
#pragma unroll
      for (int i = 0; i < 4; i++) {
        u32x4 o;
#pragma unroll
        for (int a = 0; a < 4; a++) {
          const u32 e0 = (i >> 1) ? wy[2 * a] : wx[2 * a], e1 = (i >> 1) ? wy[2 * a + 1] : wx[2 * a + 1];
          o[a] = (i & 1) ? perm_hi(e0, e1) : perm_lo(e0, e1);
        }
        gstore16<WT_AUX>(ws, WS_W2T + ((size_t)(((c2 * 2 + (i >> 1)) * 44 + ks) * 2 + (i & 1)) * 64 + lane) * 16, o);
      }
    }
  }
}

constexpr int L_HT = 0;
constexpr int L_ST = 147456;
constexpr int L_CS = L_ST + 512;
constexpr int L_RES = L_CS + 768;
constexpr int L_END = L_RES + 8192;
constexpr int L_RED = 0;
constexpr int L_FLG = L_END;
constexpr int L_TOT = L_END + 16;
#define W1_PRE 4
#define W2_LOADS                                                                                                                   \
  "buffer_load_dwordx4 %0, %14, %15, %16 offen\n\tbuffer_load_dwordx4 %1, %14, %15, %16 offen offset:1024\n\t"                      \
  "buffer_load_dwordx4 %2, %14, %15, %16 offen offset:2048\n\tbuffer_load_dwordx4 %3, %14, %15, %16 offen offset:3072\n\t"          \
  "buffer_load_dwordx4 %4, %14, %15, %17 offen\n\tbuffer_load_dwordx4 %5, %14, %15, %17 offen offset:1024\n\t"                      \
  "buffer_load_dwordx4 %6, %14, %15, %17 offen offset:2048\n\tbuffer_load_dwordx4 %7, %14, %15, %17 offen offset:3072\n\t"          \
  "buffer_load_dwordx4 %8, %14, %15, %18 offen\n\tbuffer_load_dwordx4 %9, %14, %15, %18 offen offset:1024\n\t"                      \
  "buffer_load_dwordx4 %10, %14, %15, %18 offen offset:2048\n\tbuffer_load_dwordx4 %11, %14, %15, %18 offen offset:3072\n\t"        \
  "buffer_load_dwordx4 %12, %14, %15, %19 offen\n\tbuffer_load_dwordx4 %13, %14, %15, %19 offen offset:1024\n"
__device__ __forceinline__ void load_w2_tail_support(u32x4 (&w2)[22], __amdgpu_buffer_rsrc_t r, u32 voff, int w) {
  asm volatile("s_cmp_lt_u32 %20, 4\n\ts_cbranch_scc1 1f\n\t" W2_LOADS "1:"
               : "=&v"(w2[8]), "=&v"(w2[9]), "=&v"(w2[10]), "=&v"(w2[11]), "=&v"(w2[12]), "=&v"(w2[13]), "=&v"(w2[14]), "=&v"(w2[15]), "=&v"(w2[16]),
                 "=&v"(w2[17]), "=&v"(w2[18]), "=&v"(w2[19]), "=&v"(w2[20]), "=&v"(w2[21])
               : "v"(voff), "s"(r), "s"(8192), "s"(12288), "s"(16384), "s"(20480), "s"(w) : "memory", "scc");
}
__device__ __forceinline__ void load_w2_tail_main(u32x4 (&w2)[22], __amdgpu_buffer_rsrc_t r, u32 voff, int w) {
  asm volatile("s_cmp_lt_u32 %20, 4\n\ts_cbranch_scc0 1f\n\t" W2_LOADS "1:"
               : "+v"(w2[8]), "+v"(w2[9]), "+v"(w2[10]), "+v"(w2[11]), "+v"(w2[12]), "+v"(w2[13]), "+v"(w2[14]), "+v"(w2[15]), "+v"(w2[16]),
                 "+v"(w2[17]), "+v"(w2[18]), "+v"(w2[19]), "+v"(w2[20]), "+v"(w2[21])
               : "v"(voff), "s"(r), "s"(8192), "s"(12288), "s"(16384), "s"(20480), "s"(w) : "memory", "scc");
}
__device__ __forceinline__ float silu_val(float acc, float rs, float bias) {
  const float tt = fmaf(acc, rs, bias);
  const float e = __builtin_amdgcn_exp2f(tt);
  return tt * __builtin_amdgcn_rcpf(fmaf(e, -1.44269504089f, -1.44269504089f));
}

__global__ __launch_bounds__(512, 2) void k34_ffn(char* __restrict__ ws, u16* __restrict__ OUT) {
  __shared__ __attribute__((aligned(16))) char sm[L_TOT];
  const int b = blockIdx.x, t = threadIdx.x, lane = t & 63;
  const int w = __builtin_amdgcn_readfirstlane(t >> 6);
  const int rt = 8 * (b & 3) + ((b >> 3) & 7), c = 4 * ((b >> 2) & 1) + (b >> 6);
  const int n = lane & 15, g = lane >> 4;
  const int row0 = 64 * rt;
  {
    const __amdgpu_buffer_rsrc_t rh = mk_rsrc(ws + WS_HT + ((size_t)(64 * w) * S + row0) * 2, 64 * S * 2);
    const int f0 = 2 * ((lane >> 4) & 1);
    const u32 v0 = (u32)(lane >> 3) * S * 2 + (((lane & 7) ^ f0) << 4), v1 = (u32)(lane >> 3) * S * 2 + (((lane & 7) ^ (f0 + 4)) << 4);
#pragma unroll
    for (int i = 0; i < 8; i++) glds16<false>(rh, (i & 1) ? v1 : v0, i * 8 * S * 2, (u32)(size_t)(sm + L_HT) + (64 * w + 8 * i) * 128);
  }
  if (t < 4) *(volatile LAS u32*)(sm + L_FLG + t * 4) = 0u;
  __builtin_amdgcn_s_barrier();
  const __amdgpu_buffer_rsrc_t rw2 = mk_rsrc(ws + WS_W2T + (size_t)((c * 2 + (w & 1)) * 44 + 11 * (w >> 1)) * 2048, 22 * 1024);
  const int q4 = (lane & 15) >> 2, p4 = lane & 3;
  const int fsw = 2 * ((q4 >> 1) & 1) + 4 * (g & 1);
  auto save_residual = [&]() {
    const char* p = sm + L_HT + (8 * g + q4) * 128 + (((2 * (w >> 1) + (p4 >> 1)) ^ fsw) << 4) + 8 * (p4 & 1) + (2 * c + (w & 1)) * 4096;
    const u32x2 a = tr_read(p), bq = tr_read(p + 512);
    *(u32x4*)(sm + L_RES + (w * 64 + lane) * 16) = u32x4{a[0], a[1], bq[0], bq[1]};
  };
  if (w >= 4) {
    f32x4 csp[4];
    const __amdgpu_buffer_rsrc_t rc = mk_rsrc(ws + WS_CSP + (size_t)c * 176 * 64, 176 * 64);
    const int cj = (w - 4) * 64 + lane;
#pragma unroll
    for (int i = 0; i < 4; i++) csp[i] = __builtin_bit_cast(f32x4, bload16s(rc, (u32)(cj < 176 ? cj : 175) * 64, i * 16));
    asm volatile("s_waitcnt vmcnt(4) lgkmcnt(0)" ::: "memory");
    __builtin_amdgcn_s_barrier();
    __builtin_amdgcn_s_setprio(3);
    const char* p = sm + L_HT + (8 * g + q4) * 128 + (((2 * (w - 4) + (p4 >> 1)) ^ fsw) << 4) + 8 * (p4 & 1);
    u32x2 fa[16][2];
#pragma unroll
    for (int ks = 0; ks < 16; ks++) { fa[ks][0] = tr_read(p + ks * 4096); fa[ks][1] = tr_read(p + ks * 4096 + 512); }
    f32x4 q0 = {0.f, 0.f, 0.f, 0.f}, q1 = {0.f, 0.f, 0.f, 0.f};
    const u32x4 ones = {ONES, ONES, ONES, ONES};
#pragma unroll
    for (int ks = 0; ks < 16; ks++) {
      const u32x4 f0 = {fa[ks][0][0], fa[ks][0][1], fa[ks][1][0], fa[ks][1][1]};
      q0 = mfma(f0, f0, q0);
      q1 = mfma(ones, f0, q1);
    }
    {
      const float lo = (n & 1) ? q0[1] : q0[0], hi = (n & 1) ? q0[3] : q0[2];
      const float q = (n & 2) ? hi : lo;
      const float mu = q1[0] * (1.f / D);
      const float var = fmaxf(q * (1.f / D) - mu * mu, 0.f);
      const float rstd = __builtin_amdgcn_rcpf(rb(__builtin_amdgcn_sqrtf(rb(rb(var) + 1e-5f))));
      if (g == (n >> 2)) *(f32x2*)(sm + L_ST + ((w - 4) * 16 + n) * 8) = f32x2{-1.44269504089f * rstd, rb(mu) * rstd * 1.44269504089f};
    }
    {
      const f32x4 s4 = (csp[0] + csp[1]) + (csp[2] + csp[3]);
      if (cj < 192) *(float*)(sm + L_CS + cj * 4) = (s4[0] + s4[1]) + (s4[2] + s4[3]);
    }
    save_residual();
    asm volatile("s_waitcnt lgkmcnt(0)" ::: "memory");
    if (lane == 0) __hip_atomic_fetch_add((LAS u32*)(sm + L_FLG + 4), 1u, __ATOMIC_RELAXED, __HIP_MEMORY_SCOPE_WORKGROUP);
    __builtin_amdgcn_s_setprio(0);
    lds_wait(sm + L_FLG + 8, 1u);
  } else {
    constexpr int NT = 3, NW1 = 48, PRE = W1_PRE * NT;
    u32x4 w1[NW1];
    const __amdgpu_buffer_rsrc_t rw1 = mk_rsrc(ws + WS_W1T + (size_t)(c * 11 + 3 * w) * 16 * 1024, 48 * 1024);
    auto w1load = [&](int k) { w1[k] = bload16s(rw1, lane * 16, (k % NT) * 16384 + (k / NT) * 1024); };
    auto issued = [&](int ks) constexpr { const int v = PRE + NT * (ks + 1); return v < NW1 ? v : NW1; };
#pragma unroll
    for (int i = 0; i < PRE; i++) w1load(i);
    asm volatile("s_waitcnt vmcnt(%0) lgkmcnt(0)" ::"n"(PRE) : "memory");
    __builtin_amdgcn_s_barrier();
    const char* tb[4];
#pragma unroll
    for (int m = 0; m < 4; m++) tb[m] = sm + L_HT + (8 * g + q4) * 128 + (((2 * m + (p4 >> 1)) ^ fsw) << 4) + 8 * (p4 & 1);
    f32x4 acc[NT][4];
    u32x4 hb[3][4];
#pragma unroll
    for (int i = 0; i < NT; i++)
#pragma unroll
      for (int m = 0; m < 4; m++) acc[i][m] = f32x4{0.f, 0.f, 0.f, 0.f};
#pragma unroll
    for (int d = 0; d < 2; d++)
#pragma unroll
      for (int m = 0; m < 4; m++) {
        const u32x2 a = tr_read(tb[m] + d * 4096), bq = tr_read(tb[m] + d * 4096 + 512);
        hb[d][m] = u32x4{a[0], a[1], bq[0], bq[1]};
      }
#pragma unroll
    for (int ks = 0; ks < 16; ks++) {
#pragma unroll
      for (int m = 0; m < 4; m++) {
        acc[0][m] = mfma(w1[ks * NT], hb[ks % 3][m], acc[0][m]);
        SCHED_FENCE();
        if (ks + 2 < 16) { const u32x2 a = tr_read(tb[m] + (ks + 2) * 4096); hb[(ks + 2) % 3][m][0] = a[0]; hb[(ks + 2) % 3][m][1] = a[1]; }
        SCHED_FENCE();
        acc[1][m] = mfma(w1[ks * NT + 1], hb[ks % 3][m], acc[1][m]);
        SCHED_FENCE();
        if (ks + 2 < 16) { const u32x2 a = tr_read(tb[m] + (ks + 2) * 4096 + 512); hb[(ks + 2) % 3][m][2] = a[0]; hb[(ks + 2) % 3][m][3] = a[1]; }
        SCHED_FENCE();
        acc[2][m] = mfma(w1[ks * NT + 2], hb[ks % 3][m], acc[2][m]);
        SCHED_FENCE();
        if (m < NT && issued(ks - 1) + m < issued(ks)) w1load(issued(ks - 1) + m);
        SCHED_FENCE();
      }
      if (ks == 15 - W1_PRE && w == 0 && lane == 0) *(volatile LAS u32*)(sm + L_FLG + 8) = 1u;
    }
    save_residual();
    lds_wait(sm + L_FLG + 4, 4u);
    float rs[4], mr[4];
    f32x4 cs[NT];
#pragma unroll
    for (int m = 0; m < 4; m++) { const f32x2 v = *(const f32x2*)(sm + L_ST + (m * 16 + n) * 8); rs[m] = v[0]; mr[m] = v[1]; }
#pragma unroll
    for (int i = 0; i < NT; i++) cs[i] = *(const f32x4*)(sm + L_CS + ((3 * w + i) * 16 + 4 * g) * 4);
    const __amdgpu_buffer_rsrc_t hrs = mk_rsrc(ws + WS_HF + (size_t)rt * 44 * 4096, 44 * 4096);
    u32 o_t[NT];
#pragma unroll
    for (int i = 0; i < NT; i++) {
      const int tl = 3 * w + i;
      const int ks2 = tl < 10 ? 5 * c + (tl >> 1) : 40 + (c >> 1), par = tl < 10 ? (tl & 1) : (c & 1);
      o_t[i] = (tl < 11 && (g & 1) == par) ? (u32)(ks2 * 4096 + lane * 16) : 0x40000000u;
    }
#pragma unroll
    for (int m = 0; m < 4; m++) {
#pragma unroll
      for (int i = 0; i < NT; i++) {
        float h1[4];
#pragma unroll
        for (int r = 0; r < 4; r++) h1[r] = silu_val(acc[i][m][r], rs[m], mr[m] * cs[i][r]);
        const u32 p0 = pack_bf(h1[0], h1[1]), p1 = pack_bf(h1[2], h1[3]);
        const auto s0 = __builtin_amdgcn_permlane16_swap(p0, p0, false, false), s1 = __builtin_amdgcn_permlane16_swap(p1, p1, false, false);
        __builtin_amdgcn_raw_buffer_store_b128(u32x4{s0[0], s1[0], s0[1], s1[1]}, hrs, o_t[i] + m * 1024, 0, 16);
      }
      SCHED_FENCE();
    }
  }
  u32x4 w2[22];
#pragma unroll
  for (int i = 0; i < 8; i++) bload16_asm(w2[i], rw2, lane * 16, i * 1024);
  load_w2_tail_support(w2, rw2, lane * 16, w);
  asm volatile("s_cmp_lt_u32 %0, 4\n\ts_cbranch_scc0 1f\n\ts_waitcnt vmcnt(0)\n1:\n\ts_waitcnt lgkmcnt(0)" ::"s"(w) : "memory", "scc");
  __builtin_amdgcn_s_barrier();
  if (w == 7) {
    GAS u32* flags = (GAS u32*)(ws + WS_FLAG);
    if (lane < 32) __hip_atomic_store(flags + b * 64 + lane, 1u, RLX_AGENT);
    const int pb = (b & 3) + (b & 56) + 4 * ((lane >> 2) & 1) + 64 * (lane & 3);
    for (u32 spins = 0; spins < (1u << 20); spins++) {
      const u32 v = __hip_atomic_load(flags + pb * 64, RLX_AGENT);
      if (__all(v == 1u)) break;
      __builtin_amdgcn_s_sleep(1);
    }
  }
  if (w < 4) __builtin_amdgcn_s_sleep(2);
  load_w2_tail_main(w2, rw2, lane * 16, w);
  __builtin_amdgcn_s_barrier();

  const int dh = w & 1, kq = w >> 1;
  const __amdgpu_buffer_rsrc_t rhf = mk_rsrc(ws + WS_HF + (size_t)rt * 44 * 4096 + (size_t)(11 * kq * 4 + 2 * dh) * 1024, 11 * 4096);
  const u32 ldst = (u32)(size_t)sm + (kq * 4 + 2 * dh) * 1024;
  auto stage1 = [&](int s, int e) {
    const int slot = s < 9 ? s : s - 9;
    glds16<true>(rhf, lane * 16, s * 4096 + e * 1024, ldst + slot * 16384 + e * 1024);
  };
  auto stage = [&](int s) { stage1(s, 0); stage1(s, 1); };
  stage(0); stage(1); stage(2); stage(3);
  f32x4 acc2[2][4];
#pragma unroll
  for (int i = 0; i < 2; i++)
#pragma unroll
    for (int m = 0; m < 4; m++) acc2[i][m] = f32x4{0.f, 0.f, 0.f, 0.f};
  asm volatile("s_waitcnt vmcnt(4)"
               : "+v"(w2[0]), "+v"(w2[1]), "+v"(w2[2]), "+v"(w2[3]), "+v"(w2[4]), "+v"(w2[5]), "+v"(w2[6]), "+v"(w2[7]), "+v"(w2[8]), "+v"(w2[9]), "+v"(w2[10]),
                 "+v"(w2[11]), "+v"(w2[12]), "+v"(w2[13]), "+v"(w2[14]), "+v"(w2[15]), "+v"(w2[16]), "+v"(w2[17]), "+v"(w2[18]), "+v"(w2[19]), "+v"(w2[20]), "+v"(w2[21])
               ::"memory");
  __builtin_amdgcn_s_barrier();
  {
    const char* base = sm + kq * 4096 + lane * 16;
    u32x4 hb[2][4];
#pragma unroll
    for (int m = 0; m < 4; m++) hb[0][m] = *(const u32x4*)(base + m * 1024);
#pragma unroll
    for (int s = 0; s < 11; s++) {
      if (s == 1) { asm volatile("s_waitcnt vmcnt(4)" ::: "memory"); __builtin_amdgcn_s_barrier(); }
      else if (s == 3 || s == 5) { asm volatile("s_waitcnt vmcnt(6)" ::: "memory"); __builtin_amdgcn_s_barrier(); }
      else if (s == 7) { asm volatile("s_waitcnt vmcnt(2)" ::: "memory"); __builtin_amdgcn_s_barrier(); }
      else if (s == 9) { asm volatile("s_waitcnt vmcnt(0)" ::: "memory"); __builtin_amdgcn_s_barrier(); }
      const int slot = s + 1 < 9 ? s + 1 : s + 1 - 9;
#pragma unroll
      for (int m = 0; m < 4; m++) {
        acc2[0][m] = mfma(w2[2 * s], hb[s & 1][m], acc2[0][m]);
        SCHED_FENCE();
        if (s < 10) hb[(s + 1) & 1][m] = *(const u32x4*)(base + slot * 16384 + m * 1024);
        SCHED_FENCE();
        acc2[1][m] = mfma(w2[2 * s + 1], hb[s & 1][m], acc2[1][m]);
        SCHED_FENCE();
        if (s < 2) stage1(4 + 2 * s + (m >> 1), m & 1);
        else if (s < 5 && (m & 1)) stage1(s + 6, m >> 1);
        SCHED_FENCE();
      }
    }
  }
  asm volatile("s_waitcnt lgkmcnt(0)" ::: "memory");
  __builtin_amdgcn_s_barrier();
  {
    char* red = sm + L_RED + dh * 32768;
    f32x4 o0, o1;
    auto scatter = [&](auto KQ) {
      constexpr int k = decltype(KQ)::value;
#pragma unroll
      for (int m = 0; m < 4; m++) {
        if (m != k) {
#pragma unroll
          for (int i = 0; i < 2; i++) *(f32x4*)(red + (((k * 4 + m) * 2 + i) * 64 + lane) * 16) = acc2[i][m];
        }
      }
      o0 = acc2[0][k]; o1 = acc2[1][k];
      asm volatile("" : "+v"(o0), "+v"(o1));
    };
    if (kq == 0) scatter(IntC<0>{}); else if (kq == 1) scatter(IntC<1>{}); else if (kq == 2) scatter(IntC<2>{}); else scatter(IntC<3>{});
    __syncthreads();
#pragma unroll
    for (int j = 1; j < 4; j++) {
      const int src = (kq + j) & 3;
      o0 += *(const f32x4*)(red + (((src * 4 + kq) * 2 + 0) * 64 + lane) * 16);
      o1 += *(const f32x4*)(red + (((src * 4 + kq) * 2 + 1) * 64 + lane) * 16);
    }
    const u32x4 hres = *(const u32x4*)(sm + L_RES + (w * 64 + lane) * 16);
    u32x4 res;
#pragma unroll
    for (int r = 0; r < 4; r++) res[r] = pack_bf(bf_lo(hres[r]) + rb(o0[r]), bf_hi(hres[r]) + rb(o1[r]));
    __builtin_amdgcn_raw_buffer_store_b128(res, mk_rsrc(OUT, S * D * 2), (u32)((row0 + 16 * kq + n) * D + 64 * c + 32 * dh + 8 * g) * 2, 0, 2);
  }
  asm volatile("ffn_text_end:");
}

extern "C" void kernel_launch(void* const* d_in, const int* in_sizes, int n_in, void* d_out, int out_size, void* d_ws,
                              size_t ws_size, hipStream_t stream) {
  const u16* X = (const u16*)d_in[0];
  const u16* K = (const u16*)d_in[1];
  const u16* V = (const u16*)d_in[2];
  const u16* SK = (const u16*)d_in[3];
  const u16* HK = (const u16*)d_in[4];
  const u16* DV = (const u16*)d_in[5];
  const u16* W1 = (const u16*)d_in[7];
  const u16* W2 = (const u16*)d_in[8];
  char* ws = (char*)d_ws;
  k1_prep<<<256, 512, 0, stream>>>(X, K, V, SK, HK, ws);
  k2_conv<<<256, 512, 0, stream>>>(HK, DV, W1, W2, ws);
  k34_ffn<<<256, 512, 0, stream>>>(ws, (u16*)d_out);
}
```

```cpp
#include <hip/hip_runtime.h>
#include <stdint.h>

typedef unsigned short u16;
typedef unsigned int u32;
typedef unsigned long long u64;
typedef __bf16 bf16x8 __attribute__((ext_vector_type(8)));
typedef __bf16 bf16x2 __attribute__((ext_vector_type(2)));
typedef short s16x4 __attribute__((ext_vector_type(4)));
typedef float f32x4 __attribute__((ext_vector_type(4)));
typedef float f32x2 __attribute__((ext_vector_type(2)));
typedef u32 u32x4 __attribute__((ext_vector_type(4)));
typedef u32 u32x2 __attribute__((ext_vector_type(2)));
#define GAS __attribute__((address_space(1)))
#define LAS __attribute__((address_space(3)))
#define RLX_AGENT __ATOMIC_RELAXED, __HIP_MEMORY_SCOPE_AGENT
#define SCHED_FENCE() __builtin_amdgcn_sched_barrier(0)

constexpr int S = 2048, D = 512, F = 1376;

constexpr size_t WS_EP   = 0;
constexpr size_t WS_F2   = 4096;
constexpr size_t WS_F3   = WS_F2 + 32768;
constexpr size_t WS_FW   = WS_F3 + 32768;
constexpr size_t WS_KVT  = 1 << 17;
constexpr size_t WS_YT   = WS_KVT + (size_t)D * S * 4;
constexpr size_t WS_W1T  = WS_YT + (size_t)D * S * 2;
constexpr size_t WS_W2T  = WS_W1T + 8 * 11 * 16 * 1024;
constexpr size_t WS_CSP  = WS_W2T + 8 * 4 * 44 * 1024;
constexpr size_t WS_HF   = WS_CSP + 16 * 8 * 11 * 16 * 4;
constexpr size_t WS_END  = WS_HF + (size_t)32 * 44 * 4 * 1024;

__device__ __forceinline__ float bf_lo(u32 v) { return __uint_as_float(v << 16); }
__device__ __forceinline__ float bf_hi(u32 v) { return __uint_as_float(v & 0xffff0000u); }
__device__ __forceinline__ u32 pack_bf(float a, float b) {
  f32x2 z = {a, b};
  return __builtin_bit_cast(u32, __builtin_convertvector(z, bf16x2));
}
__device__ __forceinline__ float rb(float a) { return bf_lo(pack_bf(a, 0.f)); }
__device__ __forceinline__ float dot2(u32 a, u32 b, float c) {
  return __builtin_amdgcn_fdot2_f32_bf16(__builtin_bit_cast(bf16x2, a), __builtin_bit_cast(bf16x2, b), c, false);
}
__device__ __forceinline__ u32 perm_lo(u32 a, u32 b) { return __builtin_amdgcn_perm(b, a, 0x05040100u); }
__device__ __forceinline__ u32 perm_hi(u32 a, u32 b) { return __builtin_amdgcn_perm(b, a, 0x07060302u); }
__device__ __forceinline__ f32x4 mfma(u32x4 a, u32x4 b, f32x4 c) {
  return __builtin_amdgcn_mfma_f32_16x16x32_bf16(__builtin_bit_cast(bf16x8, a), __builtin_bit_cast(bf16x8, b), c, 0, 0, 0);
}
constexpr u32 ONES = 0x3F803F80u;
template <int V> struct IntC { static constexpr int value = V; };
__device__ __forceinline__ float sum_g(float x) {
  auto s = __builtin_amdgcn_permlane16_swap(__float_as_uint(x), __float_as_uint(x), false, false);
  x = __uint_as_float(s[0]) + __uint_as_float(s[1]);
  auto t = __builtin_amdgcn_permlane32_swap(__float_as_uint(x), __float_as_uint(x), false, false);
  return __uint_as_float(t[0]) + __uint_as_float(t[1]);
}
template <int CTRL>
__device__ __forceinline__ float dpp_add(float x) {
  return x + __uint_as_float(__builtin_amdgcn_update_dpp(0u, __float_as_uint(x), CTRL, 0xf, 0xf, true));
}
__device__ __forceinline__ float wave_sum(float x) {
  x = dpp_add<0xB1>(x);
  x = dpp_add<0x4E>(x);
  x = dpp_add<0x141>(x);
  x = dpp_add<0x140>(x);
  return sum_g(x);
}
__device__ __forceinline__ __amdgpu_buffer_rsrc_t mk_rsrc(const void* p, u32 bytes) {
  return __builtin_amdgcn_make_buffer_rsrc((void*)p, 0, bytes, 0x00020000);
}
__device__ __forceinline__ u32x4 bload16(__amdgpu_buffer_rsrc_t r, u32 off) { return __builtin_amdgcn_raw_buffer_load_b128(r, off, 0, 0); }
__device__ __forceinline__ u32x2 bload8(__amdgpu_buffer_rsrc_t r, u32 off) { return __builtin_amdgcn_raw_buffer_load_b64(r, off, 0, 0); }
template <bool SC1>
__device__ __forceinline__ void glds16(__amdgpu_buffer_rsrc_t r, u32 voff, u32 soff, u32 lds_dst) {
  if (SC1)
    asm volatile("s_mov_b32 m0, %0\n\ts_nop 0\n\tbuffer_load_dwordx4 %1, %2, %3 offen sc1 lds" ::"s"(lds_dst), "v"(voff), "s"(r), "s"(soff) : "memory");
  else
    asm volatile("s_mov_b32 m0, %0\n\ts_nop 0\n\tbuffer_load_dwordx4 %1, %2, %3 offen lds" ::"s"(lds_dst), "v"(voff), "s"(r), "s"(soff) : "memory");
}
__device__ __forceinline__ void bload16_asm(u32x4& d, __amdgpu_buffer_rsrc_t r, u32 voff, u32 soff) {
  asm volatile("buffer_load_dwordx4 %0, %1, %2, %3 offen" : "=v"(d) : "v"(voff), "s"(r), "s"(soff) : "memory");
}
__device__ __forceinline__ void bload16_asm_sc1(u32x4& d, __amdgpu_buffer_rsrc_t r, u32 voff, u32 soff) {
  asm volatile("buffer_load_dwordx4 %0, %1, %2, %3 offen sc1" : "=v"(d) : "v"(voff), "s"(r), "s"(soff) : "memory");
}
template <int AUX>
__device__ __forceinline__ void gstore16(char* base, size_t off, u32x4 v) {
  const __amdgpu_buffer_rsrc_t r = mk_rsrc(base, 0x7fffffffu);
  __builtin_amdgcn_raw_buffer_store_b128(v, r, (u32)off, 0, AUX);
}
template <int AUX>
__device__ __forceinline__ void gstore8(char* base, size_t off, u32x2 v) {
  const __amdgpu_buffer_rsrc_t r = mk_rsrc(base, 0x7fffffffu);
  __builtin_amdgcn_raw_buffer_store_b64(v, r, (u32)off, 0, AUX);
}
__device__ __forceinline__ u32x4 bload16s(__amdgpu_buffer_rsrc_t r, u32 voff, u32 soff) { return __builtin_amdgcn_raw_buffer_load_b128(r, voff, soff, 16); }
__device__ __forceinline__ u32x2 tr_read(const char* p) {
  s16x4 r = __builtin_amdgcn_ds_read_tr16_b64_v4i16((LAS s16x4*)p);
  return __builtin_bit_cast(u32x2, r);
}

__device__ __forceinline__ void code_prefetch(int part, int nparts, int lane, u32 lds_dump) {
  u64 pc; u32 len;
  asm volatile("s_getpc_b64 %0\n\ts_mov_b32 %1, ffn_text_end-." : "=s"(pc), "=s"(len));
  const __amdgpu_buffer_rsrc_t r = mk_rsrc((const void*)pc, len);
  if ((u32)part * 1024 < len) glds16<false>(r, lane * 16, part * 1024, lds_dump);
}
__device__ __forceinline__ int fcol(int ks, int g, int j) {
  const int slot = 8 * (g >> 1) + j;
  if (ks < 40) { const int ct = ks / 5, p = ks - 5 * ct; return 128 * (2 * p + (g & 1)) + 8 * slot + ct; }
  return 1280 + 8 * slot + 2 * (ks - 40) + (g & 1);
}

constexpr int HC_STRIDE = 4608;
constexpr int HC_CH = 8 * HC_STRIDE;
constexpr int U_CH = (240 + 2048) * 2;
constexpr int P_H = 0, P_U = 2 * HC_CH, P_R = P_U + 2 * U_CH + 32;
constexpr int P_T = P_R + 4 * 4096;
constexpr int P_END = P_T + 8192;
constexpr int L_H = 0;
constexpr int L_Y = 65536;
constexpr int L_X1 = 131072;
constexpr int L_ST = 147456;
constexpr int L_CS = L_ST + 512;
constexpr int L_RES = L_CS + 768;
constexpr int L_RED = 0;
constexpr int L_FLG = L_RES + 8192;
constexpr int L_TOT = L_FLG + 128;
enum { W_CA = 12, W_CB = 16, W_HRDY = 24, W_CD = 28, W_PART = 32, W_WDONE = 56, W_CE = 60, W_FWOK = 20, W_LN2 = 64, W_W1S = 68 };
static_assert(P_END <= L_X1 && L_TOT <= 163840, "LDS map");
#define KV_SLEEP 8
#define KV_FIRST 32
#define W1_PRE 6
__device__ __forceinline__ constexpr int hoff(int c) {
  return c == 0 ? 4 : c == 1 ? 2 : c == 2 ? 8 : c == 3 ? 3 : c == 4 ? 7 : c == 5 ? 0 : c == 6 ? 15 : 12;
}
__device__ __forceinline__ int hoff_rt(int c) { return (0xCF073824u >> (4 * c)) & 15; }
__device__ __forceinline__ void lds_wait(const char* p, u32 want) {
  for (u32 spins = 0; spins < (1u << 22); spins++) {
    if (__builtin_amdgcn_readfirstlane(*(volatile LAS u32*)p) == want) break;
    __builtin_amdgcn_s_sleep(1);
  }
  asm volatile("" ::: "memory");
}
__device__ __forceinline__ void lds_arrive(char* p, int lane) {
  asm volatile("s_waitcnt lgkmcnt(0)" ::: "memory");
  if (lane == 0) __hip_atomic_fetch_add((LAS u32*)p, 1u, __ATOMIC_RELAXED, __HIP_MEMORY_SCOPE_WORKGROUP);
}
__device__ __forceinline__ void lds_set(char* p, u32 v, int lane) {
  asm volatile("s_waitcnt lgkmcnt(0)" ::: "memory");
  if (lane == 0) *(volatile LAS u32*)p = v;
}
__device__ __forceinline__ void flag_store(char* base, int idx, u64 ep, int lane) {
  if (lane < 16) __hip_atomic_store((GAS u64*)(base + (size_t)idx * 128 + lane * 8), ep, RLX_AGENT);
}
template <int N>
__device__ __forceinline__ void poll_flags(const char* base, int first, int stride, u64 ep) {
  for (u32 spins = 0; spins < (1u << 20); spins++) {
    bool ok = true;
#pragma unroll
    for (int i = 0; i < N; i++) ok &= __hip_atomic_load((GAS u64*)(base + (size_t)(first + i * stride) * 128), RLX_AGENT) == ep;
    if (__all(ok)) break;
    __builtin_amdgcn_s_sleep(1);
  }
  asm volatile("" ::: "memory");
}

#define W2_LOADS                                                                                                                   \
  "buffer_load_dwordx4 %0, %14, %15, %16 offen sc1\n\tbuffer_load_dwordx4 %1, %14, %15, %16 offen offset:1024 sc1\n\t"                      \
  "buffer_load_dwordx4 %2, %14, %15, %16 offen offset:2048 sc1\n\tbuffer_load_dwordx4 %3, %14, %15, %16 offen offset:3072 sc1\n\t"          \
  "buffer_load_dwordx4 %4, %14, %15, %17 offen sc1\n\tbuffer_load_dwordx4 %5, %14, %15, %17 offen offset:1024 sc1\n\t"                      \
  "buffer_load_dwordx4 %6, %14, %15, %17 offen offset:2048 sc1\n\tbuffer_load_dwordx4 %7, %14, %15, %17 offen offset:3072 sc1\n\t"          \
  "buffer_load_dwordx4 %8, %14, %15, %18 offen sc1\n\tbuffer_load_dwordx4 %9, %14, %15, %18 offen offset:1024 sc1\n\t"                      \
  "buffer_load_dwordx4 %10, %14, %15, %18 offen offset:2048 sc1\n\tbuffer_load_dwordx4 %11, %14, %15, %18 offen offset:3072 sc1\n\t"        \
  "buffer_load_dwordx4 %12, %14, %15, %19 offen sc1\n\tbuffer_load_dwordx4 %13, %14, %15, %19 offen offset:1024 sc1\n"
__device__ __forceinline__ void load_w2_tail_support(u32x4 (&w2)[22], __amdgpu_buffer_rsrc_t r, u32 voff, int w) {
  asm volatile("s_cmp_lt_u32 %20, 4\n\ts_cbranch_scc1 1f\n\t" W2_LOADS "1:"
               : "=&v"(w2[8]), "=&v"(w2[9]), "=&v"(w2[10]), "=&v"(w2[11]), "=&v"(w2[12]), "=&v"(w2[13]), "=&v"(w2[14]), "=&v"(w2[15]), "=&v"(w2[16]),
                 "=&v"(w2[17]), "=&v"(w2[18]), "=&v"(w2[19]), "=&v"(w2[20]), "=&v"(w2[21])
               : "v"(voff), "s"(r), "s"(8192), "s"(12288), "s"(16384), "s"(20480), "s"(w) : "memory", "scc");
}
__device__ __forceinline__ void load_w2_tail_main(u32x4 (&w2)[22], __amdgpu_buffer_rsrc_t r, u32 voff, int w) {
  asm volatile("s_cmp_lt_u32 %20, 4\n\ts_cbranch_scc0 1f\n\t" W2_LOADS "1:"
               : "+v"(w2[8]), "+v"(w2[9]), "+v"(w2[10]), "+v"(w2[11]), "+v"(w2[12]), "+v"(w2[13]), "+v"(w2[14]), "+v"(w2[15]), "+v"(w2[16]),
                 "+v"(w2[17]), "+v"(w2[18]), "+v"(w2[19]), "+v"(w2[20]), "+v"(w2[21])
               : "v"(voff), "s"(r), "s"(8192), "s"(12288), "s"(16384), "s"(20480), "s"(w) : "memory", "scc");
}
__device__ __forceinline__ float silu_val(float acc, float rs, float bias) {
  const float tt = fmaf(acc, rs, bias);
  const float e = __builtin_amdgcn_exp2f(tt);
  return tt * __builtin_amdgcn_rcpf(fmaf(e, -1.44269504089f, -1.44269504089f));
}

__global__ __launch_bounds__(512, 2) void fused_block(const u16* __restrict__ X, const u16* __restrict__ K, const u16* __restrict__ V,
                                                      const u16* __restrict__ SK, const u16* __restrict__ HK, const u16* __restrict__ DV,
                                                      const u16* __restrict__ W1, const u16* __restrict__ W2, char* __restrict__ ws,
                                                      u16* __restrict__ OUT) {
  __shared__ __attribute__((aligned(16))) char sm[L_TOT];
  const int b = blockIdx.x, t = threadIdx.x, lane = t & 63;
  const int w = __builtin_amdgcn_readfirstlane(t >> 6);
  const int n = lane & 15, g = lane >> 4;
  char* lf = sm + L_FLG;
  const u64 t_start = __builtin_amdgcn_s_memrealtime();
#pragma unroll
  for (int i = 0; i < 4; i++) code_prefetch(w + 8 * i, 32, lane, (u32)(size_t)(sm + L_ST));
  if (t < 32) *(volatile LAS u32*)(lf + t * 4) = 0u;
  asm volatile("s_waitcnt lgkmcnt(0)" ::: "memory");
  __builtin_amdgcn_s_barrier();
  const int chp = 64 * (b & 7) + 2 * (b >> 3);
  const int rt = 8 * (b & 3) + ((b >> 3) & 7), c = 4 * ((b >> 2) & 1) + (b >> 6);
  const int row0 = 64 * rt;
  const u64 ep = *(const u64*)(ws + WS_EP) + 1;
  const u32 dvp = *(const u32*)(DV + chp);

  u32x4 wl[8];
  const int unit = w >= 4 ? b + 256 * (w - 4) : 1 << 20;
  if (w < 4) {
    const int dt = b & 7, st = b >> 3;
    const int rp = t >> 3, c8 = t & 7;
    const int s = st * 64 + 2 * rp, ch0 = dt * 64 + c8 * 8;
    const __amdgpu_buffer_rsrc_t rk = mk_rsrc(K, S * D * 2);
    const u32 off = (u32)(s * D + ch0) * 2;
    u32x4 kk[5];
#pragma unroll
    for (int i = 0; i < 5; i++) kk[i] = bload16(rk, off + (1 - i) * 1024);
    const u32x4 v0 = *(const u32x4*)((const char*)V + off), v1 = *(const u32x4*)((const char*)V + off + 1024);
    u32x4 wa[4];
#pragma unroll
    for (int i = 0; i < 4; i++) wa[i] = *(const u32x4*)(SK + ch0 * 4 + i * 8);
    SCHED_FENCE();
    char* pk = sm + P_T + (8 * c8) * 128 + (((rp >> 2) ^ c8) << 4) + ((3 - (rp & 3)) << 2);
#pragma unroll
    for (int i = 0; i < 4; i++) {
      const u32 l01 = perm_lo(kk[0][i], kk[1][i]), l12 = perm_lo(kk[1][i], kk[2][i]), l23 = perm_lo(kk[2][i], kk[3][i]), l34 = perm_lo(kk[3][i], kk[4][i]);
      const u32 h01 = perm_hi(kk[0][i], kk[1][i]), h12 = perm_hi(kk[1][i], kk[2][i]), h23 = perm_hi(kk[2][i], kk[3][i]), h34 = perm_hi(kk[3][i], kk[4][i]);
      const float a1 = dot2(l01, wa[i][0], dot2(l23, wa[i][1], 0.f)) * bf_lo(v1[i]);
      const float a0 = dot2(l12, wa[i][0], dot2(l34, wa[i][1], 0.f)) * bf_lo(v0[i]);
      const float b1 = dot2(h01, wa[i][2], dot2(h23, wa[i][3], 0.f)) * bf_hi(v1[i]);
      const float b0 = dot2(h12, wa[i][2], dot2(h34, wa[i][3], 0.f)) * bf_hi(v0[i]);
      *(u32*)(pk + (2 * i) * 128) = pack_bf(a1, a0);
      *(u32*)(pk + (2 * i + 1) * 128) = pack_bf(b1, b0);
    }
    lds_arrive(lf + W_CA, lane);
    lds_wait(lf + W_CA, 4u);
    const u32 tag = (u32)ep;
#pragma unroll
    for (int i = 0; i < 2; i++) {
      const int u = t + 256 * i, ch = u >> 3, cc = u & 7;
#pragma unroll
      for (int hh = 0; hh < 2; hh++) {
        const int grp = 4 * hh + (cc >> 1);
        const u32x2 d = *(const u32x2*)(sm + P_T + ch * 128 + ((grp ^ ((ch >> 3) & 7)) << 4) + (cc & 1) * 8);
        gstore16<16>(ws, WS_KVT + (size_t)(dt * 64 + ch) * 8192 + (st * 8 + 4 * hh) * 32 + cc * 16, u32x4{d[0], tag, d[1], tag});
      }
    }
    lds_arrive(lf + W_CB, lane);
    {
      const __amdgpu_buffer_rsrc_t ru = mk_rsrc(ws + WS_KVT + (size_t)chp * 8192, 2 * 8192);
      if (t < 60) *(u32x4*)(sm + P_U + (t < 30 ? 0 : U_CH - 480) + 16 * t) = u32x4{0, 0, 0, 0};
      u32x4 ga[2][2];
      __builtin_amdgcn_s_sleep(KV_FIRST);
      for (u32 spins = 0; spins < (1u << 18); spins++) {
#pragma unroll
        for (int cl = 0; cl < 2; cl++) { ga[cl][0] = bload16s(ru, t * 32, cl * 8192); ga[cl][1] = bload16s(ru, t * 32 + 16, cl * 8192); }
        bool ok = true;
#pragma unroll
        for (int cl = 0; cl < 2; cl++) ok &= ga[cl][0][1] == tag && ga[cl][0][3] == tag && ga[cl][1][1] == tag && ga[cl][1][3] == tag;
        if (__all(ok)) break;
        __builtin_amdgcn_s_sleep(KV_SLEEP);
      }
#pragma unroll
      for (int cl = 0; cl < 2; cl++) *(u32x4*)(sm + P_U + cl * U_CH + 480 + 16 * t) = u32x4{ga[cl][0][0], ga[cl][0][2], ga[cl][1][0], ga[cl][1][2]};
    }
    lds_arrive(lf + W_CD, lane);
    lds_wait(lf + W_HRDY, 4u);
    lds_wait(lf + W_CD, 4u);
  } else {
    const int s = t - 256;
    u32x4 ha[2], hb[2];
    const __amdgpu_buffer_rsrc_t rh_ = mk_rsrc(HK + (size_t)chp * S, 2 * S * 2);
#pragma unroll
    for (int cl = 0; cl < 2; cl++) { ha[cl] = bload16(rh_, (u32)(cl * S + 8 * s) * 2 - 16); hb[cl] = bload16(rh_, (u32)(cl * S + 8 * s) * 2); }
    SCHED_FENCE();
#pragma unroll
    for (int cl = 0; cl < 2; cl++) {
      if (s == 0) ha[cl] = u32x4{0, 0, 0, 0};
      char* hbase = sm + P_H + cl * HC_CH;
      const u32 W[8] = {ha[cl][0], ha[cl][1], ha[cl][2], ha[cl][3], hb[cl][0], hb[cl][1], hb[cl][2], hb[cl][3]};
#pragma unroll
      for (int cc = 0; cc < 8; cc++) {
        u32x4 o;
#pragma unroll
        for (int a = 0; a < 4; a++) o[a] = (cc & 1) ? __builtin_amdgcn_alignbit(W[(cc >> 1) + a + 1], W[(cc >> 1) + a], 16) : W[(cc >> 1) + a];
        *(u32x4*)(hbase + cc * HC_STRIDE + 16 * (s + 1 + hoff(cc))) = o;
      }
      if (s < 8) *(u32x4*)(hbase + s * HC_STRIDE + 16 * hoff_rt(s)) = u32x4{0, 0, 0, 0};
      if (s == 255) *(u32x4*)(hbase + 16 * (257 + hoff(0))) = hb[cl];
    }
    lds_arrive(lf + W_HRDY, lane);
    lds_wait(lf + W_CB, 4u);
    {
      const int lc = lane & 15, lg = lane >> 4;
      if (unit < 176) {
        const __amdgpu_buffer_rsrc_t rw = mk_rsrc(W1, D * F * 2);
        const int blk = unit >> 4, ks = unit & 15;
        const int col = 128 * blk + 8 * lc;
        const u32 o0 = col < F ? (u32)((32 * ks + 8 * lg) * F + col) * 2 : 0x80000000u;
#pragma unroll
        for (int j = 0; j < 8; j++) wl[j] = bload16(rw, o0 + j * F * 2);
      } else if (unit < 528) {
        const __amdgpu_buffer_rsrc_t rw = mk_rsrc(W2, D * F * 2);
        const int u2 = unit - 176, ks = u2 >> 3, c2 = u2 & 7;
#pragma unroll
        for (int j = 0; j < 8; j++) {
          const u32 o = (u32)(fcol(ks, lg, j) * D + 64 * c2 + 8 * (lc >> 2) + 2 * (lc & 3)) * 2;
          wl[j][0] = __builtin_amdgcn_raw_buffer_load_b32(rw, o, 0, 0);
          wl[j][1] = __builtin_amdgcn_raw_buffer_load_b32(rw, o + 64, 0, 0);
        }
      }
    }
  }

  u32x4 xf[2][4];
  auto load_x = [&]() {
#pragma unroll
    for (int kk = 0; kk < 2; kk++)
#pragma unroll
      for (int m = 0; m < 4; m++) xf[kk][m] = *(const u32x4*)(X + (size_t)(row0 + 16 * m + n) * D + 32 * (2 * w + kk) + 8 * g);
  };
  auto ln1_partials = [&]() {
    const u32x4 ones = {ONES, ONES, ONES, ONES};
#pragma unroll
    for (int m = 0; m < 4; m++) {
      f32x4 q0 = {0.f, 0.f, 0.f, 0.f}, q1 = {0.f, 0.f, 0.f, 0.f};
#pragma unroll
      for (int kk = 0; kk < 2; kk++) { q0 = mfma(xf[kk][m], xf[kk][m], q0); q1 = mfma(ones, xf[kk][m], q1); }
      const float lo = (n & 1) ? q0[1] : q0[0], hi = (n & 1) ? q0[3] : q0[2];
      if (g == (n >> 2)) *(f32x2*)(sm + L_X1 + ((w * 4 + m) * 16 + n) * 8) = f32x2{q1[0], (n & 2) ? hi : lo};
    }
  };
  constexpr int NT = 3, NW1 = 48, PRE = W1_PRE * NT;
  u32x4 w1[NW1];
  const __amdgpu_buffer_rsrc_t rw1 = mk_rsrc(ws + WS_W1T + (size_t)(c * 11 + 3 * (w & 3)) * 16 * 1024, 48 * 1024);
  auto w1load = [&](int k) { w1[k] = bload16s(rw1, lane * 16, (k % NT) * 16384 + (k / NT) * 1024); };
  if (w < 4) {
    const int wcl = (w >> 1) & 1, rh = w & 1;
    const u32 dvr = dvp >> (16 * wcl);
    f32x4 acc[8];
#pragma unroll
    for (int i = 0; i < 8; i++) acc[i] = f32x4{0.f, 0.f, 0.f, 0.f};
    const int idx0 = 25 + n - 8 * g;
    const int cc = idx0 & 7;
    const char* fbase = sm + P_H + wcl * HC_CH + cc * HC_STRIDE + 16 * ((idx0 >> 3) + hoff_rt(cc)) + 64 * 4 * rh;
    const char* gbase = sm + P_U + wcl * U_CH + 2 * (16 * n + 8 * g + 224) - 64 * 4 * rh;
    u32x4 Ff[2][8], Gf[2][8];
#pragma unroll
    for (int a = 0; a < 8; a++) { Gf[0][a] = *(const u32x4*)(gbase + 64 * (8 * a)); Ff[0][a] = *(const u32x4*)(fbase + 64 * (8 * a)); }
    SCHED_FENCE();
#pragma unroll
    for (int rr = 0; rr < 4; rr++) {
      const int cur = rr & 1, nxt = cur ^ 1;
      int m = 0;
#pragma unroll
      for (int bb = 0; bb < 8; bb++) {
#pragma unroll
        for (int a = 0; a + bb < 8; a++) {
          acc[a + bb] = mfma(Ff[cur][a], Gf[cur][bb], acc[a + bb]);
          if (rr < 3 && (m & 1) == 1 && m < 32) {
            const int q = m >> 2;
            SCHED_FENCE();
            if (m & 2) Ff[nxt][q] = *(const u32x4*)(fbase + 64 * (8 * q + rr + 1));
            else Gf[nxt][q] = *(const u32x4*)(gbase + 64 * (8 * q - rr - 1));
            SCHED_FENCE();
          }
          m++;
        }
      }
      SCHED_FENCE();
    }
    f32x4* rbuf = (f32x4*)(sm + P_R) + w * 256;
    if (rh) {
#pragma unroll
      for (int i = 0; i < 4; i++) rbuf[i * 64 + lane] = acc[i];
#pragma unroll
      for (int i = 0; i < 4; i++) acc[i] = acc[4 + i];
    } else {
#pragma unroll
      for (int i = 0; i < 4; i++) rbuf[i * 64 + lane] = acc[4 + i];
    }
    lds_set(lf + W_PART + w * 4, 1u, lane);
    load_x();
    const int ch = chp + wcl;
    const char* ub = sm + P_U + wcl * U_CH;
    u32x2 uq[4];
#pragma unroll
    for (int i = 0; i < 4; i++) uq[i] = *(const u32x2*)(ub + 2 * (240 + 256 * (4 * rh + i) + 16 * n + 8 * (g >> 1) + ((g & 1) ? 0 : 4)));
    lds_wait(lf + W_PART + (w ^ 1) * 4, 1u);
    const float dv = bf_lo(dvr);
    const f32x4* pbuf = (const f32x4*)(sm + P_R) + (w ^ 1) * 256;
#pragma unroll
    for (int i = 0; i < 4; i++) {
      const int T = 4 * rh + i;
      const f32x4 a = acc[i] + pbuf[i * 64 + lane];
      const int pos = 256 * T + 16 * n + 4 * g;
      const float y0 = fmaf(bf_hi(uq[i][1]), dv, a[0]), y1 = fmaf(bf_lo(uq[i][1]), dv, a[1]), y2 = fmaf(bf_hi(uq[i][0]), dv, a[2]), y3 = fmaf(bf_lo(uq[i][0]), dv, a[3]);
      gstore8<16>(ws, WS_YT + ((size_t)ch * S + pos) * 2, u32x2{pack_bf(y0, y1), pack_bf(y2, y3)});
    }
    __builtin_amdgcn_s_waitcnt(0x0f70);
    asm volatile("s_waitcnt vmcnt(0)" ::: "memory");
    lds_arrive(lf + W_CE, lane);
    if (w == 0) {
      lds_wait(lf + W_CE, 4u);
      flag_store(ws + WS_F2, b, ep, lane);
    }
    ln1_partials();
    lds_wait(lf + W_FWOK, 1u);
#pragma unroll
    for (int i = 0; i < PRE; i++) w1load(i);
    SCHED_FENCE();
    if (w == 0) {
      poll_flags<4>(ws + WS_F2, lane, 64, ep);
      if (b == 0 && lane == 0) __hip_atomic_store((GAS u64*)(ws + WS_EP), ep, RLX_AGENT);
    }
  } else {
    if (unit < 176) {
      const int blk = unit >> 4, ks = unit & 15;
      float csel[2] = {0.f, 0.f};
#pragma unroll
      for (int i = 0; i < 8; i++) {
        u32x4 o;
#pragma unroll
        for (int a = 0; a < 4; a++) o[a] = (i & 1) ? perm_hi(wl[2 * a][i >> 1], wl[2 * a + 1][i >> 1]) : perm_lo(wl[2 * a][i >> 1], wl[2 * a + 1][i >> 1]);
        gstore16<16>(ws, WS_W1T + ((size_t)((i * 11 + blk) * 16 + ks) * 64 + lane) * 16, o);
        const float cs = sum_g(dot2(o[0], ONES, dot2(o[1], ONES, dot2(o[2], ONES, dot2(o[3], ONES, 0.f)))));
        csel[i >> 2] = g == (i & 3) ? cs : csel[i >> 2];
      }
#pragma unroll
      for (int hh = 0; hh < 2; hh++)
        __hip_atomic_store((GAS u32*)(ws + WS_CSP + (size_t)(ks * 11 + blk) * 512 + hh * 256 + lane * 4), __float_as_uint(csel[hh]), RLX_AGENT);
    } else if (unit < 528) {
      const int u2 = unit - 176, ks = u2 >> 3, c2 = u2 & 7;
#pragma unroll
      for (int i = 0; i < 4; i++) {
        u32x4 o;
#pragma unroll
        for (int a = 0; a < 4; a++) o[a] = (i & 1) ? perm_hi(wl[2 * a][i >> 1], wl[2 * a + 1][i >> 1]) : perm_lo(wl[2 * a][i >> 1], wl[2 * a + 1][i >> 1]);
        gstore16<16>(ws, WS_W2T + ((size_t)(((c2 * 2 + (i >> 1)) * 44 + ks) * 2 + (i & 1)) * 64 + lane) * 16, o);
      }
    }
    __builtin_amdgcn_s_waitcnt(0x0f70);
    asm volatile("s_waitcnt vmcnt(0)" ::: "memory");
    lds_arrive(lf + W_WDONE, lane);
    if (w == 4) {
      lds_wait(lf + W_WDONE, 4u);
      flag_store(ws + WS_FW, b, ep, lane);
    }
    load_x();
    ln1_partials();
    if (w == 4) {
      poll_flags<4>(ws + WS_FW, lane, 64, ep);
      lds_set(lf + W_FWOK, 1u, lane);
    }
  }
  asm volatile("s_waitcnt lgkmcnt(0)" ::: "memory");
  __builtin_amdgcn_s_barrier();
  {
    const __amdgpu_buffer_rsrc_t rh = mk_rsrc(ws + WS_YT + ((size_t)(64 * w) * S + row0) * 2, 64 * S * 2);
    const int f0 = 2 * ((lane >> 4) & 1);
    const u32 v0 = (u32)(lane >> 3) * S * 2 + (((lane & 7) ^ f0) << 4), v1 = (u32)(lane >> 3) * S * 2 + (((lane & 7) ^ (f0 + 4)) << 4);
#pragma unroll
    for (int i = 0; i < 8; i++) glds16<true>(rh, (i & 1) ? v1 : v0, i * 8 * S * 2, (u32)(size_t)(sm + L_Y) + (64 * w + 8 * i) * 128);
  }
  const __amdgpu_buffer_rsrc_t rw2 = mk_rsrc(ws + WS_W2T + (size_t)((c * 2 + (w & 1)) * 44 + 11 * (w >> 1)) * 2048, 22 * 1024);
  float csp[16];
  const int cj = (w & 3) * 64 + lane;
  float r1[4], c1[4];
#pragma unroll
  for (int m = 0; m < 4; m++) {
    float a = 0.f, q = 0.f;
#pragma unroll
    for (int ww = 0; ww < 8; ww++) { const f32x2 v = *(const f32x2*)(sm + L_X1 + ((ww * 4 + m) * 16 + n) * 8); a += v[0]; q += v[1]; }
    const float mu = a * (1.f / D);
    const float var = fmaxf(q * (1.f / D) - mu * mu, 0.f);
    r1[m] = __builtin_amdgcn_rcpf(rb(__builtin_amdgcn_sqrtf(rb(rb(var) + 1e-5f))));
    c1[m] = -rb(mu) * r1[m];
  }
  asm volatile("s_waitcnt vmcnt(0)" ::: "memory");
  __builtin_amdgcn_s_barrier();
  const int q4 = (lane & 15) >> 2, p4 = lane & 3;
  {
    const int fsw = 2 * ((q4 >> 1) & 1) + 4 * (g & 1);
    u32x2 ya[2][4][2];
#pragma unroll
    for (int kk = 0; kk < 2; kk++)
#pragma unroll
      for (int m = 0; m < 4; m++) {
        const char* p = sm + L_Y + (8 * g + q4) * 128 + (((2 * m + (p4 >> 1)) ^ fsw) << 4) + 8 * (p4 & 1) + (2 * w + kk) * 4096;
        ya[kk][m][0] = tr_read(p); ya[kk][m][1] = tr_read(p + 512);
      }
    SCHED_FENCE();
#pragma unroll
    for (int kk = 0; kk < 2; kk++)
#pragma unroll
      for (int m = 0; m < 4; m++) {
        f32x2 xv[4], yv[4], qv[4];
#pragma unroll
        for (int a = 0; a < 4; a++) {
          const u32 xp = xf[kk][m][a], yp = ya[kk][m][a >> 1][a & 1];
          xv[a] = f32x2{bf_lo(xp), bf_hi(xp)}; yv[a] = f32x2{bf_lo(yp), bf_hi(yp)};
        }
        SCHED_FENCE();
#pragma unroll
        for (int a = 0; a < 4; a++) qv[a] = __builtin_elementwise_fma(xv[a], f32x2{r1[m], r1[m]}, f32x2{c1[m], c1[m]});
        SCHED_FENCE();
#pragma unroll
        for (int a = 0; a < 4; a++) qv[a] = __builtin_elementwise_fma(qv[a], yv[a], xv[a]);
        SCHED_FENCE();
        u32x4 hf;
#pragma unroll
        for (int a = 0; a < 4; a++) hf[a] = pack_bf(qv[a][0], qv[a][1]);
        *(u32x4*)(sm + L_H + ((m * 16 + 2 * w + kk) * 64 + lane) * 16) = hf;
      }
  }
  if (w >= 4) {
    const __amdgpu_buffer_rsrc_t rc = mk_rsrc(ws + WS_CSP, 16 * 11 * 512);
    const int cjc = cj < 176 ? cj : 175;
    const u32 co = (u32)((cjc >> 4) * 512 + c * 64 + (cjc & 15) * 4);
#pragma unroll
    for (int ks = 0; ks < 16; ks++) csp[ks] = __uint_as_float(__builtin_amdgcn_raw_buffer_load_b32(rc, co, ks * 11 * 512, 16));
  }
  asm volatile("s_waitcnt lgkmcnt(0)" ::: "memory");
  __builtin_amdgcn_s_barrier();
  auto save_residual = [&]() {
    *(u32x4*)(sm + L_RES + (w * 64 + lane) * 16) = *(const u32x4*)(sm + L_H + (((w >> 1) * 16 + 2 * c + (w & 1)) * 64 + lane) * 16);
  };
  if (w >= 4) {
    __builtin_amdgcn_s_setprio(3);
    u32x4 fa[16];
#pragma unroll
    for (int ks = 0; ks < 16; ks++) fa[ks] = *(const u32x4*)(sm + L_H + (((w - 4) * 16 + ks) * 64 + lane) * 16);
    f32x4 q0 = {0.f, 0.f, 0.f, 0.f}, q1 = {0.f, 0.f, 0.f, 0.f};
    const u32x4 ones = {ONES, ONES, ONES, ONES};
#pragma unroll
    for (int ks = 0; ks < 16; ks++) {
      q0 = mfma(fa[ks], fa[ks], q0);
      q1 = mfma(ones, fa[ks], q1);
    }
    {
      const float lo = (n & 1) ? q0[1] : q0[0], hi = (n & 1) ? q0[3] : q0[2];
      const float q = (n & 2) ? hi : lo;
      const float mu = q1[0] * (1.f / D);
      const float var = fmaxf(q * (1.f / D) - mu * mu, 0.f);
      const float rstd = __builtin_amdgcn_rcpf(rb(__builtin_amdgcn_sqrtf(rb(rb(var) + 1e-5f))));
      if (g == (n >> 2)) *(f32x2*)(sm + L_ST + ((w - 4) * 16 + n) * 8) = f32x2{-1.44269504089f * rstd, rb(mu) * rstd * 1.44269504089f};
    }
    {
      float s4 = 0.f;
#pragma unroll
      for (int ks = 0; ks < 16; ks++) s4 += csp[ks];
      if (cj < 192) *(float*)(sm + L_CS + cj * 4) = s4;
    }
    save_residual();
    lds_arrive(lf + W_LN2, lane);
    __builtin_amdgcn_s_setprio(0);
    lds_wait(lf + W_W1S, 1u);
  } else {
    auto issued = [&](int ks) constexpr { const int v = PRE + NT * (ks + 1); return v < NW1 ? v : NW1; };
    f32x4 acc[NT][4];
    u32x4 hb[3][4];
#pragma unroll
    for (int i = 0; i < NT; i++)
#pragma unroll
      for (int m = 0; m < 4; m++) acc[i][m] = f32x4{0.f, 0.f, 0.f, 0.f};
    const char* hbase = sm + L_H + lane * 16;
#pragma unroll
    for (int d = 0; d < 2; d++)
#pragma unroll
      for (int m = 0; m < 4; m++) hb[d][m] = *(const u32x4*)(hbase + (m * 16 + d) * 1024);
#pragma unroll
    for (int ks = 0; ks < 16; ks++) {
#pragma unroll
      for (int m = 0; m < 4; m++) {
        acc[0][m] = mfma(w1[ks * NT], hb[ks % 3][m], acc[0][m]);
        SCHED_FENCE();
        if (ks + 2 < 16) hb[(ks + 2) % 3][m] = *(const u32x4*)(hbase + (m * 16 + ks + 2) * 1024);
        SCHED_FENCE();
        acc[1][m] = mfma(w1[ks * NT + 1], hb[ks % 3][m], acc[1][m]);
        SCHED_FENCE();
        if (m < NT && issued(ks - 1) + m < issued(ks)) w1load(issued(ks - 1) + m);
        SCHED_FENCE();
        acc[2][m] = mfma(w1[ks * NT + 2], hb[ks % 3][m], acc[2][m]);
        SCHED_FENCE();
      }
      if (ks == 15 - W1_PRE && w == 0 && lane == 0) *(volatile LAS u32*)(lf + W_W1S) = 1u;
    }
    save_residual();
    lds_wait(lf + W_LN2, 4u);
    float rs[4], mr[4];
    f32x4 cs[NT];
#pragma unroll
    for (int m = 0; m < 4; m++) { const f32x2 v = *(const f32x2*)(sm + L_ST + (m * 16 + n) * 8); rs[m] = v[0]; mr[m] = v[1]; }
#pragma unroll
    for (int i = 0; i < NT; i++) cs[i] = *(const f32x4*)(sm + L_CS + ((3 * w + i) * 16 + 4 * g) * 4);
    const __amdgpu_buffer_rsrc_t hrs = mk_rsrc(ws + WS_HF + (size_t)rt * 44 * 4096, 44 * 4096);
    u32 o_t[NT];
#pragma unroll
    for (int i = 0; i < NT; i++) {
      const int tl = 3 * w + i;
      const int ks2 = tl < 10 ? 5 * c + (tl >> 1) : 40 + (c >> 1), par = tl < 10 ? (tl & 1) : (c & 1);
      o_t[i] = (tl < 11 && (g & 1) == par) ? (u32)(ks2 * 4096 + lane * 16) : 0x40000000u;
    }
#pragma unroll
    for (int m = 0; m < 4; m++) {
#pragma unroll
      for (int i = 0; i < NT; i++) {
        float h1[4];
#pragma unroll
        for (int r = 0; r < 4; r++) h1[r] = silu_val(acc[i][m][r], rs[m], mr[m] * cs[i][r]);
        const u32 p0 = pack_bf(h1[0], h1[1]), p1 = pack_bf(h1[2], h1[3]);
        const auto s0 = __builtin_amdgcn_permlane16_swap(p0, p0, false, false), s1 = __builtin_amdgcn_permlane16_swap(p1, p1, false, false);
        __builtin_amdgcn_raw_buffer_store_b128(u32x4{s0[0], s1[0], s0[1], s1[1]}, hrs, o_t[i] + m * 1024, 0, 16);
      }
      SCHED_FENCE();
    }
  }
  u32x4 w2[22];
#pragma unroll
  for (int i = 0; i < 8; i++) bload16_asm_sc1(w2[i], rw2, lane * 16, i * 1024);
  load_w2_tail_support(w2, rw2, lane * 16, w);
  asm volatile("s_cmp_lt_u32 %0, 4\n\ts_cbranch_scc0 1f\n\ts_waitcnt vmcnt(0)\n1:\n\ts_waitcnt lgkmcnt(0)" ::"s"(w) : "memory", "scc");
  __builtin_amdgcn_s_barrier();
  if (w == 7) {
    flag_store(ws + WS_F3, b, ep, lane);
    poll_flags<1>(ws + WS_F3, (b & 3) + (b & 56) + 4 * ((lane >> 2) & 1) + 64 * (lane & 3), 0, ep);
  }
  if (w < 4) __builtin_amdgcn_s_sleep(2);
  load_w2_tail_main(w2, rw2, lane * 16, w);
  __builtin_amdgcn_s_barrier();

  const int dh = w & 1, kq = w >> 1;
  const __amdgpu_buffer_rsrc_t rhf = mk_rsrc(ws + WS_HF + (size_t)rt * 44 * 4096 + (size_t)(11 * kq * 4 + 2 * dh) * 1024, 11 * 4096);
  const u32 ldst = (u32)(size_t)sm + (kq * 4 + 2 * dh) * 1024;
  auto stage1 = [&](int s, int e) {
    const int slot = s < 9 ? s : s - 9;
    glds16<true>(rhf, lane * 16, s * 4096 + e * 1024, ldst + slot * 16384 + e * 1024);
  };
  auto stage = [&](int s) { stage1(s, 0); stage1(s, 1); };
  stage(0); stage(1); stage(2); stage(3);
  f32x4 acc2[2][4];
#pragma unroll
  for (int i = 0; i < 2; i++)
#pragma unroll
    for (int m = 0; m < 4; m++) acc2[i][m] = f32x4{0.f, 0.f, 0.f, 0.f};
  asm volatile("s_waitcnt vmcnt(4)"
               : "+v"(w2[0]), "+v"(w2[1]), "+v"(w2[2]), "+v"(w2[3]), "+v"(w2[4]), "+v"(w2[5]), "+v"(w2[6]), "+v"(w2[7]), "+v"(w2[8]), "+v"(w2[9]), "+v"(w2[10]),
                 "+v"(w2[11]), "+v"(w2[12]), "+v"(w2[13]), "+v"(w2[14]), "+v"(w2[15]), "+v"(w2[16]), "+v"(w2[17]), "+v"(w2[18]), "+v"(w2[19]), "+v"(w2[20]), "+v"(w2[21])
               ::"memory");
  __builtin_amdgcn_s_barrier();
  {
    const char* base = sm + kq * 4096 + lane * 16;
    u32x4 hb[2][4];
#pragma unroll
    for (int m = 0; m < 4; m++) hb[0][m] = *(const u32x4*)(base + m * 1024);
#pragma unroll
    for (int s = 0; s < 11; s++) {
      if (s == 1) { asm volatile("s_waitcnt vmcnt(4)" ::: "memory"); __builtin_amdgcn_s_barrier(); }
      else if (s == 3 || s == 5) { asm volatile("s_waitcnt vmcnt(6)" ::: "memory"); __builtin_amdgcn_s_barrier(); }
      else if (s == 7) { asm volatile("s_waitcnt vmcnt(2)" ::: "memory"); __builtin_amdgcn_s_barrier(); }
      else if (s == 9) { asm volatile("s_waitcnt vmcnt(0)" ::: "memory"); __builtin_amdgcn_s_barrier(); }
      const int slot = s + 1 < 9 ? s + 1 : s + 1 - 9;
#pragma unroll
      for (int m = 0; m < 4; m++) {
        acc2[0][m] = mfma(w2[2 * s], hb[s & 1][m], acc2[0][m]);
        SCHED_FENCE();
        if (s < 10) hb[(s + 1) & 1][m] = *(const u32x4*)(base + slot * 16384 + m * 1024);
        SCHED_FENCE();
        acc2[1][m] = mfma(w2[2 * s + 1], hb[s & 1][m], acc2[1][m]);
        SCHED_FENCE();
        if (s < 2) stage1(4 + 2 * s + (m >> 1), m & 1);
        else if (s < 5 && (m & 1)) stage1(s + 6, m >> 1);
        SCHED_FENCE();
      }
    }
  }
  asm volatile("s_waitcnt lgkmcnt(0)" ::: "memory");
  __builtin_amdgcn_s_barrier();
  {
    char* red = sm + L_RED + dh * 32768;
    f32x4 o0, o1;
    auto scatter = [&](auto KQ) {
      constexpr int k = decltype(KQ)::value;
#pragma unroll
      for (int m = 0; m < 4; m++) {
        if (m != k) {
#pragma unroll
          for (int i = 0; i < 2; i++) *(f32x4*)(red + (((k * 4 + m) * 2 + i) * 64 + lane) * 16) = acc2[i][m];
        }
      }
      o0 = acc2[0][k]; o1 = acc2[1][k];
      asm volatile("" : "+v"(o0), "+v"(o1));
    };
    if (kq == 0) scatter(IntC<0>{}); else if (kq == 1) scatter(IntC<1>{}); else if (kq == 2) scatter(IntC<2>{}); else scatter(IntC<3>{});
    __syncthreads();
#pragma unroll
    for (int j = 1; j < 4; j++) {
      const int src = (kq + j) & 3;
      o0 += *(const f32x4*)(red + (((src * 4 + kq) * 2 + 0) * 64 + lane) * 16);
      o1 += *(const f32x4*)(red + (((src * 4 + kq) * 2 + 1) * 64 + lane) * 16);
    }
    const u32x4 hres = *(const u32x4*)(sm + L_RES + (w * 64 + lane) * 16);
    u32x4 res;
#pragma unroll
    for (int r = 0; r < 4; r++) res[r] = pack_bf(bf_lo(hres[r]) + rb(o0[r]), bf_hi(hres[r]) + rb(o1[r]));
    __builtin_amdgcn_raw_buffer_store_b128(res, mk_rsrc(OUT, S * D * 2), (u32)((row0 + 16 * kq + n) * D + 64 * c + 32 * dh + 8 * g) * 2, 0, 2);
  }
  while (__builtin_amdgcn_s_memrealtime() - t_start < 4000ull) __builtin_amdgcn_s_sleep(2);
  asm volatile("ffn_text_end:");
}

extern "C" void kernel_launch(void* const* d_in, const int* in_sizes, int n_in, void* d_out, int out_size, void* d_ws,
                              size_t ws_size, hipStream_t stream) {
  fused_block<<<256, 512, 0, stream>>>((const u16*)d_in[0], (const u16*)d_in[1], (const u16*)d_in[2], (const u16*)d_in[3], (const u16*)d_in[4],
                                       (const u16*)d_in[5], (const u16*)d_in[7], (const u16*)d_in[8], (char*)d_ws, (u16*)d_out);
}
```

```cpp
#include <hip/hip_runtime.h>
#include <stdint.h>

typedef unsigned short u16;
typedef unsigned int u32;
typedef unsigned long long u64;
typedef __bf16 bf16x8 __attribute__((ext_vector_type(8)));
typedef __bf16 bf16x2 __attribute__((ext_vector_type(2)));
typedef short s16x4 __attribute__((ext_vector_type(4)));
typedef float f32x4 __attribute__((ext_vector_type(4)));
typedef float f32x2 __attribute__((ext_vector_type(2)));
typedef u32 u32x4 __attribute__((ext_vector_type(4)));
typedef u32 u32x2 __attribute__((ext_vector_type(2)));
#define GAS __attribute__((address_space(1)))
#define LAS __attribute__((address_space(3)))
#define RLX_AGENT __ATOMIC_RELAXED, __HIP_MEMORY_SCOPE_AGENT
#define SCHED_FENCE() __builtin_amdgcn_sched_barrier(0)

constexpr int S = 2048, D = 512, F = 1376;

constexpr size_t WS_FLAG = 0;
constexpr size_t WS_KVT  = 1 << 16;
constexpr size_t WS_XT   = WS_KVT + (size_t)D * S * 2;
constexpr size_t WS_HT   = WS_XT + (size_t)D * S * 2;
constexpr size_t WS_W1T  = WS_HT + (size_t)D * S * 2;
constexpr size_t WS_W2T  = WS_W1T + 8 * 11 * 16 * 1024;
constexpr size_t WS_CSP  = WS_W2T + 8 * 4 * 44 * 1024;
constexpr size_t WS_ST1  = WS_CSP + 16 * 8 * 11 * 16 * 4;
constexpr size_t WS_HF   = WS_ST1 + S * 8;
constexpr size_t WS_END  = WS_HF + (size_t)32 * 44 * 4 * 1024;

__device__ __forceinline__ float bf_lo(u32 v) { return __uint_as_float(v << 16); }
__device__ __forceinline__ float bf_hi(u32 v) { return __uint_as_float(v & 0xffff0000u); }
__device__ __forceinline__ u32 pack_bf(float a, float b) {
  f32x2 z = {a, b};
  return __builtin_bit_cast(u32, __builtin_convertvector(z, bf16x2));
}
__device__ __forceinline__ float rb(float a) { return bf_lo(pack_bf(a, 0.f)); }
__device__ __forceinline__ float dot2(u32 a, u32 b, float c) {
  return __builtin_amdgcn_fdot2_f32_bf16(__builtin_bit_cast(bf16x2, a), __builtin_bit_cast(bf16x2, b), c, false);
}
__device__ __forceinline__ u32 perm_lo(u32 a, u32 b) { return __builtin_amdgcn_perm(b, a, 0x05040100u); }
__device__ __forceinline__ u32 perm_hi(u32 a, u32 b) { return __builtin_amdgcn_perm(b, a, 0x07060302u); }
__device__ __forceinline__ f32x4 mfma(u32x4 a, u32x4 b, f32x4 c) {
  return __builtin_amdgcn_mfma_f32_16x16x32_bf16(__builtin_bit_cast(bf16x8, a), __builtin_bit_cast(bf16x8, b), c, 0, 0, 0);
}
constexpr u32 ONES = 0x3F803F80u;
template <int V> struct IntC { static constexpr int value = V; };
__device__ __forceinline__ float sum_g(float x) {
  auto s = __builtin_amdgcn_permlane16_swap(__float_as_uint(x), __float_as_uint(x), false, false);
  x = __uint_as_float(s[0]) + __uint_as_float(s[1]);
  auto t = __builtin_amdgcn_permlane32_swap(__float_as_uint(x), __float_as_uint(x), false, false);
  return __uint_as_float(t[0]) + __uint_as_float(t[1]);
}
template <int CTRL>
__device__ __forceinline__ float dpp_add(float x) {
  return x + __uint_as_float(__builtin_amdgcn_update_dpp(0u, __float_as_uint(x), CTRL, 0xf, 0xf, true));
}
__device__ __forceinline__ float wave_sum(float x) {
  x = dpp_add<0xB1>(x);
  x = dpp_add<0x4E>(x);
  x = dpp_add<0x141>(x);
  x = dpp_add<0x140>(x);
  return sum_g(x);
}
__device__ __forceinline__ __amdgpu_buffer_rsrc_t mk_rsrc(const void* p, u32 bytes) {
  return __builtin_amdgcn_make_buffer_rsrc((void*)p, 0, bytes, 0x00020000);
}
__device__ __forceinline__ u32x4 bload16(__amdgpu_buffer_rsrc_t r, u32 off) { return __builtin_amdgcn_raw_buffer_load_b128(r, off, 0, 0); }
__device__ __forceinline__ u32x2 bload8(__amdgpu_buffer_rsrc_t r, u32 off) { return __builtin_amdgcn_raw_buffer_load_b64(r, off, 0, 0); }
template <bool SC1>
__device__ __forceinline__ void glds16(__amdgpu_buffer_rsrc_t r, u32 voff, u32 soff, u32 lds_dst) {
  if (SC1)
    asm volatile("s_mov_b32 m0, %0\n\ts_nop 0\n\tbuffer_load_dwordx4 %1, %2, %3 offen sc1 lds" ::"s"(lds_dst), "v"(voff), "s"(r), "s"(soff) : "memory");
  else
    asm volatile("s_mov_b32 m0, %0\n\ts_nop 0\n\tbuffer_load_dwordx4 %1, %2, %3 offen lds" ::"s"(lds_dst), "v"(voff), "s"(r), "s"(soff) : "memory");
}
__device__ __forceinline__ void bload16_asm(u32x4& d, __amdgpu_buffer_rsrc_t r, u32 voff, u32 soff) {
  asm volatile("buffer_load_dwordx4 %0, %1, %2, %3 offen" : "=v"(d) : "v"(voff), "s"(r), "s"(soff) : "memory");
}
#define K1_AUX 0
#define WT_AUX 16
#define K2_AUX 16
template <int AUX>
__device__ __forceinline__ void gstore16(char* base, size_t off, u32x4 v) {
  const __amdgpu_buffer_rsrc_t r = mk_rsrc(base, 0x7fffffffu);
  __builtin_amdgcn_raw_buffer_store_b128(v, r, (u32)off, 0, AUX);
}
template <int AUX>
__device__ __forceinline__ void gstore8(char* base, size_t off, u32x2 v) {
  const __amdgpu_buffer_rsrc_t r = mk_rsrc(base, 0x7fffffffu);
  __builtin_amdgcn_raw_buffer_store_b64(v, r, (u32)off, 0, AUX);
}
__device__ __forceinline__ u32x4 bload16s(__amdgpu_buffer_rsrc_t r, u32 voff, u32 soff) { return __builtin_amdgcn_raw_buffer_load_b128(r, voff, soff, 0); }
__device__ __forceinline__ u32x2 tr_read(const char* p) {
  s16x4 r = __builtin_amdgcn_ds_read_tr16_b64_v4i16((LAS s16x4*)p);
  return __builtin_bit_cast(u32x2, r);
}

__device__ __forceinline__ void code_prefetch(int part, int nparts, int lane) {
  u64 pc; u32 len;
  asm volatile("s_getpc_b64 %0\n\ts_mov_b32 %1, ffn_text_end-." : "=s"(pc), "=s"(len));
  const __amdgpu_buffer_rsrc_t r = mk_rsrc((const void*)pc, len);
  u32 acc = 0;
  for (u32 o = part * 1024; o < len; o += nparts * 1024) acc |= __builtin_amdgcn_raw_buffer_load_b128(r, o + lane * 16, 0, 0)[0];
  asm volatile("" ::"v"(acc));
}
__device__ __forceinline__ int fcol(int ks, int g, int j) {
  const int slot = 8 * (g >> 1) + j;
  if (ks < 40) { const int ct = ks / 5, p = ks - 5 * ct; return 128 * (2 * p + (g & 1)) + 8 * slot + ct; }
  return 1280 + 8 * slot + 2 * (ks - 40) + (g & 1);
}

__global__ __launch_bounds__(512, 2) void k1_prep(const u16* __restrict__ X, const u16* __restrict__ K, const u16* __restrict__ V,
                                                  const u16* __restrict__ SK, const u16* __restrict__ HK, char* __restrict__ ws) {
  __shared__ __attribute__((aligned(16))) char sm[16384 + 4096];
  const int b = blockIdx.x, t = threadIdx.x, lane = t & 63;
  const int w = __builtin_amdgcn_readfirstlane(t >> 6);
  const int dt = b & 7, st = b >> 3;
  if (w < 4) {
    const int rp = t >> 3, c = t & 7;
    const int s = st * 64 + 2 * rp, ch0 = dt * 64 + c * 8;
    const __amdgpu_buffer_rsrc_t rk = mk_rsrc(K, S * D * 2);
    const u32 off = (u32)(s * D + ch0) * 2;
    u32x4 kk[5];
#pragma unroll
    for (int i = 0; i < 5; i++) kk[i] = bload16(rk, off + (1 - i) * 1024);
    const u32x4 v0 = *(const u32x4*)((const char*)V + off), v1 = *(const u32x4*)((const char*)V + off + 1024);
    const u32x4 x0 = *(const u32x4*)((const char*)X + off), x1 = *(const u32x4*)((const char*)X + off + 1024);
    u32x4 wa[4];
#pragma unroll
    for (int i = 0; i < 4; i++) wa[i] = *(const u32x4*)(SK + ch0 * 4 + i * 8);
    SCHED_FENCE();
    const int swz = ((rp >> 2) ^ c) << 4;
    char* pk = sm + (8 * c) * 128 + swz + ((3 - (rp & 3)) << 2);
    char* px = sm + 8192 + (8 * c) * 128 + swz + ((rp & 3) << 2);
#pragma unroll
    for (int i = 0; i < 4; i++) {
      const u32 l01 = perm_lo(kk[0][i], kk[1][i]), l12 = perm_lo(kk[1][i], kk[2][i]), l23 = perm_lo(kk[2][i], kk[3][i]), l34 = perm_lo(kk[3][i], kk[4][i]);
      const u32 h01 = perm_hi(kk[0][i], kk[1][i]), h12 = perm_hi(kk[1][i], kk[2][i]), h23 = perm_hi(kk[2][i], kk[3][i]), h34 = perm_hi(kk[3][i], kk[4][i]);
      const float a1 = dot2(l01, wa[i][0], dot2(l23, wa[i][1], 0.f)) * bf_lo(v1[i]);
      const float a0 = dot2(l12, wa[i][0], dot2(l34, wa[i][1], 0.f)) * bf_lo(v0[i]);
      const float b1 = dot2(h01, wa[i][2], dot2(h23, wa[i][3], 0.f)) * bf_hi(v1[i]);
      const float b0 = dot2(h12, wa[i][2], dot2(h34, wa[i][3], 0.f)) * bf_hi(v0[i]);
      *(u32*)(pk + (2 * i) * 128) = pack_bf(a1, a0);
      *(u32*)(pk + (2 * i + 1) * 128) = pack_bf(b1, b0);
      *(u32*)(px + (2 * i) * 128) = perm_lo(x0[i], x1[i]);
      *(u32*)(px + (2 * i + 1) * 128) = perm_hi(x0[i], x1[i]);
    }
    __syncthreads();
#pragma unroll
    for (int i = 0; i < 2; i++) {
      const int u = t + 256 * i, ch = u >> 3, cc = u & 7;
      const u32x4 o = *(const u32x4*)(sm + ch * 128 + cc * 16);
      const int grp = cc ^ ((ch >> 3) & 7);
      const size_t go = (size_t)(dt * 64 + ch) * S + st * 64 + grp * 8;
      gstore16<K1_AUX>(ws, WS_KVT + go * 2, o);
      gstore16<K1_AUX>(ws, WS_XT + go * 2, *(const u32x4*)(sm + 8192 + ch * 128 + cc * 16));
    }
  } else {
    const int lrow = 256 * (b & 7) + 8 * (b >> 3) + 2 * (w - 4);
    u32x4 xr[2];
#pragma unroll
    for (int i = 0; i < 2; i++) xr[i] = *(const u32x4*)(X + (size_t)(lrow + i) * D + lane * 8);
    if (w == 4 && lane < 32) __hip_atomic_store((GAS u32*)(ws + WS_FLAG) + b * 64 + lane, 0u, RLX_AGENT);
    {
      const u32 dump = (u32)(size_t)(sm + 16384) + (w - 4) * 1024;
      const int chp = 64 * (b & 7) + 2 * (b >> 3);
      const __amdgpu_buffer_rsrc_t rh = mk_rsrc(HK + (size_t)chp * S, 2 * S * 2);
      glds16<false>(rh, lane * 16, (w - 4) * 2048, dump);
      glds16<false>(rh, lane * 16, (w - 4) * 2048 + 1024, dump);
    }
    code_prefetch((b >> 3) * 4 + (w - 4), 128, lane);
    __builtin_amdgcn_s_barrier();
#pragma unroll
    for (int q = 0; q < 2; q++) {
      float sum = 0.f;
#pragma unroll
      for (int i = 0; i < 4; i++) sum = dot2(xr[q][i], ONES, sum);
      const float mean = wave_sum(sum) * (1.f / D);
      float sq = 0.f;
#pragma unroll
      for (int i = 0; i < 4; i++) { const float d0 = bf_lo(xr[q][i]) - mean, d1 = bf_hi(xr[q][i]) - mean; sq += d0 * d0 + d1 * d1; }
      sq = wave_sum(sq);
      const float sd = rb(__builtin_amdgcn_sqrtf(rb(rb(sq * (1.f / D)) + 1e-5f)));
      const float r1 = __builtin_amdgcn_rcpf(sd);
      if (lane == 0) { f32x2 o2 = {r1, -rb(mean) * r1}; *(f32x2*)(ws + WS_ST1 + (size_t)(lrow + q) * 8) = o2; }
    }
    asm volatile("s_waitcnt vmcnt(0)" ::: "memory");
  }
}

constexpr int HC_STRIDE = 4608;
constexpr int HC_CH = 8 * HC_STRIDE;
constexpr int U_CH = (240 + 2048) * 2;
constexpr int K2_H = 0, K2_U = 2 * HC_CH, K2_R = K2_U + 2 * U_CH + 32, K2_F = K2_R + 4 * 4096, K2_END = K2_F + 32;
__device__ __forceinline__ constexpr int hoff(int c) {
  return c == 0 ? 4 : c == 1 ? 2 : c == 2 ? 8 : c == 3 ? 3 : c == 4 ? 7 : c == 5 ? 0 : c == 6 ? 15 : 12;
}
__device__ __forceinline__ int hoff_rt(int c) { return (0xCF073824u >> (4 * c)) & 15; }
__device__ __forceinline__ void lds_wait(const char* p, u32 want) {
  for (u32 spins = 0; spins < (1u << 22); spins++) {
    if (__builtin_amdgcn_readfirstlane(*(volatile LAS u32*)p) == want) break;
    __builtin_amdgcn_s_sleep(1);
  }
  asm volatile("" ::: "memory");
}

__global__ __launch_bounds__(512, 2) void k2_conv(const u16* __restrict__ HK, const u16* __restrict__ DV,
                                                  const u16* __restrict__ W1, const u16* __restrict__ W2, char* __restrict__ ws) {
  __shared__ __attribute__((aligned(16))) char sm[K2_END];
  const int b = blockIdx.x, t = threadIdx.x, lane = t & 63;
  const int w = __builtin_amdgcn_readfirstlane(t >> 6);
  const int chp = 64 * (b & 7) + 2 * (b >> 3);
  const int wcl = (w >> 1) & 1, rh = w & 1;
  const int n = lane & 15, g = lane >> 4;
  {
    const int cl = t >> 8, j = t & 255;
    const __amdgpu_buffer_rsrc_t rh_ = mk_rsrc(HK + (size_t)chp * S, 2 * S * 2);
    const u32 ho = (u32)(cl * S + 8 * j) * 2;
    u32x4 ha, hb, uu;
    const __amdgpu_buffer_rsrc_t ru = mk_rsrc(ws + WS_KVT + (size_t)chp * S * 2, 2 * S * 2);
    bload16_asm(ha, rh_, ho - 16, 0);
    bload16_asm(hb, rh_, ho, 0);
    bload16_asm(uu, ru, ho, 0);
    asm volatile("s_waitcnt vmcnt(0)" : "+v"(ha), "+v"(hb), "+v"(uu)::"memory");
    if (t < 8) *(volatile LAS u32*)(sm + K2_F + t * 4) = 0u;
    SCHED_FENCE();
    if (j == 0) ha = u32x4{0, 0, 0, 0};
    char* hbase = sm + K2_H + cl * HC_CH;
    const u32 W[8] = {ha[0], ha[1], ha[2], ha[3], hb[0], hb[1], hb[2], hb[3]};
#pragma unroll
    for (int c = 0; c < 8; c++) {
      u32x4 o;
#pragma unroll
      for (int a = 0; a < 4; a++) o[a] = (c & 1) ? __builtin_amdgcn_alignbit(W[(c >> 1) + a + 1], W[(c >> 1) + a], 16) : W[(c >> 1) + a];
      *(u32x4*)(hbase + c * HC_STRIDE + 16 * (j + 1 + hoff(c))) = o;
    }
    const u32x4 z4 = {0, 0, 0, 0};
    if (j < 8) *(u32x4*)(hbase + j * HC_STRIDE + 16 * hoff_rt(j)) = z4;
    if (j == 255) *(u32x4*)(hbase + 16 * (257 + hoff(0))) = hb;
    char* ub = sm + K2_U + cl * U_CH;
    *(u32x4*)(ub + 480 + 16 * j) = uu;
    if (j < 30) *(u32x4*)(ub + 16 * j) = z4;
  }
  u32x2 xq[4];
  f32x4 sa[4], sb[4];
  u32 dvr = 0;
  if (w < 4) {
    const __amdgpu_buffer_rsrc_t rs1 = mk_rsrc(ws + WS_ST1, S * 8);
    const __amdgpu_buffer_rsrc_t rx = mk_rsrc(ws + WS_XT + (size_t)(chp + wcl) * S * 2, S * 2);
    const u32 p0 = 1024 * rh + 16 * n + 4 * g;
#pragma unroll
    for (int i = 0; i < 4; i++) {
      asm volatile("buffer_load_dwordx2 %0, %1, %2, %3 offen" : "=v"(xq[i]) : "v"(p0 * 2), "s"(rx), "s"(i * 512) : "memory");
      asm volatile("buffer_load_dwordx4 %0, %1, %2, %3 offen" : "=v"(sa[i]) : "v"(p0 * 8), "s"(rs1), "s"(i * 2048) : "memory");
      asm volatile("buffer_load_dwordx4 %0, %1, %2, %3 offen offset:16" : "=v"(sb[i]) : "v"(p0 * 8), "s"(rs1), "s"(i * 2048) : "memory");
    }
    dvr = *(const u32*)(DV + chp) >> (16 * wcl);
  }
  __syncthreads();
  if (w < 4) {
    f32x4 acc[8];
#pragma unroll
    for (int i = 0; i < 8; i++) acc[i] = f32x4{0.f, 0.f, 0.f, 0.f};
    const int idx0 = 25 + n - 8 * g;
    const int c = idx0 & 7;
    const char* fbase = sm + K2_H + wcl * HC_CH + c * HC_STRIDE + 16 * ((idx0 >> 3) + hoff_rt(c)) + 64 * 4 * rh;
    const char* gbase = sm + K2_U + wcl * U_CH + 2 * (16 * n + 8 * g + 224) - 64 * 4 * rh;
    u32x4 Ff[2][8], Gf[2][8];
#pragma unroll
    for (int a = 0; a < 8; a++) { Gf[0][a] = *(const u32x4*)(gbase + 64 * (8 * a)); Ff[0][a] = *(const u32x4*)(fbase + 64 * (8 * a)); }
    SCHED_FENCE();
#pragma unroll
    for (int rr = 0; rr < 4; rr++) {
      const int cur = rr & 1, nxt = cur ^ 1;
      int m = 0;
#pragma unroll
      for (int bb = 0; bb < 8; bb++) {
#pragma unroll
        for (int a = 0; a + bb < 8; a++) {
          acc[a + bb] = mfma(Ff[cur][a], Gf[cur][bb], acc[a + bb]);
          if (rr < 3 && (m & 1) == 1 && m < 32) {
            const int q = m >> 2;
            SCHED_FENCE();
            if (m & 2) Ff[nxt][q] = *(const u32x4*)(fbase + 64 * (8 * q + rr + 1));
            else Gf[nxt][q] = *(const u32x4*)(gbase + 64 * (8 * q - rr - 1));
            SCHED_FENCE();
          }
          m++;
        }
      }
      SCHED_FENCE();
    }
    f32x4* rbuf = (f32x4*)(sm + K2_R) + w * 256;
    if (rh) {
#pragma unroll
      for (int i = 0; i < 4; i++) rbuf[i * 64 + lane] = acc[i];
#pragma unroll
      for (int i = 0; i < 4; i++) acc[i] = acc[4 + i];
    } else {
#pragma unroll
      for (int i = 0; i < 4; i++) rbuf[i * 64 + lane] = acc[4 + i];
    }
    asm volatile("s_waitcnt lgkmcnt(0)" ::: "memory");
    if (lane == 0) *(volatile LAS u32*)(sm + K2_F + w * 4) = 1u;
    const int ch = chp + wcl;
    const char* ub = sm + K2_U + wcl * U_CH;
    u32x2 uq[4];
#pragma unroll
    for (int i = 0; i < 4; i++) uq[i] = *(const u32x2*)(ub + 2 * (240 + 256 * (4 * rh + i) + 16 * n + 8 * (g >> 1) + ((g & 1) ? 0 : 4)));
    asm volatile("s_waitcnt vmcnt(0)" : "+v"(xq[0]), "+v"(xq[1]), "+v"(xq[2]), "+v"(xq[3]), "+v"(sa[0]), "+v"(sa[1]), "+v"(sa[2]), "+v"(sa[3]),
                 "+v"(sb[0]), "+v"(sb[1]), "+v"(sb[2]), "+v"(sb[3])::"memory");
    lds_wait(sm + K2_F + (w ^ 1) * 4, 1u);
    const float dv = bf_lo(dvr);
    const f32x4* pbuf = (const f32x4*)(sm + K2_R) + (w ^ 1) * 256;
#pragma unroll
    for (int i = 0; i < 4; i++) {
      const int T = 4 * rh + i;
      const f32x4 a = acc[i] + pbuf[i * 64 + lane];
      const int pos = 256 * T + 16 * n + 4 * g;
      const float y0 = fmaf(bf_hi(uq[i][1]), dv, a[0]), y1 = fmaf(bf_lo(uq[i][1]), dv, a[1]), y2 = fmaf(bf_hi(uq[i][0]), dv, a[2]), y3 = fmaf(bf_lo(uq[i][0]), dv, a[3]);
      const float x0 = bf_lo(xq[i][0]), x1 = bf_hi(xq[i][0]), x2 = bf_lo(xq[i][1]), x3 = bf_hi(xq[i][1]);
      const float h0 = fmaf(fmaf(x0, sa[i][0], sa[i][1]), y0, x0), h1 = fmaf(fmaf(x1, sa[i][2], sa[i][3]), y1, x1);
      const float h2 = fmaf(fmaf(x2, sb[i][0], sb[i][1]), y2, x2), h3 = fmaf(fmaf(x3, sb[i][2], sb[i][3]), y3, x3);
      const u32x2 o = {pack_bf(h0, h1), pack_bf(h2, h3)};
      gstore8<K2_AUX>(ws, WS_HT + ((size_t)ch * S + pos) * 2, o);
    }
  } else {
    const int unit = b + 256 * (w - 4);
    const int lc = lane & 15, lg = lane >> 4;
    if (unit < 176) {
      const __amdgpu_buffer_rsrc_t rw = mk_rsrc(W1, D * F * 2);
      const int blk = unit >> 4, ks = unit & 15;
      const int col = 128 * blk + 8 * lc;
      const u32 o0 = col < F ? (u32)((32 * ks + 8 * lg) * F + col) * 2 : 0x80000000u;
      u32x4 wl[8];
#pragma unroll
      for (int j = 0; j < 8; j++) wl[j] = bload16(rw, o0 + j * F * 2);
#pragma unroll
      for (int i = 0; i < 8; i++) {
        u32x4 o;
#pragma unroll
        for (int a = 0; a < 4; a++) o[a] = (i & 1) ? perm_hi(wl[2 * a][i >> 1], wl[2 * a + 1][i >> 1]) : perm_lo(wl[2 * a][i >> 1], wl[2 * a + 1][i >> 1]);
        gstore16<WT_AUX>(ws, WS_W1T + ((size_t)((i * 11 + blk) * 16 + ks) * 64 + lane) * 16, o);
        const float cs = sum_g(dot2(o[0], ONES, dot2(o[1], ONES, dot2(o[2], ONES, dot2(o[3], ONES, 0.f)))));
        if (lane < 16) *(float*)(ws + WS_CSP + ((size_t)(i * 176 + blk * 16 + lane) * 16 + ks) * 4) = cs;
      }
    } else if (unit < 528) {
      const __amdgpu_buffer_rsrc_t rw = mk_rsrc(W2, D * F * 2);
      const int u2 = unit - 176, ks = u2 >> 3, c2 = u2 & 7;
      u32 wx[8], wy[8];
#pragma unroll
      for (int j = 0; j < 8; j++) {
        const u32 o = (u32)(fcol(ks, lg, j) * D + 64 * c2 + 8 * (lc >> 2) + 2 * (lc & 3)) * 2;
        wx[j] = __builtin_amdgcn_raw_buffer_load_b32(rw, o, 0, 0);
        wy[j] = __builtin_amdgcn_raw_buffer_load_b32(rw, o + 64, 0, 0);
      }
#pragma unroll
      for (int i = 0; i < 4; i++) {
        u32x4 o;
#pragma unroll
        for (int a = 0; a < 4; a++) {
          const u32 e0 = (i >> 1) ? wy[2 * a] : wx[2 * a], e1 = (i >> 1) ? wy[2 * a + 1] : wx[2 * a + 1];
          o[a] = (i & 1) ? perm_hi(e0, e1) : perm_lo(e0, e1);
        }
        gstore16<WT_AUX>(ws, WS_W2T + ((size_t)(((c2 * 2 + (i >> 1)) * 44 + ks) * 2 + (i & 1)) * 64 + lane) * 16, o);
      }
    }
  }
}

constexpr int L_HT = 0;
constexpr int L_ST = 147456;
constexpr int L_CS = L_ST + 512;
constexpr int L_RES = L_CS + 768;
constexpr int L_END = L_RES + 8192;
constexpr int L_RED = 0;
constexpr int L_FLG = L_END;
constexpr int L_TOT = L_END + 16;
#define W1_PRE 4
#define W2_LOADS                                                                                                                   \
  "buffer_load_dwordx4 %0, %14, %15, %16 offen\n\tbuffer_load_dwordx4 %1, %14, %15, %16 offen offset:1024\n\t"                      \
  "buffer_load_dwordx4 %2, %14, %15, %16 offen offset:2048\n\tbuffer_load_dwordx4 %3, %14, %15, %16 offen offset:3072\n\t"          \
  "buffer_load_dwordx4 %4, %14, %15, %17 offen\n\tbuffer_load_dwordx4 %5, %14, %15, %17 offen offset:1024\n\t"                      \
  "buffer_load_dwordx4 %6, %14, %15, %17 offen offset:2048\n\tbuffer_load_dwordx4 %7, %14, %15, %17 offen offset:3072\n\t"          \
  "buffer_load_dwordx4 %8, %14, %15, %18 offen\n\tbuffer_load_dwordx4 %9, %14, %15, %18 offen offset:1024\n\t"                      \
  "buffer_load_dwordx4 %10, %14, %15, %18 offen offset:2048\n\tbuffer_load_dwordx4 %11, %14, %15, %18 offen offset:3072\n\t"        \
  "buffer_load_dwordx4 %12, %14, %15, %19 offen\n\tbuffer_load_dwordx4 %13, %14, %15, %19 offen offset:1024\n"
__device__ __forceinline__ void load_w2_tail_support(u32x4 (&w2)[22], __amdgpu_buffer_rsrc_t r, u32 voff, int w) {
  asm volatile("s_cmp_lt_u32 %20, 4\n\ts_cbranch_scc1 1f\n\t" W2_LOADS "1:"
               : "=&v"(w2[8]), "=&v"(w2[9]), "=&v"(w2[10]), "=&v"(w2[11]), "=&v"(w2[12]), "=&v"(w2[13]), "=&v"(w2[14]), "=&v"(w2[15]), "=&v"(w2[16]),
                 "=&v"(w2[17]), "=&v"(w2[18]), "=&v"(w2[19]), "=&v"(w2[20]), "=&v"(w2[21])
               : "v"(voff), "s"(r), "s"(8192), "s"(12288), "s"(16384), "s"(20480), "s"(w) : "memory", "scc");
}
__device__ __forceinline__ void load_w2_tail_main(u32x4 (&w2)[22], __amdgpu_buffer_rsrc_t r, u32 voff, int w) {
  asm volatile("s_cmp_lt_u32 %20, 4\n\ts_cbranch_scc0 1f\n\t" W2_LOADS "1:"
               : "+v"(w2[8]), "+v"(w2[9]), "+v"(w2[10]), "+v"(w2[11]), "+v"(w2[12]), "+v"(w2[13]), "+v"(w2[14]), "+v"(w2[15]), "+v"(w2[16]),
                 "+v"(w2[17]), "+v"(w2[18]), "+v"(w2[19]), "+v"(w2[20]), "+v"(w2[21])
               : "v"(voff), "s"(r), "s"(8192), "s"(12288), "s"(16384), "s"(20480), "s"(w) : "memory", "scc");
}
__device__ __forceinline__ float silu_val(float acc, float rs, float bias) {
  const float tt = fmaf(acc, rs, bias);
  const float e = __builtin_amdgcn_exp2f(tt);
  return tt * __builtin_amdgcn_rcpf(fmaf(e, -1.44269504089f, -1.44269504089f));
}

__global__ __launch_bounds__(512, 2) void k34_ffn(char* __restrict__ ws, u16* __restrict__ OUT) {
  __shared__ __attribute__((aligned(16))) char sm[L_TOT];
  const int b = blockIdx.x, t = threadIdx.x, lane = t & 63;
  const int w = __builtin_amdgcn_readfirstlane(t >> 6);
  const int rt = 8 * (b & 3) + ((b >> 3) & 7), c = 4 * ((b >> 2) & 1) + (b >> 6);
  const int n = lane & 15, g = lane >> 4;
  const int row0 = 64 * rt;
  {
    const __amdgpu_buffer_rsrc_t rh = mk_rsrc(ws + WS_HT + ((size_t)(64 * w) * S + row0) * 2, 64 * S * 2);
    const int f0 = 2 * ((lane >> 4) & 1);
    const u32 v0 = (u32)(lane >> 3) * S * 2 + (((lane & 7) ^ f0) << 4), v1 = (u32)(lane >> 3) * S * 2 + (((lane & 7) ^ (f0 + 4)) << 4);
#pragma unroll
    for (int i = 0; i < 8; i++) glds16<false>(rh, (i & 1) ? v1 : v0, i * 8 * S * 2, (u32)(size_t)(sm + L_HT) + (64 * w + 8 * i) * 128);
  }
  if (t < 4) *(volatile LAS u32*)(sm + L_FLG + t * 4) = 0u;
  __builtin_amdgcn_s_barrier();
  const __amdgpu_buffer_rsrc_t rw2 = mk_rsrc(ws + WS_W2T + (size_t)((c * 2 + (w & 1)) * 44 + 11 * (w >> 1)) * 2048, 22 * 1024);
  const int q4 = (lane & 15) >> 2, p4 = lane & 3;
  const int fsw = 2 * ((q4 >> 1) & 1) + 4 * (g & 1);
  auto save_residual = [&]() {
    const char* p = sm + L_HT + (8 * g + q4) * 128 + (((2 * (w >> 1) + (p4 >> 1)) ^ fsw) << 4) + 8 * (p4 & 1) + (2 * c + (w & 1)) * 4096;
    const u32x2 a = tr_read(p), bq = tr_read(p + 512);
    *(u32x4*)(sm + L_RES + (w * 64 + lane) * 16) = u32x4{a[0], a[1], bq[0], bq[1]};
  };
  if (w >= 4) {
    f32x4 csp[4];
    const __amdgpu_buffer_rsrc_t rc = mk_rsrc(ws + WS_CSP + (size_t)c * 176 * 64, 176 * 64);
    const int cj = (w - 4) * 64 + lane;
#pragma unroll
    for (int i = 0; i < 4; i++) csp[i] = __builtin_bit_cast(f32x4, bload16s(rc, (u32)(cj < 176 ? cj : 175) * 64, i * 16));
    asm volatile("s_waitcnt vmcnt(4) lgkmcnt(0)" ::: "memory");
    __builtin_amdgcn_s_barrier();
    __builtin_amdgcn_s_setprio(3);
    const char* p = sm + L_HT + (8 * g + q4) * 128 + (((2 * (w - 4) + (p4 >> 1)) ^ fsw) << 4) + 8 * (p4 & 1);
    u32x2 fa[16][2];
#pragma unroll
    for (int ks = 0; ks < 16; ks++) { fa[ks][0] = tr_read(p + ks * 4096); fa[ks][1] = tr_read(p + ks * 4096 + 512); }
    f32x4 q0 = {0.f, 0.f, 0.f, 0.f}, q1 = {0.f, 0.f, 0.f, 0.f};
    const u32x4 ones = {ONES, ONES, ONES, ONES};
#pragma unroll
    for (int ks = 0; ks < 16; ks++) {
      const u32x4 f0 = {fa[ks][0][0], fa[ks][0][1], fa[ks][1][0], fa[ks][1][1]};
      q0 = mfma(f0, f0, q0);
      q1 = mfma(ones, f0, q1);
    }
    {
      const float lo = (n & 1) ? q0[1] : q0[0], hi = (n & 1) ? q0[3] : q0[2];
      const float q = (n & 2) ? hi : lo;
      const float mu = q1[0] * (1.f / D);
      const float var = fmaxf(q * (1.f / D) - mu * mu, 0.f);
      const float rstd = __builtin_amdgcn_rcpf(rb(__builtin_amdgcn_sqrtf(rb(rb(var) + 1e-5f))));
      if (g == (n >> 2)) *(f32x2*)(sm + L_ST + ((w - 4) * 16 + n) * 8) = f32x2{-1.44269504089f * rstd, rb(mu) * rstd * 1.44269504089f};
    }
    {
      const f32x4 s4 = (csp[0] + csp[1]) + (csp[2] + csp[3]);
      if (cj < 192) *(float*)(sm + L_CS + cj * 4) = (s4[0] + s4[1]) + (s4[2] + s4[3]);
    }
    save_residual();
    asm volatile("s_waitcnt lgkmcnt(0)" ::: "memory");
    if (lane == 0) __hip_atomic_fetch_add((LAS u32*)(sm + L_FLG + 4), 1u, __ATOMIC_RELAXED, __HIP_MEMORY_SCOPE_WORKGROUP);
    __builtin_amdgcn_s_setprio(0);
    lds_wait(sm + L_FLG + 8, 1u);
  } else {
    constexpr int NT = 3, NW1 = 48, PRE = W1_PRE * NT;
    u32x4 w1[NW1];
    const __amdgpu_buffer_rsrc_t rw1 = mk_rsrc(ws + WS_W1T + (size_t)(c * 11 + 3 * w) * 16 * 1024, 48 * 1024);
    auto w1load = [&](int k) { w1[k] = bload16s(rw1, lane * 16, (k % NT) * 16384 + (k / NT) * 1024); };
    auto issued = [&](int ks) constexpr { const int v = PRE + NT * (ks + 1); return v < NW1 ? v : NW1; };
#pragma unroll
    for (int i = 0; i < PRE; i++) w1load(i);
    asm volatile("s_waitcnt vmcnt(%0) lgkmcnt(0)" ::"n"(PRE) : "memory");
    __builtin_amdgcn_s_barrier();
    const char* tb[4];
#pragma unroll
    for (int m = 0; m < 4; m++) tb[m] = sm + L_HT + (8 * g + q4) * 128 + (((2 * m + (p4 >> 1)) ^ fsw) << 4) + 8 * (p4 & 1);
    f32x4 acc[NT][4];
    u32x4 hb[3][4];
#pragma unroll
    for (int i = 0; i < NT; i++)
#pragma unroll
      for (int m = 0; m < 4; m++) acc[i][m] = f32x4{0.f, 0.f, 0.f, 0.f};
#pragma unroll
    for (int d = 0; d < 2; d++)
#pragma unroll
      for (int m = 0; m < 4; m++) {
        const u32x2 a = tr_read(tb[m] + d * 4096), bq = tr_read(tb[m] + d * 4096 + 512);
        hb[d][m] = u32x4{a[0], a[1], bq[0], bq[1]};
      }
#pragma unroll
    for (int ks = 0; ks < 16; ks++) {
#pragma unroll
      for (int m = 0; m < 4; m++) {
        acc[0][m] = mfma(w1[ks * NT], hb[ks % 3][m], acc[0][m]);
        SCHED_FENCE();
        if (ks + 2 < 16) { const u32x2 a = tr_read(tb[m] + (ks + 2) * 4096); hb[(ks + 2) % 3][m][0] = a[0]; hb[(ks + 2) % 3][m][1] = a[1]; }
        SCHED_FENCE();
        acc[1][m] = mfma(w1[ks * NT + 1], hb[ks % 3][m], acc[1][m]);
        SCHED_FENCE();
        if (ks + 2 < 16) { const u32x2 a = tr_read(tb[m] + (ks + 2) * 4096 + 512); hb[(ks + 2) % 3][m][2] = a[0]; hb[(ks + 2) % 3][m][3] = a[1]; }
        SCHED_FENCE();
        acc[2][m] = mfma(w1[ks * NT + 2], hb[ks % 3][m], acc[2][m]);
        SCHED_FENCE();
        if (m < NT && issued(ks - 1) + m < issued(ks)) w1load(issued(ks - 1) + m);
        SCHED_FENCE();
      }
      if (ks == 15 - W1_PRE && w == 0 && lane == 0) *(volatile LAS u32*)(sm + L_FLG + 8) = 1u;
    }
    save_residual();
    lds_wait(sm + L_FLG + 4, 4u);
    float rs[4], mr[4];
    f32x4 cs[NT];
#pragma unroll
    for (int m = 0; m < 4; m++) { const f32x2 v = *(const f32x2*)(sm + L_ST + (m * 16 + n) * 8); rs[m] = v[0]; mr[m] = v[1]; }
#pragma unroll
    for (int i = 0; i < NT; i++) cs[i] = *(const f32x4*)(sm + L_CS + ((3 * w + i) * 16 + 4 * g) * 4);
    const __amdgpu_buffer_rsrc_t hrs = mk_rsrc(ws + WS_HF + (size_t)rt * 44 * 4096, 44 * 4096);
    u32 o_t[NT];
#pragma unroll
    for (int i = 0; i < NT; i++) {
      const int tl = 3 * w + i;
      const int ks2 = tl < 10 ? 5 * c + (tl >> 1) : 40 + (c >> 1), par = tl < 10 ? (tl & 1) : (c & 1);
      o_t[i] = (tl < 11 && (g & 1) == par) ? (u32)(ks2 * 4096 + lane * 16) : 0x40000000u;
    }
#pragma unroll
    for (int m = 0; m < 4; m++) {
#pragma unroll
      for (int i = 0; i < NT; i++) {
        float h1[4];
#pragma unroll
        for (int r = 0; r < 4; r++) h1[r] = silu_val(acc[i][m][r], rs[m], mr[m] * cs[i][r]);
        const u32 p0 = pack_bf(h1[0], h1[1]), p1 = pack_bf(h1[2], h1[3]);
        const auto s0 = __builtin_amdgcn_permlane16_swap(p0, p0, false, false), s1 = __builtin_amdgcn_permlane16_swap(p1, p1, false, false);
        __builtin_amdgcn_raw_buffer_store_b128(u32x4{s0[0], s1[0], s0[1], s1[1]}, hrs, o_t[i] + m * 1024, 0, 16);
      }
      SCHED_FENCE();
    }
  }
  u32x4 w2[22];
#pragma unroll
  for (int i = 0; i < 8; i++) bload16_asm(w2[i], rw2, lane * 16, i * 1024);
  load_w2_tail_support(w2, rw2, lane * 16, w);
  asm volatile("s_cmp_lt_u32 %0, 4\n\ts_cbranch_scc0 1f\n\ts_waitcnt vmcnt(0)\n1:\n\ts_waitcnt lgkmcnt(0)" ::"s"(w) : "memory", "scc");
  __builtin_amdgcn_s_barrier();
  if (w == 7) {
    GAS u32* flags = (GAS u32*)(ws + WS_FLAG);
    if (lane < 32) __hip_atomic_store(flags + b * 64 + lane, 1u, RLX_AGENT);
    const int pb = (b & 3) + (b & 56) + 4 * ((lane >> 2) & 1) + 64 * (lane & 3);
    for (u32 spins = 0; spins < (1u << 20); spins++) {
      const u32 v = __hip_atomic_load(flags + pb * 64, RLX_AGENT);
      if (__all(v == 1u)) break;
      __builtin_amdgcn_s_sleep(1);
    }
  }
  if (w < 4) __builtin_amdgcn_s_sleep(2);
  load_w2_tail_main(w2, rw2, lane * 16, w);
  __builtin_amdgcn_s_barrier();

  const int dh = w & 1, kq = w >> 1;
  const __amdgpu_buffer_rsrc_t rhf = mk_rsrc(ws + WS_HF + (size_t)rt * 44 * 4096 + (size_t)(11 * kq * 4 + 2 * dh) * 1024, 11 * 4096);
  const u32 ldst = (u32)(size_t)sm + (kq * 4 + 2 * dh) * 1024;
  auto stage1 = [&](int s, int e) {
    const int slot = s < 9 ? s : s - 9;
    glds16<true>(rhf, lane * 16, s * 4096 + e * 1024, ldst + slot * 16384 + e * 1024);
  };
  auto stage = [&](int s) { stage1(s, 0); stage1(s, 1); };
  stage(0); stage(1); stage(2); stage(3);
  f32x4 acc2[2][4];
#pragma unroll
  for (int i = 0; i < 2; i++)
#pragma unroll
    for (int m = 0; m < 4; m++) acc2[i][m] = f32x4{0.f, 0.f, 0.f, 0.f};
  asm volatile("s_waitcnt vmcnt(4)"
               : "+v"(w2[0]), "+v"(w2[1]), "+v"(w2[2]), "+v"(w2[3]), "+v"(w2[4]), "+v"(w2[5]), "+v"(w2[6]), "+v"(w2[7]), "+v"(w2[8]), "+v"(w2[9]), "+v"(w2[10]),
                 "+v"(w2[11]), "+v"(w2[12]), "+v"(w2[13]), "+v"(w2[14]), "+v"(w2[15]), "+v"(w2[16]), "+v"(w2[17]), "+v"(w2[18]), "+v"(w2[19]), "+v"(w2[20]), "+v"(w2[21])
               ::"memory");
  __builtin_amdgcn_s_barrier();
  {
    const char* base = sm + kq * 4096 + lane * 16;
    u32x4 hb[2][4];
#pragma unroll
    for (int m = 0; m < 4; m++) hb[0][m] = *(const u32x4*)(base + m * 1024);
#pragma unroll
    for (int s = 0; s < 11; s++) {
      if (s == 1) { asm volatile("s_waitcnt vmcnt(4)" ::: "memory"); __builtin_amdgcn_s_barrier(); }
      else if (s == 3 || s == 5) { asm volatile("s_waitcnt vmcnt(6)" ::: "memory"); __builtin_amdgcn_s_barrier(); }
      else if (s == 7) { asm volatile("s_waitcnt vmcnt(2)" ::: "memory"); __builtin_amdgcn_s_barrier(); }
      else if (s == 9) { asm volatile("s_waitcnt vmcnt(0)" ::: "memory"); __builtin_amdgcn_s_barrier(); }
      const int slot = s + 1 < 9 ? s + 1 : s + 1 - 9;
#pragma unroll
      for (int m = 0; m < 4; m++) {
        acc2[0][m] = mfma(w2[2 * s], hb[s & 1][m], acc2[0][m]);
        SCHED_FENCE();
        if (s < 10) hb[(s + 1) & 1][m] = *(const u32x4*)(base + slot * 16384 + m * 1024);
        SCHED_FENCE();
        acc2[1][m] = mfma(w2[2 * s + 1], hb[s & 1][m], acc2[1][m]);
        SCHED_FENCE();
        if (s < 2) stage1(4 + 2 * s + (m >> 1), m & 1);
        else if (s < 5 && (m & 1)) stage1(s + 6, m >> 1);
        SCHED_FENCE();
      }
    }
  }
  asm volatile("s_waitcnt lgkmcnt(0)" ::: "memory");
  __builtin_amdgcn_s_barrier();
  {
    char* red = sm + L_RED + dh * 32768;
    f32x4 o0, o1;
    auto scatter = [&](auto KQ) {
      constexpr int k = decltype(KQ)::value;
#pragma unroll
      for (int m = 0; m < 4; m++) {
        if (m != k) {
#pragma unroll
          for (int i = 0; i < 2; i++) *(f32x4*)(red + (((k * 4 + m) * 2 + i) * 64 + lane) * 16) = acc2[i][m];
        }
      }
      o0 = acc2[0][k]; o1 = acc2[1][k];
      asm volatile("" : "+v"(o0), "+v"(o1));
    };
    if (kq == 0) scatter(IntC<0>{}); else if (kq == 1) scatter(IntC<1>{}); else if (kq == 2) scatter(IntC<2>{}); else scatter(IntC<3>{});
    __syncthreads();
#pragma unroll
    for (int j = 1; j < 4; j++) {
      const int src = (kq + j) & 3;
      o0 += *(const f32x4*)(red + (((src * 4 + kq) * 2 + 0) * 64 + lane) * 16);
      o1 += *(const f32x4*)(red + (((src * 4 + kq) * 2 + 1) * 64 + lane) * 16);
    }
    const u32x4 hres = *(const u32x4*)(sm + L_RES + (w * 64 + lane) * 16);
    u32x4 res;
#pragma unroll
    for (int r = 0; r < 4; r++) res[r] = pack_bf(bf_lo(hres[r]) + rb(o0[r]), bf_hi(hres[r]) + rb(o1[r]));
    __builtin_amdgcn_raw_buffer_store_b128(res, mk_rsrc(OUT, S * D * 2), (u32)((row0 + 16 * kq + n) * D + 64 * c + 32 * dh + 8 * g) * 2, 0, 2);
  }
  asm volatile("ffn_text_end:");
}

extern "C" void kernel_launch(void* const* d_in, const int* in_sizes, int n_in, void* d_out, int out_size, void* d_ws,
                              size_t ws_size, hipStream_t stream) {
  const u16* X = (const u16*)d_in[0];
  const u16* K = (const u16*)d_in[1];
  const u16* V = (const u16*)d_in[2];
  const u16* SK = (const u16*)d_in[3];
  const u16* HK = (const u16*)d_in[4];
  const u16* DV = (const u16*)d_in[5];
  const u16* W1 = (const u16*)d_in[7];
  const u16* W2 = (const u16*)d_in[8];
  char* ws = (char*)d_ws;
  k1_prep<<<256, 512, 0, stream>>>(X, K, V, SK, HK, ws);
  k2_conv<<<256, 512, 0, stream>>>(HK, DV, W1, W2, ws);
  k34_ffn<<<256, 512, 0, stream>>>(ws, (u16*)d_out);
}
```

```cpp
#include <hip/hip_runtime.h>
#include <stdint.h>

typedef unsigned short u16;
typedef unsigned int u32;
typedef unsigned long long u64;
typedef __bf16 bf16x8 __attribute__((ext_vector_type(8)));
typedef __bf16 bf16x2 __attribute__((ext_vector_type(2)));
typedef short s16x4 __attribute__((ext_vector_type(4)));
typedef float f32x4 __attribute__((ext_vector_type(4)));
typedef float f32x2 __attribute__((ext_vector_type(2)));
typedef u32 u32x4 __attribute__((ext_vector_type(4)));
typedef u32 u32x2 __attribute__((ext_vector_type(2)));
#define GAS __attribute__((address_space(1)))
#define LAS __attribute__((address_space(3)))
#define RLX_AGENT __ATOMIC_RELAXED, __HIP_MEMORY_SCOPE_AGENT
#define SCHED_FENCE() __builtin_amdgcn_sched_barrier(0)

constexpr int S = 2048, D = 512, F = 1376;

constexpr size_t WS_FLAG = 0;
constexpr size_t WS_KVT  = 1 << 16;
constexpr size_t WS_XT   = WS_KVT + (size_t)D * S * 2;
constexpr size_t WS_HT   = WS_XT + (size_t)D * S * 2;
constexpr size_t WS_W1T  = WS_HT + (size_t)D * S * 2;
constexpr size_t WS_W2T  = WS_W1T + 8 * 11 * 16 * 1024;
constexpr size_t WS_CSP  = WS_W2T + 8 * 4 * 44 * 1024;
constexpr size_t WS_ST1  = WS_CSP + 16 * 8 * 11 * 16 * 4;
constexpr size_t WS_HF   = WS_ST1 + S * 8;
constexpr size_t WS_END  = WS_HF + (size_t)32 * 44 * 4 * 1024;

__device__ __forceinline__ float bf_lo(u32 v) { return __uint_as_float(v << 16); }
__device__ __forceinline__ float bf_hi(u32 v) { return __uint_as_float(v & 0xffff0000u); }
__device__ __forceinline__ u32 pack_bf(float a, float b) {
  f32x2 z = {a, b};
  return __builtin_bit_cast(u32, __builtin_convertvector(z, bf16x2));
}
__device__ __forceinline__ float rb(float a) { return bf_lo(pack_bf(a, 0.f)); }
__device__ __forceinline__ float dot2(u32 a, u32 b, float c) {
  return __builtin_amdgcn_fdot2_f32_bf16(__builtin_bit_cast(bf16x2, a), __builtin_bit_cast(bf16x2, b), c, false);
}
__device__ __forceinline__ u32 perm_lo(u32 a, u32 b) { return __builtin_amdgcn_perm(b, a, 0x05040100u); }
__device__ __forceinline__ u32 perm_hi(u32 a, u32 b) { return __builtin_amdgcn_perm(b, a, 0x07060302u); }
__device__ __forceinline__ f32x4 mfma(u32x4 a, u32x4 b, f32x4 c) {
  return __builtin_amdgcn_mfma_f32_16x16x32_bf16(__builtin_bit_cast(bf16x8, a), __builtin_bit_cast(bf16x8, b), c, 0, 0, 0);
}
constexpr u32 ONES = 0x3F803F80u;
template <int V> struct IntC { static constexpr int value = V; };
__device__ __forceinline__ float sum_g(float x) {
  auto s = __builtin_amdgcn_permlane16_swap(__float_as_uint(x), __float_as_uint(x), false, false);
  x = __uint_as_float(s[0]) + __uint_as_float(s[1]);
  auto t = __builtin_amdgcn_permlane32_swap(__float_as_uint(x), __float_as_uint(x), false, false);
  return __uint_as_float(t[0]) + __uint_as_float(t[1]);
}
template <int CTRL>
__device__ __forceinline__ float dpp_add(float x) {
  return x + __uint_as_float(__builtin_amdgcn_update_dpp(0u, __float_as_uint(x), CTRL, 0xf, 0xf, true));
}
__device__ __forceinline__ float wave_sum(float x) {
  x = dpp_add<0xB1>(x);
  x = dpp_add<0x4E>(x);
  x = dpp_add<0x141>(x);
  x = dpp_add<0x140>(x);
  return sum_g(x);
}
__device__ __forceinline__ __amdgpu_buffer_rsrc_t mk_rsrc(const void* p, u32 bytes) {
  return __builtin_amdgcn_make_buffer_rsrc((void*)p, 0, bytes, 0x00020000);
}
__device__ __forceinline__ u32x4 bload16(__amdgpu_buffer_rsrc_t r, u32 off) { return __builtin_amdgcn_raw_buffer_load_b128(r, off, 0, 0); }
__device__ __forceinline__ u32x2 bload8(__amdgpu_buffer_rsrc_t r, u32 off) { return __builtin_amdgcn_raw_buffer_load_b64(r, off, 0, 0); }
template <bool SC1>
__device__ __forceinline__ void glds16(__amdgpu_buffer_rsrc_t r, u32 voff, u32 soff, u32 lds_dst) {
  if (SC1)
    asm volatile("s_mov_b32 m0, %0\n\ts_nop 0\n\tbuffer_load_dwordx4 %1, %2, %3 offen sc1 lds" ::"s"(lds_dst), "v"(voff), "s"(r), "s"(soff) : "memory");
  else
    asm volatile("s_mov_b32 m0, %0\n\ts_nop 0\n\tbuffer_load_dwordx4 %1, %2, %3 offen lds" ::"s"(lds_dst), "v"(voff), "s"(r), "s"(soff) : "memory");
}
__device__ __forceinline__ void bload16_asm(u32x4& d, __amdgpu_buffer_rsrc_t r, u32 voff, u32 soff) {
  asm volatile("buffer_load_dwordx4 %0, %1, %2, %3 offen" : "=v"(d) : "v"(voff), "s"(r), "s"(soff) : "memory");
}
#define K1_AUX 0
#define WT_AUX 16
#define K2_AUX 16
template <int AUX>
__device__ __forceinline__ void gstore16(char* base, size_t off, u32x4 v) {
  const __amdgpu_buffer_rsrc_t r = mk_rsrc(base, 0x7fffffffu);
  __builtin_amdgcn_raw_buffer_store_b128(v, r, (u32)off, 0, AUX);
}
template <int AUX>
__device__ __forceinline__ void gstore8(char* base, size_t off, u32x2 v) {
  const __amdgpu_buffer_rsrc_t r = mk_rsrc(base, 0x7fffffffu);
  __builtin_amdgcn_raw_buffer_store_b64(v, r, (u32)off, 0, AUX);
}
__device__ __forceinline__ u32x4 bload16s(__amdgpu_buffer_rsrc_t r, u32 voff, u32 soff) { return __builtin_amdgcn_raw_buffer_load_b128(r, voff, soff, 0); }
__device__ __forceinline__ u32x2 tr_read(const char* p) {
  s16x4 r = __builtin_amdgcn_ds_read_tr16_b64_v4i16((LAS s16x4*)p);
  return __builtin_bit_cast(u32x2, r);
}

__device__ __forceinline__ void code_prefetch(int part, int nparts, int lane) {
  u64 pc; u32 len;
  asm volatile("s_getpc_b64 %0\n\ts_mov_b32 %1, ffn_text_end-." : "=s"(pc), "=s"(len));
  const __amdgpu_buffer_rsrc_t r = mk_rsrc((const void*)pc, len);
  u32 acc = 0;
  for (u32 o = part * 1024; o < len; o += nparts * 1024) acc |= __builtin_amdgcn_raw_buffer_load_b128(r, o + lane * 16, 0, 0)[0];
  asm volatile("" ::"v"(acc));
}
__device__ __forceinline__ int fcol(int ks, int g, int j) {
  const int slot = 8 * (g >> 1) + j;
  if (ks < 40) { const int ct = ks / 5, p = ks - 5 * ct; return 128 * (2 * p + (g & 1)) + 8 * slot + ct; }
  return 1280 + 8 * slot + 2 * (ks - 40) + (g & 1);
}

__global__ __launch_bounds__(512, 2) void k1_prep(const u16* __restrict__ X, const u16* __restrict__ K, const u16* __restrict__ V,
                                                  const u16* __restrict__ SK, const u16* __restrict__ HK, char* __restrict__ ws) {
  __shared__ __attribute__((aligned(16))) char sm[16384 + 4096];
  const int b = blockIdx.x, t = threadIdx.x, lane = t & 63;
  const int w = __builtin_amdgcn_readfirstlane(t >> 6);
  const int dt = b & 7, st = b >> 3;
  if (w < 4) {
    const int rp = t >> 3, c = t & 7;
    const int s = st * 64 + 2 * rp, ch0 = dt * 64 + c * 8;
    const __amdgpu_buffer_rsrc_t rk = mk_rsrc(K, S * D * 2);
    const u32 off = (u32)(s * D + ch0) * 2;
    u32x4 kk[5];
#pragma unroll
    for (int i = 0; i < 5; i++) kk[i] = bload16(rk, off + (1 - i) * 1024);
    const u32x4 v0 = *(const u32x4*)((const char*)V + off), v1 = *(const u32x4*)((const char*)V + off + 1024);
    const u32x4 x0 = *(const u32x4*)((const char*)X + off), x1 = *(const u32x4*)((const char*)X + off + 1024);
    u32x4 wa[4];
#pragma unroll
    for (int i = 0; i < 4; i++) wa[i] = *(const u32x4*)(SK + ch0 * 4 + i * 8);
    SCHED_FENCE();
    const int swz = ((rp >> 2) ^ c) << 4;
    char* pk = sm + (8 * c) * 128 + swz + ((3 - (rp & 3)) << 2);
    char* px = sm + 8192 + (8 * c) * 128 + swz + ((rp & 3) << 2);
#pragma unroll
    for (int i = 0; i < 4; i++) {
      const u32 l01 = perm_lo(kk[0][i], kk[1][i]), l12 = perm_lo(kk[1][i], kk[2][i]), l23 = perm_lo(kk[2][i], kk[3][i]), l34 = perm_lo(kk[3][i], kk[4][i]);
      const u32 h01 = perm_hi(kk[0][i], kk[1][i]), h12 = perm_hi(kk[1][i], kk[2][i]), h23 = perm_hi(kk[2][i], kk[3][i]), h34 = perm_hi(kk[3][i], kk[4][i]);
      const float a1 = dot2(l01, wa[i][0], dot2(l23, wa[i][1], 0.f)) * bf_lo(v1[i]);
      const float a0 = dot2(l12, wa[i][0], dot2(l34, wa[i][1], 0.f)) * bf_lo(v0[i]);
      const float b1 = dot2(h01, wa[i][2], dot2(h23, wa[i][3], 0.f)) * bf_hi(v1[i]);
      const float b0 = dot2(h12, wa[i][2], dot2(h34, wa[i][3], 0.f)) * bf_hi(v0[i]);
      *(u32*)(pk + (2 * i) * 128) = pack_bf(a1, a0);
      *(u32*)(pk + (2 * i + 1) * 128) = pack_bf(b1, b0);
      *(u32*)(px + (2 * i) * 128) = perm_lo(x0[i], x1[i]);
      *(u32*)(px + (2 * i + 1) * 128) = perm_hi(x0[i], x1[i]);
    }
    __syncthreads();
#pragma unroll
    for (int i = 0; i < 2; i++) {
      const int u = t + 256 * i, ch = u >> 3, cc = u & 7;
      const u32x4 o = *(const u32x4*)(sm + ch * 128 + cc * 16);
      const int grp = cc ^ ((ch >> 3) & 7);
      const size_t go = (size_t)(dt * 64 + ch) * S + st * 64 + grp * 8;
      gstore16<K1_AUX>(ws, WS_KVT + go * 2, o);
      gstore16<K1_AUX>(ws, WS_XT + go * 2, *(const u32x4*)(sm + 8192 + ch * 128 + cc * 16));
    }
  } else {
    const int lrow = 256 * (b & 7) + 8 * (b >> 3) + 2 * (w - 4);
    u32x4 xr[2];
#pragma unroll
    for (int i = 0; i < 2; i++) xr[i] = *(const u32x4*)(X + (size_t)(lrow + i) * D + lane * 8);
    if (w == 4 && lane < 32) __hip_atomic_store((GAS u32*)(ws + WS_FLAG) + b * 64 + lane, 0u, RLX_AGENT);
    {
      const u32 dump = (u32)(size_t)(sm + 16384) + (w - 4) * 1024;
      const int chp = 64 * (b & 7) + 2 * (b >> 3);
      const __amdgpu_buffer_rsrc_t rh = mk_rsrc(HK + (size_t)chp * S, 2 * S * 2);
      glds16<false>(rh, lane * 16, (w - 4) * 2048, dump);
      glds16<false>(rh, lane * 16, (w - 4) * 2048 + 1024, dump);
    }
    code_prefetch((b >> 3) * 4 + (w - 4), 128, lane);
    __builtin_amdgcn_s_barrier();
#pragma unroll
    for (int q = 0; q < 2; q++) {
      float sum = 0.f;
#pragma unroll
      for (int i = 0; i < 4; i++) sum = dot2(xr[q][i], ONES, sum);
      const float mean = wave_sum(sum) * (1.f / D);
      float sq = 0.f;
#pragma unroll
      for (int i = 0; i < 4; i++) { const float d0 = bf_lo(xr[q][i]) - mean, d1 = bf_hi(xr[q][i]) - mean; sq += d0 * d0 + d1 * d1; }
      sq = wave_sum(sq);
      const float sd = rb(__builtin_amdgcn_sqrtf(rb(rb(sq * (1.f / D)) + 1e-5f)));
      const float r1 = __builtin_amdgcn_rcpf(sd);
      if (lane == 0) { f32x2 o2 = {r1, -rb(mean) * r1}; *(f32x2*)(ws + WS_ST1 + (size_t)(lrow + q) * 8) = o2; }
    }
    asm volatile("s_waitcnt vmcnt(0)" ::: "memory");
  }
}

constexpr int HC_STRIDE = 4608;
constexpr int HC_CH = 8 * HC_STRIDE;
constexpr int U_CH = (240 + 2048) * 2;
constexpr int K2_H = 0, K2_U = 2 * HC_CH, K2_R = K2_U + 2 * U_CH + 32, K2_F = K2_R + 4 * 4096, K2_D = K2_F + 32, K2_END = K2_D + 256;
__device__ __forceinline__ constexpr int hoff(int c) {
  return c == 0 ? 4 : c == 1 ? 2 : c == 2 ? 8 : c == 3 ? 3 : c == 4 ? 7 : c == 5 ? 0 : c == 6 ? 15 : 12;
}
__device__ __forceinline__ int hoff_rt(int c) { return (0xCF073824u >> (4 * c)) & 15; }
__device__ __forceinline__ void lds_wait(const char* p, u32 want) {
  for (u32 spins = 0; spins < (1u << 22); spins++) {
    if (__builtin_amdgcn_readfirstlane(*(volatile LAS u32*)p) == want) break;
    __builtin_amdgcn_s_sleep(1);
  }
  asm volatile("" ::: "memory");
}

__global__ __launch_bounds__(512, 2) void k2_conv(const u16* __restrict__ HK, const u16* __restrict__ DV,
                                                  const u16* __restrict__ W1, const u16* __restrict__ W2, char* __restrict__ ws, const u16* __restrict__ OUT) {
  __shared__ __attribute__((aligned(16))) char sm[K2_END];
  const int b = blockIdx.x, t = threadIdx.x, lane = t & 63;
  const int w = __builtin_amdgcn_readfirstlane(t >> 6);
  const int chp = 64 * (b & 7) + 2 * (b >> 3);
  const int wcl = (w >> 1) & 1, rh = w & 1;
  const int n = lane & 15, g = lane >> 4;
  {
    const int cl = t >> 8, j = t & 255;
    const __amdgpu_buffer_rsrc_t rh_ = mk_rsrc(HK + (size_t)chp * S, 2 * S * 2);
    const u32 ho = (u32)(cl * S + 8 * j) * 2;
    u32x4 ha, hb, uu;
    const __amdgpu_buffer_rsrc_t ru = mk_rsrc(ws + WS_KVT + (size_t)chp * S * 2, 2 * S * 2);
    bload16_asm(ha, rh_, ho - 16, 0);
    bload16_asm(hb, rh_, ho, 0);
    bload16_asm(uu, ru, ho, 0);
    asm volatile("s_waitcnt vmcnt(0)" : "+v"(ha), "+v"(hb), "+v"(uu)::"memory");
    if (t < 8) *(volatile LAS u32*)(sm + K2_F + t * 4) = 0u;
    SCHED_FENCE();
    if (j == 0) ha = u32x4{0, 0, 0, 0};
    char* hbase = sm + K2_H + cl * HC_CH;
    const u32 W[8] = {ha[0], ha[1], ha[2], ha[3], hb[0], hb[1], hb[2], hb[3]};
#pragma unroll
    for (int c = 0; c < 8; c++) {
      u32x4 o;
#pragma unroll
      for (int a = 0; a < 4; a++) o[a] = (c & 1) ? __builtin_amdgcn_alignbit(W[(c >> 1) + a + 1], W[(c >> 1) + a], 16) : W[(c >> 1) + a];
      *(u32x4*)(hbase + c * HC_STRIDE + 16 * (j + 1 + hoff(c))) = o;
    }
    const u32x4 z4 = {0, 0, 0, 0};
    if (j < 8) *(u32x4*)(hbase + j * HC_STRIDE + 16 * hoff_rt(j)) = z4;
    if (j == 255) *(u32x4*)(hbase + 16 * (257 + hoff(0))) = hb;
    char* ub = sm + K2_U + cl * U_CH;
    *(u32x4*)(ub + 480 + 16 * j) = uu;
    if (j < 30) *(u32x4*)(ub + 16 * j) = z4;
  }
  u32x2 xq[4];
  f32x4 sa[4], sb[4];
  u32 dvr = 0;
  if (w < 4) {
    const __amdgpu_buffer_rsrc_t rs1 = mk_rsrc(ws + WS_ST1, S * 8);
    const __amdgpu_buffer_rsrc_t rx = mk_rsrc(ws + WS_XT + (size_t)(chp + wcl) * S * 2, S * 2);
    const u32 p0 = 1024 * rh + 16 * n + 4 * g;
#pragma unroll
    for (int i = 0; i < 4; i++) {
      asm volatile("buffer_load_dwordx2 %0, %1, %2, %3 offen" : "=v"(xq[i]) : "v"(p0 * 2), "s"(rx), "s"(i * 512) : "memory");
      asm volatile("buffer_load_dwordx4 %0, %1, %2, %3 offen" : "=v"(sa[i]) : "v"(p0 * 8), "s"(rs1), "s"(i * 2048) : "memory");
      asm volatile("buffer_load_dwordx4 %0, %1, %2, %3 offen offset:16" : "=v"(sb[i]) : "v"(p0 * 8), "s"(rs1), "s"(i * 2048) : "memory");
    }
    dvr = *(const u32*)(DV + chp) >> (16 * wcl);
  }
  __syncthreads();
  if (w < 4) {
    f32x4 acc[8];
#pragma unroll
    for (int i = 0; i < 8; i++) acc[i] = f32x4{0.f, 0.f, 0.f, 0.f};
    const int idx0 = 25 + n - 8 * g;
    const int c = idx0 & 7;
    const char* fbase = sm + K2_H + wcl * HC_CH + c * HC_STRIDE + 16 * ((idx0 >> 3) + hoff_rt(c)) + 64 * 4 * rh;
    const char* gbase = sm + K2_U + wcl * U_CH + 2 * (16 * n + 8 * g + 224) - 64 * 4 * rh;
    u32x4 Ff[2][8], Gf[2][8];
#pragma unroll
    for (int a = 0; a < 8; a++) { Gf[0][a] = *(const u32x4*)(gbase + 64 * (8 * a)); Ff[0][a] = *(const u32x4*)(fbase + 64 * (8 * a)); }
    SCHED_FENCE();
#pragma unroll
    for (int rr = 0; rr < 4; rr++) {
      const int cur = rr & 1, nxt = cur ^ 1;
      int m = 0;
#pragma unroll
      for (int bb = 0; bb < 8; bb++) {
#pragma unroll
        for (int a = 0; a + bb < 8; a++) {
          acc[a + bb] = mfma(Ff[cur][a], Gf[cur][bb], acc[a + bb]);
          if (rr < 3 && (m & 1) == 1 && m < 32) {
            const int q = m >> 2;
            SCHED_FENCE();
            if (m & 2) Ff[nxt][q] = *(const u32x4*)(fbase + 64 * (8 * q + rr + 1));
            else Gf[nxt][q] = *(const u32x4*)(gbase + 64 * (8 * q - rr - 1));
            SCHED_FENCE();
          }
          m++;
        }
      }
      SCHED_FENCE();
    }
    f32x4* rbuf = (f32x4*)(sm + K2_R) + w * 256;
    if (rh) {
#pragma unroll
      for (int i = 0; i < 4; i++) rbuf[i * 64 + lane] = acc[i];
#pragma unroll
      for (int i = 0; i < 4; i++) acc[i] = acc[4 + i];
    } else {
#pragma unroll
      for (int i = 0; i < 4; i++) rbuf[i * 64 + lane] = acc[4 + i];
    }
    asm volatile("s_waitcnt lgkmcnt(0)" ::: "memory");
    if (lane == 0) *(volatile LAS u32*)(sm + K2_F + w * 4) = 1u;
    const int ch = chp + wcl;
    const char* ub = sm + K2_U + wcl * U_CH;
    u32x2 uq[4];
#pragma unroll
    for (int i = 0; i < 4; i++) uq[i] = *(const u32x2*)(ub + 2 * (240 + 256 * (4 * rh + i) + 16 * n + 8 * (g >> 1) + ((g & 1) ? 0 : 4)));
    asm volatile("s_waitcnt vmcnt(0)" : "+v"(xq[0]), "+v"(xq[1]), "+v"(xq[2]), "+v"(xq[3]), "+v"(sa[0]), "+v"(sa[1]), "+v"(sa[2]), "+v"(sa[3]),
                 "+v"(sb[0]), "+v"(sb[1]), "+v"(sb[2]), "+v"(sb[3])::"memory");
    lds_wait(sm + K2_F + (w ^ 1) * 4, 1u);
    const float dv = bf_lo(dvr);
    const f32x4* pbuf = (const f32x4*)(sm + K2_R) + (w ^ 1) * 256;
#pragma unroll
    for (int i = 0; i < 4; i++) {
      const int T = 4 * rh + i;
      const f32x4 a = acc[i] + pbuf[i * 64 + lane];
      const int pos = 256 * T + 16 * n + 4 * g;
      const float y0 = fmaf(bf_hi(uq[i][1]), dv, a[0]), y1 = fmaf(bf_lo(uq[i][1]), dv, a[1]), y2 = fmaf(bf_hi(uq[i][0]), dv, a[2]), y3 = fmaf(bf_lo(uq[i][0]), dv, a[3]);
      const float x0 = bf_lo(xq[i][0]), x1 = bf_hi(xq[i][0]), x2 = bf_lo(xq[i][1]), x3 = bf_hi(xq[i][1]);
      const float h0 = fmaf(fmaf(x0, sa[i][0], sa[i][1]), y0, x0), h1 = fmaf(fmaf(x1, sa[i][2], sa[i][3]), y1, x1);
      const float h2 = fmaf(fmaf(x2, sb[i][0], sb[i][1]), y2, x2), h3 = fmaf(fmaf(x3, sb[i][2], sb[i][3]), y3, x3);
      const u32x2 o = {pack_bf(h0, h1), pack_bf(h2, h3)};
      gstore8<K2_AUX>(ws, WS_HT + ((size_t)ch * S + pos) * 2, o);
    }
  } else {
    if (w == 7) {
      const char* p = lane < 44 ? ws + WS_HF + (size_t)(8 * (b & 3)) * 44 * 4096 + (size_t)lane * 32768
                                : (const char*)OUT + (size_t)(512 * (b & 3)) * D * 2 + (size_t)((lane - 44) & 15) * 32768;
      asm volatile("s_mov_b32 m0, %0\n\ts_nop 0\n\tglobal_load_lds_dword %1, off" ::"s"((u32)(size_t)(sm + K2_D)), "v"(p) : "memory");
    }
    const int unit = b + 256 * (w - 4);
    const int lc = lane & 15, lg = lane >> 4;
    if (unit < 176) {
      const __amdgpu_buffer_rsrc_t rw = mk_rsrc(W1, D * F * 2);
      const int blk = unit >> 4, ks = unit & 15;
      const int col = 128 * blk + 8 * lc;
      const u32 o0 = col < F ? (u32)((32 * ks + 8 * lg) * F + col) * 2 : 0x80000000u;
      u32x4 wl[8];
#pragma unroll
      for (int j = 0; j < 8; j++) wl[j] = bload16(rw, o0 + j * F * 2);
#pragma unroll
      for (int i = 0; i < 8; i++) {
        u32x4 o;
#pragma unroll
        for (int a = 0; a < 4; a++) o[a] = (i & 1) ? perm_hi(wl[2 * a][i >> 1], wl[2 * a + 1][i >> 1]) : perm_lo(wl[2 * a][i >> 1], wl[2 * a + 1][i >> 1]);
        gstore16<WT_AUX>(ws, WS_W1T + ((size_t)((i * 11 + blk) * 16 + ks) * 64 + lane) * 16, o);
        const float cs = sum_g(dot2(o[0], ONES, dot2(o[1], ONES, dot2(o[2], ONES, dot2(o[3], ONES, 0.f)))));
        if (lane < 16) *(float*)(ws + WS_CSP + ((size_t)(i * 176 + blk * 16 + lane) * 16 + ks) * 4) = cs;
      }
    } else if (unit < 528) {
      const __amdgpu_buffer_rsrc_t rw = mk_rsrc(W2, D * F * 2);
      const int u2 = unit - 176, ks = u2 >> 3, c2 = u2 & 7;
      u32 wx[8], wy[8];
#pragma unroll
      for (int j = 0; j < 8; j++) {
        const u32 o = (u32)(fcol(ks, lg, j) * D + 64 * c2 + 8 * (lc >> 2) + 2 * (lc & 3)) * 2;
        wx[j] = __builtin_amdgcn_raw_buffer_load_b32(rw, o, 0, 0);
        wy[j] = __builtin_amdgcn_raw_buffer_load_b32(rw, o + 64, 0, 0);
      }
#pragma unroll
      for (int i = 0; i < 4; i++) {
        u32x4 o;
#pragma unroll
        for (int a = 0; a < 4; a++) {
          const u32 e0 = (i >> 1) ? wy[2 * a] : wx[2 * a], e1 = (i >> 1) ? wy[2 * a + 1] : wx[2 * a + 1];
          o[a] = (i & 1) ? perm_hi(e0, e1) : perm_lo(e0, e1);
        }
        gstore16<WT_AUX>(ws, WS_W2T + ((size_t)(((c2 * 2 + (i >> 1)) * 44 + ks) * 2 + (i & 1)) * 64 + lane) * 16, o);
      }
    }
    asm volatile("s_waitcnt vmcnt(0)" ::: "memory");
  }
}

constexpr int L_HT = 0;
constexpr int L_ST = 147456;
constexpr int L_CS = L_ST + 512;
constexpr int L_RES = L_CS + 768;
constexpr int L_END = L_RES + 8192;
constexpr int L_RED = 0;
constexpr int L_FLG = L_END;
constexpr int L_TOT = L_END + 16;
#define W1_PRE 4
#define W2_LOADS                                                                                                                   \
  "buffer_load_dwordx4 %0, %14, %15, %16 offen\n\tbuffer_load_dwordx4 %1, %14, %15, %16 offen offset:1024\n\t"                      \
  "buffer_load_dwordx4 %2, %14, %15, %16 offen offset:2048\n\tbuffer_load_dwordx4 %3, %14, %15, %16 offen offset:3072\n\t"          \
  "buffer_load_dwordx4 %4, %14, %15, %17 offen\n\tbuffer_load_dwordx4 %5, %14, %15, %17 offen offset:1024\n\t"                      \
  "buffer_load_dwordx4 %6, %14, %15, %17 offen offset:2048\n\tbuffer_load_dwordx4 %7, %14, %15, %17 offen offset:3072\n\t"          \
  "buffer_load_dwordx4 %8, %14, %15, %18 offen\n\tbuffer_load_dwordx4 %9, %14, %15, %18 offen offset:1024\n\t"                      \
  "buffer_load_dwordx4 %10, %14, %15, %18 offen offset:2048\n\tbuffer_load_dwordx4 %11, %14, %15, %18 offen offset:3072\n\t"        \
  "buffer_load_dwordx4 %12, %14, %15, %19 offen\n\tbuffer_load_dwordx4 %13, %14, %15, %19 offen offset:1024\n"
__device__ __forceinline__ void load_w2_tail_support(u32x4 (&w2)[22], __amdgpu_buffer_rsrc_t r, u32 voff, int w) {
  asm volatile("s_cmp_lt_u32 %20, 4\n\ts_cbranch_scc1 1f\n\t" W2_LOADS "1:"
               : "=&v"(w2[8]), "=&v"(w2[9]), "=&v"(w2[10]), "=&v"(w2[11]), "=&v"(w2[12]), "=&v"(w2[13]), "=&v"(w2[14]), "=&v"(w2[15]), "=&v"(w2[16]),
                 "=&v"(w2[17]), "=&v"(w2[18]), "=&v"(w2[19]), "=&v"(w2[20]), "=&v"(w2[21])
               : "v"(voff), "s"(r), "s"(8192), "s"(12288), "s"(16384), "s"(20480), "s"(w) : "memory", "scc");
}
__device__ __forceinline__ void load_w2_tail_main(u32x4 (&w2)[22], __amdgpu_buffer_rsrc_t r, u32 voff, int w) {
  asm volatile("s_cmp_lt_u32 %20, 4\n\ts_cbranch_scc0 1f\n\t" W2_LOADS "1:"
               : "+v"(w2[8]), "+v"(w2[9]), "+v"(w2[10]), "+v"(w2[11]), "+v"(w2[12]), "+v"(w2[13]), "+v"(w2[14]), "+v"(w2[15]), "+v"(w2[16]),
                 "+v"(w2[17]), "+v"(w2[18]), "+v"(w2[19]), "+v"(w2[20]), "+v"(w2[21])
               : "v"(voff), "s"(r), "s"(8192), "s"(12288), "s"(16384), "s"(20480), "s"(w) : "memory", "scc");
}
__device__ __forceinline__ float silu_val(float acc, float rs, float bias) {
  const float tt = fmaf(acc, rs, bias);
  const float e = __builtin_amdgcn_exp2f(tt);
  return tt * __builtin_amdgcn_rcpf(fmaf(e, -1.44269504089f, -1.44269504089f));
}

__global__ __launch_bounds__(512, 2) void k34_ffn(char* __restrict__ ws, u16* __restrict__ OUT) {
  __shared__ __attribute__((aligned(16))) char sm[L_TOT];
  const int b = blockIdx.x, t = threadIdx.x, lane = t & 63;
  const int w = __builtin_amdgcn_readfirstlane(t >> 6);
  const int rt = 8 * (b & 3) + ((b >> 3) & 7), c = 4 * ((b >> 2) & 1) + (b >> 6);
  const int n = lane & 15, g = lane >> 4;
  const int row0 = 64 * rt;
  {
    const __amdgpu_buffer_rsrc_t rh = mk_rsrc(ws + WS_HT + ((size_t)(64 * w) * S + row0) * 2, 64 * S * 2);
    const int f0 = 2 * ((lane >> 4) & 1);
    const u32 v0 = (u32)(lane >> 3) * S * 2 + (((lane & 7) ^ f0) << 4), v1 = (u32)(lane >> 3) * S * 2 + (((lane & 7) ^ (f0 + 4)) << 4);
#pragma unroll
    for (int i = 0; i < 8; i++) glds16<false>(rh, (i & 1) ? v1 : v0, i * 8 * S * 2, (u32)(size_t)(sm + L_HT) + (64 * w + 8 * i) * 128);
  }
  if (t < 4) *(volatile LAS u32*)(sm + L_FLG + t * 4) = 0u;
  __builtin_amdgcn_s_barrier();
  const __amdgpu_buffer_rsrc_t rw2 = mk_rsrc(ws + WS_W2T + (size_t)((c * 2 + (w & 1)) * 44 + 11 * (w >> 1)) * 2048, 22 * 1024);
  const int q4 = (lane & 15) >> 2, p4 = lane & 3;
  const int fsw = 2 * ((q4 >> 1) & 1) + 4 * (g & 1);
  auto save_residual = [&]() {
    const char* p = sm + L_HT + (8 * g + q4) * 128 + (((2 * (w >> 1) + (p4 >> 1)) ^ fsw) << 4) + 8 * (p4 & 1) + (2 * c + (w & 1)) * 4096;
    const u32x2 a = tr_read(p), bq = tr_read(p + 512);
    *(u32x4*)(sm + L_RES + (w * 64 + lane) * 16) = u32x4{a[0], a[1], bq[0], bq[1]};
  };
  if (w >= 4) {
    f32x4 csp[4];
    const __amdgpu_buffer_rsrc_t rc = mk_rsrc(ws + WS_CSP + (size_t)c * 176 * 64, 176 * 64);
    const int cj = (w - 4) * 64 + lane;
#pragma unroll
    for (int i = 0; i < 4; i++) csp[i] = __builtin_bit_cast(f32x4, bload16s(rc, (u32)(cj < 176 ? cj : 175) * 64, i * 16));
    asm volatile("s_waitcnt vmcnt(4) lgkmcnt(0)" ::: "memory");
    __builtin_amdgcn_s_barrier();
    __builtin_amdgcn_s_setprio(3);
    const char* p = sm + L_HT + (8 * g + q4) * 128 + (((2 * (w - 4) + (p4 >> 1)) ^ fsw) << 4) + 8 * (p4 & 1);
    u32x2 fa[16][2];
#pragma unroll
    for (int ks = 0; ks < 16; ks++) { fa[ks][0] = tr_read(p + ks * 4096); fa[ks][1] = tr_read(p + ks * 4096 + 512); }
    f32x4 q0 = {0.f, 0.f, 0.f, 0.f}, q1 = {0.f, 0.f, 0.f, 0.f};
    const u32x4 ones = {ONES, ONES, ONES, ONES};
#pragma unroll
    for (int ks = 0; ks < 16; ks++) {
      const u32x4 f0 = {fa[ks][0][0], fa[ks][0][1], fa[ks][1][0], fa[ks][1][1]};
      q0 = mfma(f0, f0, q0);
      q1 = mfma(ones, f0, q1);
    }
    {
      const float lo = (n & 1) ? q0[1] : q0[0], hi = (n & 1) ? q0[3] : q0[2];
      const float q = (n & 2) ? hi : lo;
      const float mu = q1[0] * (1.f / D);
      const float var = fmaxf(q * (1.f / D) - mu * mu, 0.f);
      const float rstd = __builtin_amdgcn_rcpf(rb(__builtin_amdgcn_sqrtf(rb(rb(var) + 1e-5f))));
      if (g == (n >> 2)) *(f32x2*)(sm + L_ST + ((w - 4) * 16 + n) * 8) = f32x2{-1.44269504089f * rstd, rb(mu) * rstd * 1.44269504089f};
    }
    {
      const f32x4 s4 = (csp[0] + csp[1]) + (csp[2] + csp[3]);
      if (cj < 192) *(float*)(sm + L_CS + cj * 4) = (s4[0] + s4[1]) + (s4[2] + s4[3]);
    }
    save_residual();
    asm volatile("s_waitcnt lgkmcnt(0)" ::: "memory");
    if (lane == 0) __hip_atomic_fetch_add((LAS u32*)(sm + L_FLG + 4), 1u, __ATOMIC_RELAXED, __HIP_MEMORY_SCOPE_WORKGROUP);
    __builtin_amdgcn_s_setprio(0);
    lds_wait(sm + L_FLG + 8, 1u);
  } else {
    constexpr int NT = 3, NW1 = 48, PRE = W1_PRE * NT;
    u32x4 w1[NW1];
    const __amdgpu_buffer_rsrc_t rw1 = mk_rsrc(ws + WS_W1T + (size_t)(c * 11 + 3 * w) * 16 * 1024, 48 * 1024);
    auto w1load = [&](int k) { w1[k] = bload16s(rw1, lane * 16, (k % NT) * 16384 + (k / NT) * 1024); };
    auto issued = [&](int ks) constexpr { const int v = PRE + NT * (ks + 1); return v < NW1 ? v : NW1; };
#pragma unroll
    for (int i = 0; i < PRE; i++) w1load(i);
    asm volatile("s_waitcnt vmcnt(%0) lgkmcnt(0)" ::"n"(PRE) : "memory");
    __builtin_amdgcn_s_barrier();
    const char* tb[4];
#pragma unroll
    for (int m = 0; m < 4; m++) tb[m] = sm + L_HT + (8 * g + q4) * 128 + (((2 * m + (p4 >> 1)) ^ fsw) << 4) + 8 * (p4 & 1);
    f32x4 acc[NT][4];
    u32x4 hb[3][4];
#pragma unroll
    for (int i = 0; i < NT; i++)
#pragma unroll
      for (int m = 0; m < 4; m++) acc[i][m] = f32x4{0.f, 0.f, 0.f, 0.f};
#pragma unroll
    for (int d = 0; d < 2; d++)
#pragma unroll
      for (int m = 0; m < 4; m++) {
        const u32x2 a = tr_read(tb[m] + d * 4096), bq = tr_read(tb[m] + d * 4096 + 512);
        hb[d][m] = u32x4{a[0], a[1], bq[0], bq[1]};
      }
#pragma unroll
    for (int ks = 0; ks < 16; ks++) {
#pragma unroll
      for (int m = 0; m < 4; m++) {
        acc[0][m] = mfma(w1[ks * NT], hb[ks % 3][m], acc[0][m]);
        SCHED_FENCE();
        if (ks + 2 < 16) { const u32x2 a = tr_read(tb[m] + (ks + 2) * 4096); hb[(ks + 2) % 3][m][0] = a[0]; hb[(ks + 2) % 3][m][1] = a[1]; }
        SCHED_FENCE();
        acc[1][m] = mfma(w1[ks * NT + 1], hb[ks % 3][m], acc[1][m]);
        SCHED_FENCE();
        if (ks + 2 < 16) { const u32x2 a = tr_read(tb[m] + (ks + 2) * 4096 + 512); hb[(ks + 2) % 3][m][2] = a[0]; hb[(ks + 2) % 3][m][3] = a[1]; }
        SCHED_FENCE();
        acc[2][m] = mfma(w1[ks * NT + 2], hb[ks % 3][m], acc[2][m]);
        SCHED_FENCE();
        if (m < NT && issued(ks - 1) + m < issued(ks)) w1load(issued(ks - 1) + m);
        SCHED_FENCE();
      }
      if (ks == 15 - W1_PRE && w == 0 && lane == 0) *(volatile LAS u32*)(sm + L_FLG + 8) = 1u;
    }
    save_residual();
    lds_wait(sm + L_FLG + 4, 4u);
    float rs[4], mr[4];
    f32x4 cs[NT];
#pragma unroll
    for (int m = 0; m < 4; m++) { const f32x2 v = *(const f32x2*)(sm + L_ST + (m * 16 + n) * 8); rs[m] = v[0]; mr[m] = v[1]; }
#pragma unroll
    for (int i = 0; i < NT; i++) cs[i] = *(const f32x4*)(sm + L_CS + ((3 * w + i) * 16 + 4 * g) * 4);
    const __amdgpu_buffer_rsrc_t hrs = mk_rsrc(ws + WS_HF + (size_t)rt * 44 * 4096, 44 * 4096);
    u32 o_t[NT];
#pragma unroll
    for (int i = 0; i < NT; i++) {
      const int tl = 3 * w + i;
      const int ks2 = tl < 10 ? 5 * c + (tl >> 1) : 40 + (c >> 1), par = tl < 10 ? (tl & 1) : (c & 1);
      o_t[i] = (tl < 11 && (g & 1) == par) ? (u32)(ks2 * 4096 + lane * 16) : 0x40000000u;
    }
#pragma unroll
    for (int m = 0; m < 4; m++) {
#pragma unroll
      for (int i = 0; i < NT; i++) {
        float h1[4];
#pragma unroll
        for (int r = 0; r < 4; r++) h1[r] = silu_val(acc[i][m][r], rs[m], mr[m] * cs[i][r]);
        const u32 p0 = pack_bf(h1[0], h1[1]), p1 = pack_bf(h1[2], h1[3]);
        const auto s0 = __builtin_amdgcn_permlane16_swap(p0, p0, false, false), s1 = __builtin_amdgcn_permlane16_swap(p1, p1, false, false);
        __builtin_amdgcn_raw_buffer_store_b128(u32x4{s0[0], s1[0], s0[1], s1[1]}, hrs, o_t[i] + m * 1024, 0, 16);
      }
      SCHED_FENCE();
    }
  }
  u32x4 w2[22];
#pragma unroll
  for (int i = 0; i < 8; i++) bload16_asm(w2[i], rw2, lane * 16, i * 1024);
  load_w2_tail_support(w2, rw2, lane * 16, w);
  asm volatile("s_cmp_lt_u32 %0, 4\n\ts_cbranch_scc0 1f\n\ts_waitcnt vmcnt(0)\n1:\n\ts_waitcnt lgkmcnt(0)" ::"s"(w) : "memory", "scc");
  __builtin_amdgcn_s_barrier();
  if (w == 7) {
    GAS u32* flags = (GAS u32*)(ws + WS_FLAG);
    if (lane < 32) __hip_atomic_store(flags + b * 64 + lane, 1u, RLX_AGENT);
    const int pb = (b & 3) + (b & 56) + 4 * ((lane >> 2) & 1) + 64 * (lane & 3);
    for (u32 spins = 0; spins < (1u << 20); spins++) {
      const u32 v = __hip_atomic_load(flags + pb * 64, RLX_AGENT);
      if (__all(v == 1u)) break;
      __builtin_amdgcn_s_sleep(1);
    }
  }
  if (w < 4) __builtin_amdgcn_s_sleep(2);
  load_w2_tail_main(w2, rw2, lane * 16, w);
  __builtin_amdgcn_s_barrier();

  const int dh = w & 1, kq = w >> 1;
  const __amdgpu_buffer_rsrc_t rhf = mk_rsrc(ws + WS_HF + (size_t)rt * 44 * 4096 + (size_t)(11 * kq * 4 + 2 * dh) * 1024, 11 * 4096);
  const u32 ldst = (u32)(size_t)sm + (kq * 4 + 2 * dh) * 1024;
  auto stage1 = [&](int s, int e) {
    const int slot = s < 9 ? s : s - 9;
    glds16<true>(rhf, lane * 16, s * 4096 + e * 1024, ldst + slot * 16384 + e * 1024);
  };
  auto stage = [&](int s) { stage1(s, 0); stage1(s, 1); };
  stage(0); stage(1); stage(2); stage(3);
  f32x4 acc2[2][4];
#pragma unroll
  for (int i = 0; i < 2; i++)
#pragma unroll
    for (int m = 0; m < 4; m++) acc2[i][m] = f32x4{0.f, 0.f, 0.f, 0.f};
  asm volatile("s_waitcnt vmcnt(4)"
               : "+v"(w2[0]), "+v"(w2[1]), "+v"(w2[2]), "+v"(w2[3]), "+v"(w2[4]), "+v"(w2[5]), "+v"(w2[6]), "+v"(w2[7]), "+v"(w2[8]), "+v"(w2[9]), "+v"(w2[10]),
                 "+v"(w2[11]), "+v"(w2[12]), "+v"(w2[13]), "+v"(w2[14]), "+v"(w2[15]), "+v"(w2[16]), "+v"(w2[17]), "+v"(w2[18]), "+v"(w2[19]), "+v"(w2[20]), "+v"(w2[21])
               ::"memory");
  __builtin_amdgcn_s_barrier();
  {
    const char* base = sm + kq * 4096 + lane * 16;
    u32x4 hb[2][4];
#pragma unroll
    for (int m = 0; m < 4; m++) hb[0][m] = *(const u32x4*)(base + m * 1024);
#pragma unroll
    for (int s = 0; s < 11; s++) {
      if (s == 1) { asm volatile("s_waitcnt vmcnt(4)" ::: "memory"); __builtin_amdgcn_s_barrier(); }
      else if (s == 3 || s == 5) { asm volatile("s_waitcnt vmcnt(6)" ::: "memory"); __builtin_amdgcn_s_barrier(); }
      else if (s == 7) { asm volatile("s_waitcnt vmcnt(2)" ::: "memory"); __builtin_amdgcn_s_barrier(); }
      else if (s == 9) { asm volatile("s_waitcnt vmcnt(0)" ::: "memory"); __builtin_amdgcn_s_barrier(); }
      const int slot = s + 1 < 9 ? s + 1 : s + 1 - 9;
#pragma unroll
      for (int m = 0; m < 4; m++) {
        acc2[0][m] = mfma(w2[2 * s], hb[s & 1][m], acc2[0][m]);
        SCHED_FENCE();
        if (s < 10) hb[(s + 1) & 1][m] = *(const u32x4*)(base + slot * 16384 + m * 1024);
        SCHED_FENCE();
        acc2[1][m] = mfma(w2[2 * s + 1], hb[s & 1][m], acc2[1][m]);
        SCHED_FENCE();
        if (s < 2) stage1(4 + 2 * s + (m >> 1), m & 1);
        else if (s < 5 && (m & 1)) stage1(s + 6, m >> 1);
        SCHED_FENCE();
      }
    }
  }
  asm volatile("s_waitcnt lgkmcnt(0)" ::: "memory");
  __builtin_amdgcn_s_barrier();
  {
    char* red = sm + L_RED + dh * 32768;
    f32x4 o0, o1;
    auto scatter = [&](auto KQ) {
      constexpr int k = decltype(KQ)::value;
#pragma unroll
      for (int m = 0; m < 4; m++) {
        if (m != k) {
#pragma unroll
          for (int i = 0; i < 2; i++) *(f32x4*)(red + (((k * 4 + m) * 2 + i) * 64 + lane) * 16) = acc2[i][m];
        }
      }
      o0 = acc2[0][k]; o1 = acc2[1][k];
      asm volatile("" : "+v"(o0), "+v"(o1));
    };
    if (kq == 0) scatter(IntC<0>{}); else if (kq == 1) scatter(IntC<1>{}); else if (kq == 2) scatter(IntC<2>{}); else scatter(IntC<3>{});
    __syncthreads();
#pragma unroll
    for (int j = 1; j < 4; j++) {
      const int src = (kq + j) & 3;
      o0 += *(const f32x4*)(red + (((src * 4 + kq) * 2 + 0) * 64 + lane) * 16);
      o1 += *(const f32x4*)(red + (((src * 4 + kq) * 2 + 1) * 64 + lane) * 16);
    }
    const u32x4 hres = *(const u32x4*)(sm + L_RES + (w * 64 + lane) * 16);
    u32x4 res;
#pragma unroll
    for (int r = 0; r < 4; r++) res[r] = pack_bf(bf_lo(hres[r]) + rb(o0[r]), bf_hi(hres[r]) + rb(o1[r]));
    __builtin_amdgcn_raw_buffer_store_b128(res, mk_rsrc(OUT, S * D * 2), (u32)((row0 + 16 * kq + n) * D + 64 * c + 32 * dh + 8 * g) * 2, 0, 2);
  }
  asm volatile("ffn_text_end:");
}

extern "C" void kernel_launch(void* const* d_in, const int* in_sizes, int n_in, void* d_out, int out_size, void* d_ws,
                              size_t ws_size, hipStream_t stream) {
  const u16* X = (const u16*)d_in[0];
  const u16* K = (const u16*)d_in[1];
  const u16* V = (const u16*)d_in[2];
  const u16* SK = (const u16*)d_in[3];
  const u16* HK = (const u16*)d_in[4];
  const u16* DV = (const u16*)d_in[5];
  const u16* W1 = (const u16*)d_in[7];
  const u16* W2 = (const u16*)d_in[8];
  char* ws = (char*)d_ws;
  k1_prep<<<256, 512, 0, stream>>>(X, K, V, SK, HK, ws);
  k2_conv<<<256, 512, 0, stream>>>(HK, DV, W1, W2, ws, (const u16*)d_out);
  k34_ffn<<<256, 512, 0, stream>>>(ws, (u16*)d_out);
}
```

```cpp
#include <hip/hip_runtime.h>
#include <stdint.h>

#include <stdio.h>
struct Stamps { unsigned long long t[16]; };
#define STAMP_DECL Stamps stp; _Pragma("unroll") for (int _i = 0; _i < 16; _i++) stp.t[_i] = 0
#define STAMP(ws, kidx, i) do { __builtin_amdgcn_sched_barrier(0); stp.t[i] = __builtin_amdgcn_s_memrealtime(); __builtin_amdgcn_s_waitcnt(0xc07f); __builtin_amdgcn_sched_barrier(0); } while (0)
#define AT(i) (stp.t[i] ? (int)(stp.t[i] % 10000000ull) : -1)
#define STAMP_FLUSH(ws, kidx) do { if ((threadIdx.x == 0 || threadIdx.x == 256) && (blockIdx.x % 51 == 0)) { \
  printf("k%d b%d t%d : %d %d %d %d %d %d %d %d %d %d %d %d %d\n", kidx, (int)blockIdx.x, (int)threadIdx.x, AT(0), AT(1), AT(2), AT(3), AT(4), AT(5), AT(6), AT(7), AT(8), AT(9), AT(10), AT(12), AT(15)); } } while (0)

typedef unsigned short u16;
typedef unsigned int u32;
typedef unsigned long long u64;
typedef __bf16 bf16x8 __attribute__((ext_vector_type(8)));
typedef __bf16 bf16x2 __attribute__((ext_vector_type(2)));
typedef short s16x4 __attribute__((ext_vector_type(4)));
typedef float f32x4 __attribute__((ext_vector_type(4)));
typedef float f32x2 __attribute__((ext_vector_type(2)));
typedef u32 u32x4 __attribute__((ext_vector_type(4)));
typedef u32 u32x2 __attribute__((ext_vector_type(2)));
#define GAS __attribute__((address_space(1)))
#define LAS __attribute__((address_space(3)))
#define RLX_AGENT __ATOMIC_RELAXED, __HIP_MEMORY_SCOPE_AGENT
#define SCHED_FENCE() __builtin_amdgcn_sched_barrier(0)

constexpr int S = 2048, D = 512, F = 1376;

constexpr size_t WS_FLAG = 0;
constexpr size_t WS_KVT  = 1 << 16;
constexpr size_t WS_XT   = WS_KVT + (size_t)D * S * 2;
constexpr size_t WS_HT   = WS_XT + (size_t)D * S * 2;
constexpr size_t WS_W1T  = WS_HT + (size_t)D * S * 2;
constexpr size_t WS_W2T  = WS_W1T + 8 * 11 * 16 * 1024;
constexpr size_t WS_CSP  = WS_W2T + 8 * 4 * 44 * 1024;
constexpr size_t WS_ST1  = WS_CSP + 16 * 8 * 11 * 16 * 4;
constexpr size_t WS_HF   = WS_ST1 + S * 8;
constexpr size_t WS_END  = WS_HF + (size_t)32 * 44 * 4 * 1024;

__device__ __forceinline__ float bf_lo(u32 v) { return __uint_as_float(v << 16); }
__device__ __forceinline__ float bf_hi(u32 v) { return __uint_as_float(v & 0xffff0000u); }
__device__ __forceinline__ u32 pack_bf(float a, float b) {
  f32x2 z = {a, b};
  return __builtin_bit_cast(u32, __builtin_convertvector(z, bf16x2));
}
__device__ __forceinline__ float rb(float a) { return bf_lo(pack_bf(a, 0.f)); }
__device__ __forceinline__ float dot2(u32 a, u32 b, float c) {
  return __builtin_amdgcn_fdot2_f32_bf16(__builtin_bit_cast(bf16x2, a), __builtin_bit_cast(bf16x2, b), c, false);
}
__device__ __forceinline__ u32 perm_lo(u32 a, u32 b) { return __builtin_amdgcn_perm(b, a, 0x05040100u); }
__device__ __forceinline__ u32 perm_hi(u32 a, u32 b) { return __builtin_amdgcn_perm(b, a, 0x07060302u); }
__device__ __forceinline__ f32x4 mfma(u32x4 a, u32x4 b, f32x4 c) {
  return __builtin_amdgcn_mfma_f32_16x16x32_bf16(__builtin_bit_cast(bf16x8, a), __builtin_bit_cast(bf16x8, b), c, 0, 0, 0);
}
constexpr u32 ONES = 0x3F803F80u;
template <int V> struct IntC { static constexpr int value = V; };
__device__ __forceinline__ float sum_g(float x) {
  auto s = __builtin_amdgcn_permlane16_swap(__float_as_uint(x), __float_as_uint(x), false, false);
  x = __uint_as_float(s[0]) + __uint_as_float(s[1]);
  auto t = __builtin_amdgcn_permlane32_swap(__float_as_uint(x), __float_as_uint(x), false, false);
  return __uint_as_float(t[0]) + __uint_as_float(t[1]);
}
template <int CTRL>
__device__ __forceinline__ float dpp_add(float x) {
  return x + __uint_as_float(__builtin_amdgcn_update_dpp(0u, __float_as_uint(x), CTRL, 0xf, 0xf, true));
}
__device__ __forceinline__ float wave_sum(float x) {
  x = dpp_add<0xB1>(x);
  x = dpp_add<0x4E>(x);
  x = dpp_add<0x141>(x);
  x = dpp_add<0x140>(x);
  return sum_g(x);
}
__device__ __forceinline__ __amdgpu_buffer_rsrc_t mk_rsrc(const void* p, u32 bytes) {
  return __builtin_amdgcn_make_buffer_rsrc((void*)p, 0, bytes, 0x00020000);
}
__device__ __forceinline__ u32x4 bload16(__amdgpu_buffer_rsrc_t r, u32 off) { return __builtin_amdgcn_raw_buffer_load_b128(r, off, 0, 0); }
__device__ __forceinline__ u32x2 bload8(__amdgpu_buffer_rsrc_t r, u32 off) { return __builtin_amdgcn_raw_buffer_load_b64(r, off, 0, 0); }
template <bool SC1>
__device__ __forceinline__ void glds16(__amdgpu_buffer_rsrc_t r, u32 voff, u32 soff, u32 lds_dst) {
  if (SC1)
    asm volatile("s_mov_b32 m0, %0\n\ts_nop 0\n\tbuffer_load_dwordx4 %1, %2, %3 offen sc1 lds" ::"s"(lds_dst), "v"(voff), "s"(r), "s"(soff) : "memory");
  else
    asm volatile("s_mov_b32 m0, %0\n\ts_nop 0\n\tbuffer_load_dwordx4 %1, %2, %3 offen lds" ::"s"(lds_dst), "v"(voff), "s"(r), "s"(soff) : "memory");
}
__device__ __forceinline__ void bload16_asm(u32x4& d, __amdgpu_buffer_rsrc_t r, u32 voff, u32 soff) {
  asm volatile("buffer_load_dwordx4 %0, %1, %2, %3 offen" : "=v"(d) : "v"(voff), "s"(r), "s"(soff) : "memory");
}
#define K1_AUX 0
#define WT_AUX 16
#define K2_AUX 16
template <int AUX>
__device__ __forceinline__ void gstore16(char* base, size_t off, u32x4 v) {
  const __amdgpu_buffer_rsrc_t r = mk_rsrc(base, 0x7fffffffu);
  __builtin_amdgcn_raw_buffer_store_b128(v, r, (u32)off, 0, AUX);
}
template <int AUX>
__device__ __forceinline__ void gstore8(char* base, size_t off, u32x2 v) {
  const __amdgpu_buffer_rsrc_t r = mk_rsrc(base, 0x7fffffffu);
  __builtin_amdgcn_raw_buffer_store_b64(v, r, (u32)off, 0, AUX);
}
__device__ __forceinline__ u32x4 bload16s(__amdgpu_buffer_rsrc_t r, u32 voff, u32 soff) { return __builtin_amdgcn_raw_buffer_load_b128(r, voff, soff, 0); }
__device__ __forceinline__ u32x2 tr_read(const char* p) {
  s16x4 r = __builtin_amdgcn_ds_read_tr16_b64_v4i16((LAS s16x4*)p);
  return __builtin_bit_cast(u32x2, r);
}

__device__ __forceinline__ void code_prefetch(int part, int nparts, int lane) {
  u64 pc; u32 len;
  asm volatile("s_getpc_b64 %0\n\ts_mov_b32 %1, ffn_text_end-." : "=s"(pc), "=s"(len));
  const __amdgpu_buffer_rsrc_t r = mk_rsrc((const void*)pc, len);
  u32 acc = 0;
  for (u32 o = part * 1024; o < len; o += nparts * 1024) acc |= __builtin_amdgcn_raw_buffer_load_b128(r, o + lane * 16, 0, 0)[0];
  asm volatile("" ::"v"(acc));
}
__device__ __forceinline__ int fcol(int ks, int g, int j) {
  const int slot = 8 * (g >> 1) + j;
  if (ks < 40) { const int ct = ks / 5, p = ks - 5 * ct; return 128 * (2 * p + (g & 1)) + 8 * slot + ct; }
  return 1280 + 8 * slot + 2 * (ks - 40) + (g & 1);
}

__global__ __launch_bounds__(512, 2) void k1_prep(const u16* __restrict__ X, const u16* __restrict__ K, const u16* __restrict__ V,
                                                  const u16* __restrict__ SK, const u16* __restrict__ HK, char* __restrict__ ws) {
  __shared__ __attribute__((aligned(16))) char sm[16384 + 4096];
  const int b = blockIdx.x, t = threadIdx.x, lane = t & 63;
  const int w = __builtin_amdgcn_readfirstlane(t >> 6);
  STAMP_DECL;
  STAMP(ws, 0, 0);
  const int dt = b & 7, st = b >> 3;
  if (w < 4) {
    const int rp = t >> 3, c = t & 7;
    const int s = st * 64 + 2 * rp, ch0 = dt * 64 + c * 8;
    const __amdgpu_buffer_rsrc_t rk = mk_rsrc(K, S * D * 2);
    const u32 off = (u32)(s * D + ch0) * 2;
    u32x4 kk[5];
#pragma unroll
    for (int i = 0; i < 5; i++) kk[i] = bload16(rk, off + (1 - i) * 1024);
    const u32x4 v0 = *(const u32x4*)((const char*)V + off), v1 = *(const u32x4*)((const char*)V + off + 1024);
    const u32x4 x0 = *(const u32x4*)((const char*)X + off), x1 = *(const u32x4*)((const char*)X + off + 1024);
    u32x4 wa[4];
#pragma unroll
    for (int i = 0; i < 4; i++) wa[i] = *(const u32x4*)(SK + ch0 * 4 + i * 8);
    SCHED_FENCE();
    STAMP(ws, 0, 1);
    const int swz = ((rp >> 2) ^ c) << 4;
    char* pk = sm + (8 * c) * 128 + swz + ((3 - (rp & 3)) << 2);
    char* px = sm + 8192 + (8 * c) * 128 + swz + ((rp & 3) << 2);
#pragma unroll
    for (int i = 0; i < 4; i++) {
      const u32 l01 = perm_lo(kk[0][i], kk[1][i]), l12 = perm_lo(kk[1][i], kk[2][i]), l23 = perm_lo(kk[2][i], kk[3][i]), l34 = perm_lo(kk[3][i], kk[4][i]);
      const u32 h01 = perm_hi(kk[0][i], kk[1][i]), h12 = perm_hi(kk[1][i], kk[2][i]), h23 = perm_hi(kk[2][i], kk[3][i]), h34 = perm_hi(kk[3][i], kk[4][i]);
      const float a1 = dot2(l01, wa[i][0], dot2(l23, wa[i][1], 0.f)) * bf_lo(v1[i]);
      const float a0 = dot2(l12, wa[i][0], dot2(l34, wa[i][1], 0.f)) * bf_lo(v0[i]);
      const float b1 = dot2(h01, wa[i][2], dot2(h23, wa[i][3], 0.f)) * bf_hi(v1[i]);
      const float b0 = dot2(h12, wa[i][2], dot2(h34, wa[i][3], 0.f)) * bf_hi(v0[i]);
      *(u32*)(pk + (2 * i) * 128) = pack_bf(a1, a0);
      *(u32*)(pk + (2 * i + 1) * 128) = pack_bf(b1, b0);
      *(u32*)(px + (2 * i) * 128) = perm_lo(x0[i], x1[i]);
      *(u32*)(px + (2 * i + 1) * 128) = perm_hi(x0[i], x1[i]);
    }
    STAMP(ws, 0, 2);
    __syncthreads();
#pragma unroll
    for (int i = 0; i < 2; i++) {
      const int u = t + 256 * i, ch = u >> 3, cc = u & 7;
      const u32x4 o = *(const u32x4*)(sm + ch * 128 + cc * 16);
      const int grp = cc ^ ((ch >> 3) & 7);
      const size_t go = (size_t)(dt * 64 + ch) * S + st * 64 + grp * 8;
      gstore16<K1_AUX>(ws, WS_KVT + go * 2, o);
      gstore16<K1_AUX>(ws, WS_XT + go * 2, *(const u32x4*)(sm + 8192 + ch * 128 + cc * 16));
    }
    STAMP(ws, 0, 3);
  } else {
    const int lrow = 256 * (b & 7) + 8 * (b >> 3) + 2 * (w - 4);
    u32x4 xr[2];
#pragma unroll
    for (int i = 0; i < 2; i++) xr[i] = *(const u32x4*)(X + (size_t)(lrow + i) * D + lane * 8);
    if (w == 4 && lane < 32) __hip_atomic_store((GAS u32*)(ws + WS_FLAG) + b * 64 + lane, 0u, RLX_AGENT);
    {
      const u32 dump = (u32)(size_t)(sm + 16384) + (w - 4) * 1024;
      const int chp = 64 * (b & 7) + 2 * (b >> 3);
      const __amdgpu_buffer_rsrc_t rh = mk_rsrc(HK + (size_t)chp * S, 2 * S * 2);
      glds16<false>(rh, lane * 16, (w - 4) * 2048, dump);
      glds16<false>(rh, lane * 16, (w - 4) * 2048 + 1024, dump);
    }
    code_prefetch((b >> 3) * 4 + (w - 4), 128, lane);
    STAMP(ws, 0, 1);
    __builtin_amdgcn_s_barrier();
#pragma unroll
    for (int q = 0; q < 2; q++) {
      float sum = 0.f;
#pragma unroll
      for (int i = 0; i < 4; i++) sum = dot2(xr[q][i], ONES, sum);
      const float mean = wave_sum(sum) * (1.f / D);
      float sq = 0.f;
#pragma unroll
      for (int i = 0; i < 4; i++) { const float d0 = bf_lo(xr[q][i]) - mean, d1 = bf_hi(xr[q][i]) - mean; sq += d0 * d0 + d1 * d1; }
      sq = wave_sum(sq);
      const float sd = rb(__builtin_amdgcn_sqrtf(rb(rb(sq * (1.f / D)) + 1e-5f)));
      const float r1 = __builtin_amdgcn_rcpf(sd);
      if (lane == 0) { f32x2 o2 = {r1, -rb(mean) * r1}; *(f32x2*)(ws + WS_ST1 + (size_t)(lrow + q) * 8) = o2; }
    }
    asm volatile("s_waitcnt vmcnt(0)" ::: "memory");
    STAMP(ws, 0, 4);
  }
  STAMP_FLUSH(ws, 0);
}

constexpr int HC_STRIDE = 4608;
constexpr int HC_CH = 8 * HC_STRIDE;
constexpr int U_CH = (240 + 2048) * 2;
constexpr int K2_H = 0, K2_U = 2 * HC_CH, K2_R = K2_U + 2 * U_CH + 32, K2_F = K2_R + 4 * 4096, K2_END = K2_F + 32;
__device__ __forceinline__ constexpr int hoff(int c) {
  return c == 0 ? 4 : c == 1 ? 2 : c == 2 ? 8 : c == 3 ? 3 : c == 4 ? 7 : c == 5 ? 0 : c == 6 ? 15 : 12;
}
__device__ __forceinline__ int hoff_rt(int c) { return (0xCF073824u >> (4 * c)) & 15; }
__device__ __forceinline__ void lds_wait(const char* p, u32 want) {
  for (u32 spins = 0; spins < (1u << 22); spins++) {
    if (__builtin_amdgcn_readfirstlane(*(volatile LAS u32*)p) == want) break;
    __builtin_amdgcn_s_sleep(1);
  }
  asm volatile("" ::: "memory");
}

__global__ __launch_bounds__(512, 2) void k2_conv(const u16* __restrict__ HK, const u16* __restrict__ DV,
                                                  const u16* __restrict__ W1, const u16* __restrict__ W2, char* __restrict__ ws) {
  __shared__ __attribute__((aligned(16))) char sm[K2_END];
  const int b = blockIdx.x, t = threadIdx.x, lane = t & 63;
  const int w = __builtin_amdgcn_readfirstlane(t >> 6);
  STAMP_DECL;
  STAMP(ws, 1, 0);
  const int chp = 64 * (b & 7) + 2 * (b >> 3);
  const int wcl = (w >> 1) & 1, rh = w & 1;
  const int n = lane & 15, g = lane >> 4;
  {
    const int cl = t >> 8, j = t & 255;
    const __amdgpu_buffer_rsrc_t rh_ = mk_rsrc(HK + (size_t)chp * S, 2 * S * 2);
    const u32 ho = (u32)(cl * S + 8 * j) * 2;
    u32x4 ha, hb, uu;
    const __amdgpu_buffer_rsrc_t ru = mk_rsrc(ws + WS_KVT + (size_t)chp * S * 2, 2 * S * 2);
    bload16_asm(ha, rh_, ho - 16, 0);
    bload16_asm(hb, rh_, ho, 0);
    bload16_asm(uu, ru, ho, 0);
    asm volatile("s_waitcnt vmcnt(0)" : "+v"(ha), "+v"(hb), "+v"(uu)::"memory");
    if (t < 8) *(volatile LAS u32*)(sm + K2_F + t * 4) = 0u;
    SCHED_FENCE();
    if (j == 0) ha = u32x4{0, 0, 0, 0};
    char* hbase = sm + K2_H + cl * HC_CH;
    const u32 W[8] = {ha[0], ha[1], ha[2], ha[3], hb[0], hb[1], hb[2], hb[3]};
#pragma unroll
    for (int c = 0; c < 8; c++) {
      u32x4 o;
#pragma unroll
      for (int a = 0; a < 4; a++) o[a] = (c & 1) ? __builtin_amdgcn_alignbit(W[(c >> 1) + a + 1], W[(c >> 1) + a], 16) : W[(c >> 1) + a];
      *(u32x4*)(hbase + c * HC_STRIDE + 16 * (j + 1 + hoff(c))) = o;
    }
    const u32x4 z4 = {0, 0, 0, 0};
    if (j < 8) *(u32x4*)(hbase + j * HC_STRIDE + 16 * hoff_rt(j)) = z4;
    if (j == 255) *(u32x4*)(hbase + 16 * (257 + hoff(0))) = hb;
    char* ub = sm + K2_U + cl * U_CH;
    *(u32x4*)(ub + 480 + 16 * j) = uu;
    if (j < 30) *(u32x4*)(ub + 16 * j) = z4;
  }
  u32x2 xq[4];
  f32x4 sa[4], sb[4];
  u32 dvr = 0;
  if (w < 4) {
    const __amdgpu_buffer_rsrc_t rs1 = mk_rsrc(ws + WS_ST1, S * 8);
    const __amdgpu_buffer_rsrc_t rx = mk_rsrc(ws + WS_XT + (size_t)(chp + wcl) * S * 2, S * 2);
    const u32 p0 = 1024 * rh + 16 * n + 4 * g;
#pragma unroll
    for (int i = 0; i < 4; i++) {
      asm volatile("buffer_load_dwordx2 %0, %1, %2, %3 offen" : "=v"(xq[i]) : "v"(p0 * 2), "s"(rx), "s"(i * 512) : "memory");
      asm volatile("buffer_load_dwordx4 %0, %1, %2, %3 offen" : "=v"(sa[i]) : "v"(p0 * 8), "s"(rs1), "s"(i * 2048) : "memory");
      asm volatile("buffer_load_dwordx4 %0, %1, %2, %3 offen offset:16" : "=v"(sb[i]) : "v"(p0 * 8), "s"(rs1), "s"(i * 2048) : "memory");
    }
    dvr = *(const u32*)(DV + chp) >> (16 * wcl);
  }
  STAMP(ws, 1, 1);
  __syncthreads();
  STAMP(ws, 1, 2);
  if (w < 4) {
    f32x4 acc[8];
#pragma unroll
    for (int i = 0; i < 8; i++) acc[i] = f32x4{0.f, 0.f, 0.f, 0.f};
    const int idx0 = 25 + n - 8 * g;
    const int c = idx0 & 7;
    const char* fbase = sm + K2_H + wcl * HC_CH + c * HC_STRIDE + 16 * ((idx0 >> 3) + hoff_rt(c)) + 64 * 4 * rh;
    const char* gbase = sm + K2_U + wcl * U_CH + 2 * (16 * n + 8 * g + 224) - 64 * 4 * rh;
    u32x4 Ff[2][8], Gf[2][8];
#pragma unroll
    for (int a = 0; a < 8; a++) { Gf[0][a] = *(const u32x4*)(gbase + 64 * (8 * a)); Ff[0][a] = *(const u32x4*)(fbase + 64 * (8 * a)); }
    SCHED_FENCE();
#pragma unroll
    for (int rr = 0; rr < 4; rr++) {
      const int cur = rr & 1, nxt = cur ^ 1;
      int m = 0;
#pragma unroll
      for (int bb = 0; bb < 8; bb++) {
#pragma unroll
        for (int a = 0; a + bb < 8; a++) {
          acc[a + bb] = mfma(Ff[cur][a], Gf[cur][bb], acc[a + bb]);
          if (rr < 3 && (m & 1) == 1 && m < 32) {
            const int q = m >> 2;
            SCHED_FENCE();
            if (m & 2) Ff[nxt][q] = *(const u32x4*)(fbase + 64 * (8 * q + rr + 1));
            else Gf[nxt][q] = *(const u32x4*)(gbase + 64 * (8 * q - rr - 1));
            SCHED_FENCE();
          }
          m++;
        }
      }
      SCHED_FENCE();
    }
    STAMP(ws, 1, 3);
    f32x4* rbuf = (f32x4*)(sm + K2_R) + w * 256;
    if (rh) {
#pragma unroll
      for (int i = 0; i < 4; i++) rbuf[i * 64 + lane] = acc[i];
#pragma unroll
      for (int i = 0; i < 4; i++) acc[i] = acc[4 + i];
    } else {
#pragma unroll
      for (int i = 0; i < 4; i++) rbuf[i * 64 + lane] = acc[4 + i];
    }
    asm volatile("s_waitcnt lgkmcnt(0)" ::: "memory");
    if (lane == 0) *(volatile LAS u32*)(sm + K2_F + w * 4) = 1u;
    const int ch = chp + wcl;
    const char* ub = sm + K2_U + wcl * U_CH;
    u32x2 uq[4];
#pragma unroll
    for (int i = 0; i < 4; i++) uq[i] = *(const u32x2*)(ub + 2 * (240 + 256 * (4 * rh + i) + 16 * n + 8 * (g >> 1) + ((g & 1) ? 0 : 4)));
    asm volatile("s_waitcnt vmcnt(0)" : "+v"(xq[0]), "+v"(xq[1]), "+v"(xq[2]), "+v"(xq[3]), "+v"(sa[0]), "+v"(sa[1]), "+v"(sa[2]), "+v"(sa[3]),
                 "+v"(sb[0]), "+v"(sb[1]), "+v"(sb[2]), "+v"(sb[3])::"memory");
    lds_wait(sm + K2_F + (w ^ 1) * 4, 1u);
    const float dv = bf_lo(dvr);
    const f32x4* pbuf = (const f32x4*)(sm + K2_R) + (w ^ 1) * 256;
#pragma unroll
    for (int i = 0; i < 4; i++) {
      const int T = 4 * rh + i;
      const f32x4 a = acc[i] + pbuf[i * 64 + lane];
      const int pos = 256 * T + 16 * n + 4 * g;
      const float y0 = fmaf(bf_hi(uq[i][1]), dv, a[0]), y1 = fmaf(bf_lo(uq[i][1]), dv, a[1]), y2 = fmaf(bf_hi(uq[i][0]), dv, a[2]), y3 = fmaf(bf_lo(uq[i][0]), dv, a[3]);
      const float x0 = bf_lo(xq[i][0]), x1 = bf_hi(xq[i][0]), x2 = bf_lo(xq[i][1]), x3 = bf_hi(xq[i][1]);
      const float h0 = fmaf(fmaf(x0, sa[i][0], sa[i][1]), y0, x0), h1 = fmaf(fmaf(x1, sa[i][2], sa[i][3]), y1, x1);
      const float h2 = fmaf(fmaf(x2, sb[i][0], sb[i][1]), y2, x2), h3 = fmaf(fmaf(x3, sb[i][2], sb[i][3]), y3, x3);
      const u32x2 o = {pack_bf(h0, h1), pack_bf(h2, h3)};
      gstore8<K2_AUX>(ws, WS_HT + ((size_t)ch * S + pos) * 2, o);
    }
    STAMP(ws, 1, 4);
  } else {
    const int unit = b + 256 * (w - 4);
    const int lc = lane & 15, lg = lane >> 4;
    if (unit < 176) {
      const __amdgpu_buffer_rsrc_t rw = mk_rsrc(W1, D * F * 2);
      const int blk = unit >> 4, ks = unit & 15;
      const int col = 128 * blk + 8 * lc;
      const u32 o0 = col < F ? (u32)((32 * ks + 8 * lg) * F + col) * 2 : 0x80000000u;
      u32x4 wl[8];
#pragma unroll
      for (int j = 0; j < 8; j++) wl[j] = bload16(rw, o0 + j * F * 2);
#pragma unroll
      for (int i = 0; i < 8; i++) {
        u32x4 o;
#pragma unroll
        for (int a = 0; a < 4; a++) o[a] = (i & 1) ? perm_hi(wl[2 * a][i >> 1], wl[2 * a + 1][i >> 1]) : perm_lo(wl[2 * a][i >> 1], wl[2 * a + 1][i >> 1]);
        gstore16<WT_AUX>(ws, WS_W1T + ((size_t)((i * 11 + blk) * 16 + ks) * 64 + lane) * 16, o);
        const float cs = sum_g(dot2(o[0], ONES, dot2(o[1], ONES, dot2(o[2], ONES, dot2(o[3], ONES, 0.f)))));
        if (lane < 16) *(float*)(ws + WS_CSP + ((size_t)(i * 176 + blk * 16 + lane) * 16 + ks) * 4) = cs;
      }
    } else if (unit < 528) {
      const __amdgpu_buffer_rsrc_t rw = mk_rsrc(W2, D * F * 2);
      const int u2 = unit - 176, ks = u2 >> 3, c2 = u2 & 7;
      u32 wx[8], wy[8];
#pragma unroll
      for (int j = 0; j < 8; j++) {
        const u32 o = (u32)(fcol(ks, lg, j) * D + 64 * c2 + 8 * (lc >> 2) + 2 * (lc & 3)) * 2;
        wx[j] = __builtin_amdgcn_raw_buffer_load_b32(rw, o, 0, 0);
        wy[j] = __builtin_amdgcn_raw_buffer_load_b32(rw, o + 64, 0, 0);
      }
#pragma unroll
      for (int i = 0; i < 4; i++) {
        u32x4 o;
#pragma unroll
        for (int a = 0; a < 4; a++) {
          const u32 e0 = (i >> 1) ? wy[2 * a] : wx[2 * a], e1 = (i >> 1) ? wy[2 * a + 1] : wx[2 * a + 1];
          o[a] = (i & 1) ? perm_hi(e0, e1) : perm_lo(e0, e1);
        }
        gstore16<WT_AUX>(ws, WS_W2T + ((size_t)(((c2 * 2 + (i >> 1)) * 44 + ks) * 2 + (i & 1)) * 64 + lane) * 16, o);
      }
    }
    STAMP(ws, 1, 4);
  }
  STAMP_FLUSH(ws, 1);
}

constexpr int L_HT = 0;
constexpr int L_ST = 147456;
constexpr int L_CS = L_ST + 512;
constexpr int L_RES = L_CS + 768;
constexpr int L_END = L_RES + 8192;
constexpr int L_RED = 0;
constexpr int L_FLG = L_END;
constexpr int L_TOT = L_END + 16;
#define W1_PRE 4
#define W2_LOADS                                                                                                                   \
  "buffer_load_dwordx4 %0, %14, %15, %16 offen\n\tbuffer_load_dwordx4 %1, %14, %15, %16 offen offset:1024\n\t"                      \
  "buffer_load_dwordx4 %2, %14, %15, %16 offen offset:2048\n\tbuffer_load_dwordx4 %3, %14, %15, %16 offen offset:3072\n\t"          \
  "buffer_load_dwordx4 %4, %14, %15, %17 offen\n\tbuffer_load_dwordx4 %5, %14, %15, %17 offen offset:1024\n\t"                      \
  "buffer_load_dwordx4 %6, %14, %15, %17 offen offset:2048\n\tbuffer_load_dwordx4 %7, %14, %15, %17 offen offset:3072\n\t"          \
  "buffer_load_dwordx4 %8, %14, %15, %18 offen\n\tbuffer_load_dwordx4 %9, %14, %15, %18 offen offset:1024\n\t"                      \
  "buffer_load_dwordx4 %10, %14, %15, %18 offen offset:2048\n\tbuffer_load_dwordx4 %11, %14, %15, %18 offen offset:3072\n\t"        \
  "buffer_load_dwordx4 %12, %14, %15, %19 offen\n\tbuffer_load_dwordx4 %13, %14, %15, %19 offen offset:1024\n"
__device__ __forceinline__ void load_w2_tail_support(u32x4 (&w2)[22], __amdgpu_buffer_rsrc_t r, u32 voff, int w) {
  asm volatile("s_cmp_lt_u32 %20, 4\n\ts_cbranch_scc1 1f\n\t" W2_LOADS "1:"
               : "=&v"(w2[8]), "=&v"(w2[9]), "=&v"(w2[10]), "=&v"(w2[11]), "=&v"(w2[12]), "=&v"(w2[13]), "=&v"(w2[14]), "=&v"(w2[15]), "=&v"(w2[16]),
                 "=&v"(w2[17]), "=&v"(w2[18]), "=&v"(w2[19]), "=&v"(w2[20]), "=&v"(w2[21])
               : "v"(voff), "s"(r), "s"(8192), "s"(12288), "s"(16384), "s"(20480), "s"(w) : "memory", "scc");
}
__device__ __forceinline__ void load_w2_tail_main(u32x4 (&w2)[22], __amdgpu_buffer_rsrc_t r, u32 voff, int w) {
  asm volatile("s_cmp_lt_u32 %20, 4\n\ts_cbranch_scc0 1f\n\t" W2_LOADS "1:"
               : "+v"(w2[8]), "+v"(w2[9]), "+v"(w2[10]), "+v"(w2[11]), "+v"(w2[12]), "+v"(w2[13]), "+v"(w2[14]), "+v"(w2[15]), "+v"(w2[16]),
                 "+v"(w2[17]), "+v"(w2[18]), "+v"(w2[19]), "+v"(w2[20]), "+v"(w2[21])
               : "v"(voff), "s"(r), "s"(8192), "s"(12288), "s"(16384), "s"(20480), "s"(w) : "memory", "scc");
}
__device__ __forceinline__ float silu_val(float acc, float rs, float bias) {
  const float tt = fmaf(acc, rs, bias);
  const float e = __builtin_amdgcn_exp2f(tt);
  return tt * __builtin_amdgcn_rcpf(fmaf(e, -1.44269504089f, -1.44269504089f));
}

__global__ __launch_bounds__(512, 2) void k34_ffn(char* __restrict__ ws, u16* __restrict__ OUT) {
  __shared__ __attribute__((aligned(16))) char sm[L_TOT];
  const int b = blockIdx.x, t = threadIdx.x, lane = t & 63;
  const int w = __builtin_amdgcn_readfirstlane(t >> 6);
  const int rt = 8 * (b & 3) + ((b >> 3) & 7), c = 4 * ((b >> 2) & 1) + (b >> 6);
  const int n = lane & 15, g = lane >> 4;
  const int row0 = 64 * rt;
  STAMP_DECL;
  STAMP(ws, 2, 0);
  {
    const __amdgpu_buffer_rsrc_t rh = mk_rsrc(ws + WS_HT + ((size_t)(64 * w) * S + row0) * 2, 64 * S * 2);
    const int f0 = 2 * ((lane >> 4) & 1);
    const u32 v0 = (u32)(lane >> 3) * S * 2 + (((lane & 7) ^ f0) << 4), v1 = (u32)(lane >> 3) * S * 2 + (((lane & 7) ^ (f0 + 4)) << 4);
#pragma unroll
    for (int i = 0; i < 8; i++) glds16<false>(rh, (i & 1) ? v1 : v0, i * 8 * S * 2, (u32)(size_t)(sm + L_HT) + (64 * w + 8 * i) * 128);
  }
  if (t < 4) *(volatile LAS u32*)(sm + L_FLG + t * 4) = 0u;
  __builtin_amdgcn_s_barrier();
  const __amdgpu_buffer_rsrc_t rw2 = mk_rsrc(ws + WS_W2T + (size_t)((c * 2 + (w & 1)) * 44 + 11 * (w >> 1)) * 2048, 22 * 1024);
  const int q4 = (lane & 15) >> 2, p4 = lane & 3;
  const int fsw = 2 * ((q4 >> 1) & 1) + 4 * (g & 1);
  auto save_residual = [&]() {
    const char* p = sm + L_HT + (8 * g + q4) * 128 + (((2 * (w >> 1) + (p4 >> 1)) ^ fsw) << 4) + 8 * (p4 & 1) + (2 * c + (w & 1)) * 4096;
    const u32x2 a = tr_read(p), bq = tr_read(p + 512);
    *(u32x4*)(sm + L_RES + (w * 64 + lane) * 16) = u32x4{a[0], a[1], bq[0], bq[1]};
  };
  if (w >= 4) {
    f32x4 csp[4];
    const __amdgpu_buffer_rsrc_t rc = mk_rsrc(ws + WS_CSP + (size_t)c * 176 * 64, 176 * 64);
    const int cj = (w - 4) * 64 + lane;
#pragma unroll
    for (int i = 0; i < 4; i++) csp[i] = __builtin_bit_cast(f32x4, bload16s(rc, (u32)(cj < 176 ? cj : 175) * 64, i * 16));
    asm volatile("s_waitcnt vmcnt(4) lgkmcnt(0)" ::: "memory");
    __builtin_amdgcn_s_barrier();
    STAMP(ws, 2, 1);
    __builtin_amdgcn_s_setprio(3);
    const char* p = sm + L_HT + (8 * g + q4) * 128 + (((2 * (w - 4) + (p4 >> 1)) ^ fsw) << 4) + 8 * (p4 & 1);
    u32x2 fa[16][2];
#pragma unroll
    for (int ks = 0; ks < 16; ks++) { fa[ks][0] = tr_read(p + ks * 4096); fa[ks][1] = tr_read(p + ks * 4096 + 512); }
    f32x4 q0 = {0.f, 0.f, 0.f, 0.f}, q1 = {0.f, 0.f, 0.f, 0.f};
    const u32x4 ones = {ONES, ONES, ONES, ONES};
#pragma unroll
    for (int ks = 0; ks < 16; ks++) {
      const u32x4 f0 = {fa[ks][0][0], fa[ks][0][1], fa[ks][1][0], fa[ks][1][1]};
      q0 = mfma(f0, f0, q0);
      q1 = mfma(ones, f0, q1);
    }
    {
      const float lo = (n & 1) ? q0[1] : q0[0], hi = (n & 1) ? q0[3] : q0[2];
      const float q = (n & 2) ? hi : lo;
      const float mu = q1[0] * (1.f / D);
      const float var = fmaxf(q * (1.f / D) - mu * mu, 0.f);
      const float rstd = __builtin_amdgcn_rcpf(rb(__builtin_amdgcn_sqrtf(rb(rb(var) + 1e-5f))));
      if (g == (n >> 2)) *(f32x2*)(sm + L_ST + ((w - 4) * 16 + n) * 8) = f32x2{-1.44269504089f * rstd, rb(mu) * rstd * 1.44269504089f};
    }
    {
      const f32x4 s4 = (csp[0] + csp[1]) + (csp[2] + csp[3]);
      if (cj < 192) *(float*)(sm + L_CS + cj * 4) = (s4[0] + s4[1]) + (s4[2] + s4[3]);
    }
    save_residual();
    asm volatile("s_waitcnt lgkmcnt(0)" ::: "memory");
    if (lane == 0) __hip_atomic_fetch_add((LAS u32*)(sm + L_FLG + 4), 1u, __ATOMIC_RELAXED, __HIP_MEMORY_SCOPE_WORKGROUP);
    __builtin_amdgcn_s_setprio(0);
    STAMP(ws, 2, 12);
    lds_wait(sm + L_FLG + 8, 1u);
  } else {
    constexpr int NT = 3, NW1 = 48, PRE = W1_PRE * NT;
    u32x4 w1[NW1];
    const __amdgpu_buffer_rsrc_t rw1 = mk_rsrc(ws + WS_W1T + (size_t)(c * 11 + 3 * w) * 16 * 1024, 48 * 1024);
    auto w1load = [&](int k) { w1[k] = bload16s(rw1, lane * 16, (k % NT) * 16384 + (k / NT) * 1024); };
    auto issued = [&](int ks) constexpr { const int v = PRE + NT * (ks + 1); return v < NW1 ? v : NW1; };
#pragma unroll
    for (int i = 0; i < PRE; i++) w1load(i);
    asm volatile("s_waitcnt vmcnt(%0) lgkmcnt(0)" ::"n"(PRE) : "memory");
    __builtin_amdgcn_s_barrier();
    STAMP(ws, 2, 1);
    const char* tb[4];
#pragma unroll
    for (int m = 0; m < 4; m++) tb[m] = sm + L_HT + (8 * g + q4) * 128 + (((2 * m + (p4 >> 1)) ^ fsw) << 4) + 8 * (p4 & 1);
    f32x4 acc[NT][4];
    u32x4 hb[3][4];
#pragma unroll
    for (int i = 0; i < NT; i++)
#pragma unroll
      for (int m = 0; m < 4; m++) acc[i][m] = f32x4{0.f, 0.f, 0.f, 0.f};
#pragma unroll
    for (int d = 0; d < 2; d++)
#pragma unroll
      for (int m = 0; m < 4; m++) {
        const u32x2 a = tr_read(tb[m] + d * 4096), bq = tr_read(tb[m] + d * 4096 + 512);
        hb[d][m] = u32x4{a[0], a[1], bq[0], bq[1]};
      }
#pragma unroll
    for (int ks = 0; ks < 16; ks++) {
#pragma unroll
      for (int m = 0; m < 4; m++) {
        acc[0][m] = mfma(w1[ks * NT], hb[ks % 3][m], acc[0][m]);
        SCHED_FENCE();
        if (ks + 2 < 16) { const u32x2 a = tr_read(tb[m] + (ks + 2) * 4096); hb[(ks + 2) % 3][m][0] = a[0]; hb[(ks + 2) % 3][m][1] = a[1]; }
        SCHED_FENCE();
        acc[1][m] = mfma(w1[ks * NT + 1], hb[ks % 3][m], acc[1][m]);
        SCHED_FENCE();
        if (ks + 2 < 16) { const u32x2 a = tr_read(tb[m] + (ks + 2) * 4096 + 512); hb[(ks + 2) % 3][m][2] = a[0]; hb[(ks + 2) % 3][m][3] = a[1]; }
        SCHED_FENCE();
        acc[2][m] = mfma(w1[ks * NT + 2], hb[ks % 3][m], acc[2][m]);
        SCHED_FENCE();
        if (m < NT && issued(ks - 1) + m < issued(ks)) w1load(issued(ks - 1) + m);
        SCHED_FENCE();
      }
      if (ks == 15 - W1_PRE && w == 0 && lane == 0) *(volatile LAS u32*)(sm + L_FLG + 8) = 1u;
    }
    STAMP(ws, 2, 12);
    save_residual();
    lds_wait(sm + L_FLG + 4, 4u);
    STAMP(ws, 2, 2);
    float rs[4], mr[4];
    f32x4 cs[NT];
#pragma unroll
    for (int m = 0; m < 4; m++) { const f32x2 v = *(const f32x2*)(sm + L_ST + (m * 16 + n) * 8); rs[m] = v[0]; mr[m] = v[1]; }
#pragma unroll
    for (int i = 0; i < NT; i++) cs[i] = *(const f32x4*)(sm + L_CS + ((3 * w + i) * 16 + 4 * g) * 4);
    const __amdgpu_buffer_rsrc_t hrs = mk_rsrc(ws + WS_HF + (size_t)rt * 44 * 4096, 44 * 4096);
    u32 o_t[NT];
#pragma unroll
    for (int i = 0; i < NT; i++) {
      const int tl = 3 * w + i;
      const int ks2 = tl < 10 ? 5 * c + (tl >> 1) : 40 + (c >> 1), par = tl < 10 ? (tl & 1) : (c & 1);
      o_t[i] = (tl < 11 && (g & 1) == par) ? (u32)(ks2 * 4096 + lane * 16) : 0x40000000u;
    }
#pragma unroll
    for (int m = 0; m < 4; m++) {
#pragma unroll
      for (int i = 0; i < NT; i++) {
        float h1[4];
#pragma unroll
        for (int r = 0; r < 4; r++) h1[r] = silu_val(acc[i][m][r], rs[m], mr[m] * cs[i][r]);
        const u32 p0 = pack_bf(h1[0], h1[1]), p1 = pack_bf(h1[2], h1[3]);
        const auto s0 = __builtin_amdgcn_permlane16_swap(p0, p0, false, false), s1 = __builtin_amdgcn_permlane16_swap(p1, p1, false, false);
        __builtin_amdgcn_raw_buffer_store_b128(u32x4{s0[0], s1[0], s0[1], s1[1]}, hrs, o_t[i] + m * 1024, 0, 16);
      }
      SCHED_FENCE();
    }
    STAMP(ws, 2, 4);
  }
  u32x4 w2[22];
#pragma unroll
  for (int i = 0; i < 8; i++) bload16_asm(w2[i], rw2, lane * 16, i * 1024);
  load_w2_tail_support(w2, rw2, lane * 16, w);
  asm volatile("s_cmp_lt_u32 %0, 4\n\ts_cbranch_scc0 1f\n\ts_waitcnt vmcnt(0)\n1:\n\ts_waitcnt lgkmcnt(0)" ::"s"(w) : "memory", "scc");
  STAMP(ws, 2, 5);
  __builtin_amdgcn_s_barrier();
  STAMP(ws, 2, 6);
  if (w == 7) {
    GAS u32* flags = (GAS u32*)(ws + WS_FLAG);
    if (lane < 32) __hip_atomic_store(flags + b * 64 + lane, 1u, RLX_AGENT);
    const int pb = (b & 3) + (b & 56) + 4 * ((lane >> 2) & 1) + 64 * (lane & 3);
    for (u32 spins = 0; spins < (1u << 20); spins++) {
      const u32 v = __hip_atomic_load(flags + pb * 64, RLX_AGENT);
      if (__all(v == 1u)) break;
      __builtin_amdgcn_s_sleep(1);
    }
  }
  if (w < 4) __builtin_amdgcn_s_sleep(2);
  load_w2_tail_main(w2, rw2, lane * 16, w);
  __builtin_amdgcn_s_barrier();
  STAMP(ws, 2, 7);

  const int dh = w & 1, kq = w >> 1;
  const __amdgpu_buffer_rsrc_t rhf = mk_rsrc(ws + WS_HF + (size_t)rt * 44 * 4096 + (size_t)(11 * kq * 4 + 2 * dh) * 1024, 11 * 4096);
  const u32 ldst = (u32)(size_t)sm + (kq * 4 + 2 * dh) * 1024;
  auto stage1 = [&](int s, int e) {
    const int slot = s < 9 ? s : s - 9;
    glds16<true>(rhf, lane * 16, s * 4096 + e * 1024, ldst + slot * 16384 + e * 1024);
  };
  auto stage = [&](int s) { stage1(s, 0); stage1(s, 1); };
  stage(0); stage(1); stage(2); stage(3);
  f32x4 acc2[2][4];
#pragma unroll
  for (int i = 0; i < 2; i++)
#pragma unroll
    for (int m = 0; m < 4; m++) acc2[i][m] = f32x4{0.f, 0.f, 0.f, 0.f};
  asm volatile("s_waitcnt vmcnt(4)"
               : "+v"(w2[0]), "+v"(w2[1]), "+v"(w2[2]), "+v"(w2[3]), "+v"(w2[4]), "+v"(w2[5]), "+v"(w2[6]), "+v"(w2[7]), "+v"(w2[8]), "+v"(w2[9]), "+v"(w2[10]),
                 "+v"(w2[11]), "+v"(w2[12]), "+v"(w2[13]), "+v"(w2[14]), "+v"(w2[15]), "+v"(w2[16]), "+v"(w2[17]), "+v"(w2[18]), "+v"(w2[19]), "+v"(w2[20]), "+v"(w2[21])
               ::"memory");
  __builtin_amdgcn_s_barrier();
  STAMP(ws, 2, 8);
  {
    const char* base = sm + kq * 4096 + lane * 16;
    u32x4 hb[2][4];
#pragma unroll
    for (int m = 0; m < 4; m++) hb[0][m] = *(const u32x4*)(base + m * 1024);
#pragma unroll
    for (int s = 0; s < 11; s++) {
      if (s == 1) { asm volatile("s_waitcnt vmcnt(4)" ::: "memory"); __builtin_amdgcn_s_barrier(); }
      else if (s == 3 || s == 5) { asm volatile("s_waitcnt vmcnt(6)" ::: "memory"); __builtin_amdgcn_s_barrier(); }
      else if (s == 7) { asm volatile("s_waitcnt vmcnt(2)" ::: "memory"); __builtin_amdgcn_s_barrier(); }
      else if (s == 9) { asm volatile("s_waitcnt vmcnt(0)" ::: "memory"); __builtin_amdgcn_s_barrier(); }
      const int slot = s + 1 < 9 ? s + 1 : s + 1 - 9;
#pragma unroll
      for (int m = 0; m < 4; m++) {
        acc2[0][m] = mfma(w2[2 * s], hb[s & 1][m], acc2[0][m]);
        SCHED_FENCE();
        if (s < 10) hb[(s + 1) & 1][m] = *(const u32x4*)(base + slot * 16384 + m * 1024);
        SCHED_FENCE();
        acc2[1][m] = mfma(w2[2 * s + 1], hb[s & 1][m], acc2[1][m]);
        SCHED_FENCE();
        if (s < 2) stage1(4 + 2 * s + (m >> 1), m & 1);
        else if (s < 5 && (m & 1)) stage1(s + 6, m >> 1);
        SCHED_FENCE();
      }
      if (s == 2) STAMP(ws, 2, 3);
      if (s == 5) STAMP(ws, 2, 11);
      if (s == 8) STAMP(ws, 2, 15);
    }
  }
  asm volatile("s_waitcnt lgkmcnt(0)" ::: "memory");
  __builtin_amdgcn_s_barrier();
  STAMP(ws, 2, 9);
  {
    char* red = sm + L_RED + dh * 32768;
    f32x4 o0, o1;
    auto scatter = [&](auto KQ) {
      constexpr int k = decltype(KQ)::value;
#pragma unroll
      for (int m = 0; m < 4; m++) {
        if (m != k) {
#pragma unroll
          for (int i = 0; i < 2; i++) *(f32x4*)(red + (((k * 4 + m) * 2 + i) * 64 + lane) * 16) = acc2[i][m];
        }
      }
      o0 = acc2[0][k]; o1 = acc2[1][k];
      asm volatile("" : "+v"(o0), "+v"(o1));
    };
    if (kq == 0) scatter(IntC<0>{}); else if (kq == 1) scatter(IntC<1>{}); else if (kq == 2) scatter(IntC<2>{}); else scatter(IntC<3>{});
    __syncthreads();
#pragma unroll
    for (int j = 1; j < 4; j++) {
      const int src = (kq + j) & 3;
      o0 += *(const f32x4*)(red + (((src * 4 + kq) * 2 + 0) * 64 + lane) * 16);
      o1 += *(const f32x4*)(red + (((src * 4 + kq) * 2 + 1) * 64 + lane) * 16);
    }
    const u32x4 hres = *(const u32x4*)(sm + L_RES + (w * 64 + lane) * 16);
    u32x4 res;
#pragma unroll
    for (int r = 0; r < 4; r++) res[r] = pack_bf(bf_lo(hres[r]) + rb(o0[r]), bf_hi(hres[r]) + rb(o1[r]));
    __builtin_amdgcn_raw_buffer_store_b128(res, mk_rsrc(OUT, S * D * 2), (u32)((row0 + 16 * kq + n) * D + 64 * c + 32 * dh + 8 * g) * 2, 0, 2);
  }
  STAMP(ws, 2, 10);
  STAMP_FLUSH(ws, 2);
  asm volatile("ffn_text_end:");
}

extern "C" void kernel_launch(void* const* d_in, const int* in_sizes, int n_in, void* d_out, int out_size, void* d_ws,
                              size_t ws_size, hipStream_t stream) {
  const u16* X = (const u16*)d_in[0];
  const u16* K = (const u16*)d_in[1];
  const u16* V = (const u16*)d_in[2];
  const u16* SK = (const u16*)d_in[3];
  const u16* HK = (const u16*)d_in[4];
  const u16* DV = (const u16*)d_in[5];
  const u16* W1 = (const u16*)d_in[7];
  const u16* W2 = (const u16*)d_in[8];
  char* ws = (char*)d_ws;
  k1_prep<<<256, 512, 0, stream>>>(X, K, V, SK, HK, ws);
  k2_conv<<<256, 512, 0, stream>>>(HK, DV, W1, W2, ws);
  k34_ffn<<<256, 512, 0, stream>>>(ws, (u16*)d_out);
}
```

```cpp
#include <hip/hip_runtime.h>
#include <stdint.h>

typedef unsigned short u16;
typedef unsigned int u32;
typedef unsigned long long u64;
typedef __bf16 bf16x8 __attribute__((ext_vector_type(8)));
typedef __bf16 bf16x2 __attribute__((ext_vector_type(2)));
typedef short s16x4 __attribute__((ext_vector_type(4)));
typedef float f32x4 __attribute__((ext_vector_type(4)));
typedef float f32x2 __attribute__((ext_vector_type(2)));
typedef u32 u32x4 __attribute__((ext_vector_type(4)));
typedef u32 u32x2 __attribute__((ext_vector_type(2)));
#define GAS __attribute__((address_space(1)))
#define LAS __attribute__((address_space(3)))
#define RLX_AGENT __ATOMIC_RELAXED, __HIP_MEMORY_SCOPE_AGENT
#define SCHED_FENCE() __builtin_amdgcn_sched_barrier(0)

constexpr int S = 2048, D = 512, F = 1376;

constexpr size_t WS_FLAG = 0;
constexpr size_t WS_KVT  = 1 << 16;
constexpr size_t WS_XT   = WS_KVT + (size_t)D * S * 2;
constexpr size_t WS_HT   = WS_XT + (size_t)D * S * 2;
constexpr size_t WS_W1T  = WS_HT + (size_t)D * S * 2;
constexpr size_t WS_W2T  = WS_W1T + 8 * 11 * 16 * 1024;
constexpr size_t WS_CSP  = WS_W2T + 8 * 4 * 44 * 1024;
constexpr size_t WS_ST1  = WS_CSP + 16 * 8 * 11 * 16 * 4;
constexpr size_t WS_HF   = WS_ST1 + S * 8;
constexpr size_t WS_END  = WS_HF + (size_t)32 * 44 * 4 * 1024;

__device__ __forceinline__ float bf_lo(u32 v) { return __uint_as_float(v << 16); }
__device__ __forceinline__ float bf_hi(u32 v) { return __uint_as_float(v & 0xffff0000u); }
__device__ __forceinline__ u32 pack_bf(float a, float b) {
  f32x2 z = {a, b};
  return __builtin_bit_cast(u32, __builtin_convertvector(z, bf16x2));
}
__device__ __forceinline__ float rb(float a) { return bf_lo(pack_bf(a, 0.f)); }
__device__ __forceinline__ float dot2(u32 a, u32 b, float c) {
  return __builtin_amdgcn_fdot2_f32_bf16(__builtin_bit_cast(bf16x2, a), __builtin_bit_cast(bf16x2, b), c, false);
}
__device__ __forceinline__ u32 perm_lo(u32 a, u32 b) { return __builtin_amdgcn_perm(b, a, 0x05040100u); }
__device__ __forceinline__ u32 perm_hi(u32 a, u32 b) { return __builtin_amdgcn_perm(b, a, 0x07060302u); }
__device__ __forceinline__ f32x4 mfma(u32x4 a, u32x4 b, f32x4 c) {
  return __builtin_amdgcn_mfma_f32_16x16x32_bf16(__builtin_bit_cast(bf16x8, a), __builtin_bit_cast(bf16x8, b), c, 0, 0, 0);
}
constexpr u32 ONES = 0x3F803F80u;
template <int V> struct IntC { static constexpr int value = V; };
__device__ __forceinline__ float sum_g(float x) {
  auto s = __builtin_amdgcn_permlane16_swap(__float_as_uint(x), __float_as_uint(x), false, false);
  x = __uint_as_float(s[0]) + __uint_as_float(s[1]);
  auto t = __builtin_amdgcn_permlane32_swap(__float_as_uint(x), __float_as_uint(x), false, false);
  return __uint_as_float(t[0]) + __uint_as_float(t[1]);
}
template <int CTRL>
__device__ __forceinline__ float dpp_add(float x) {
  return x + __uint_as_float(__builtin_amdgcn_update_dpp(0u, __float_as_uint(x), CTRL, 0xf, 0xf, true));
}
__device__ __forceinline__ float wave_sum(float x) {
  x = dpp_add<0xB1>(x);
  x = dpp_add<0x4E>(x);
  x = dpp_add<0x141>(x);
  x = dpp_add<0x140>(x);
  return sum_g(x);
}
__device__ __forceinline__ __amdgpu_buffer_rsrc_t mk_rsrc(const void* p, u32 bytes) {
  return __builtin_amdgcn_make_buffer_rsrc((void*)p, 0, bytes, 0x00020000);
}
__device__ __forceinline__ u32x4 bload16(__amdgpu_buffer_rsrc_t r, u32 off) { return __builtin_amdgcn_raw_buffer_load_b128(r, off, 0, 0); }
template <bool SC1>
__device__ __forceinline__ void glds16(__amdgpu_buffer_rsrc_t r, u32 voff, u32 soff, u32 lds_dst) {
  if (SC1)
    asm volatile("s_mov_b32 m0, %0\n\ts_nop 0\n\tbuffer_load_dwordx4 %1, %2, %3 offen sc1 lds" ::"s"(lds_dst), "v"(voff), "s"(r), "s"(soff) : "memory");
  else
    asm volatile("s_mov_b32 m0, %0\n\ts_nop 0\n\tbuffer_load_dwordx4 %1, %2, %3 offen lds" ::"s"(lds_dst), "v"(voff), "s"(r), "s"(soff) : "memory");
}
__device__ __forceinline__ void bload16_asm(u32x4& d, __amdgpu_buffer_rsrc_t r, u32 voff, u32 soff) {
  asm volatile("buffer_load_dwordx4 %0, %1, %2, %3 offen" : "=v"(d) : "v"(voff), "s"(r), "s"(soff) : "memory");
}
#define K1_AUX 0
#define WT_AUX 16
#define K2_AUX 16
template <int AUX>
__device__ __forceinline__ void gstore16(char* base, size_t off, u32x4 v) {
  const __amdgpu_buffer_rsrc_t r = mk_rsrc(base, 0x7fffffffu);
  __builtin_amdgcn_raw_buffer_store_b128(v, r, (u32)off, 0, AUX);
}
template <int AUX>
__device__ __forceinline__ void gstore8(char* base, size_t off, u32x2 v) {
  const __amdgpu_buffer_rsrc_t r = mk_rsrc(base, 0x7fffffffu);
  __builtin_amdgcn_raw_buffer_store_b64(v, r, (u32)off, 0, AUX);
}
__device__ __forceinline__ u32x4 bload16s(__amdgpu_buffer_rsrc_t r, u32 voff, u32 soff) { return __builtin_amdgcn_raw_buffer_load_b128(r, voff, soff, 0); }
__device__ __forceinline__ u32x2 tr_read(const char* p) {
  s16x4 r = __builtin_amdgcn_ds_read_tr16_b64_v4i16((LAS s16x4*)p);
  return __builtin_bit_cast(u32x2, r);
}

__device__ __forceinline__ void code_prefetch(int part, int nparts, int lane) {
  u64 pc; u32 len;
  asm volatile("s_getpc_b64 %0\n\ts_mov_b32 %1, ffn_text_end-." : "=s"(pc), "=s"(len));
  const __amdgpu_buffer_rsrc_t r = mk_rsrc((const void*)pc, len);
  u32 acc = 0;
  for (u32 o = part * 1024; o < len; o += nparts * 1024) acc |= __builtin_amdgcn_raw_buffer_load_b128(r, o + lane * 16, 0, 0)[0];
  asm volatile("" ::"v"(acc));
}
__device__ __forceinline__ int fcol(int ks, int g, int j) {
  const int slot = 8 * (g >> 1) + j;
  if (ks < 40) { const int ct = ks / 5, p = ks - 5 * ct; return 128 * (2 * p + (g & 1)) + 8 * slot + ct; }
  return 1280 + 8 * slot + 2 * (ks - 40) + (g & 1);
}

__global__ __launch_bounds__(512, 2) void k1_prep(const u16* __restrict__ X, const u16* __restrict__ K, const u16* __restrict__ V,
                                                  const u16* __restrict__ SK, const u16* __restrict__ HK, char* __restrict__ ws) {
  __shared__ __attribute__((aligned(16))) char sm[16384 + 4096];
  const int b = blockIdx.x, t = threadIdx.x, lane = t & 63;
  const int w = __builtin_amdgcn_readfirstlane(t >> 6);
  const int dt = b & 7, st = b >> 3;
  if (w < 4) {
    const int rp = t >> 3, c = t & 7;
    const int s = st * 64 + 2 * rp, ch0 = dt * 64 + c * 8;
    const __amdgpu_buffer_rsrc_t rk = mk_rsrc(K, S * D * 2);
    const u32 off = (u32)(s * D + ch0) * 2;
    u32x4 kk[5];
#pragma unroll
    for (int i = 0; i < 5; i++) kk[i] = bload16(rk, off + (1 - i) * 1024);
    const u32x4 v0 = *(const u32x4*)((const char*)V + off), v1 = *(const u32x4*)((const char*)V + off + 1024);
    const u32x4 x0 = *(const u32x4*)((const char*)X + off), x1 = *(const u32x4*)((const char*)X + off + 1024);
    u32x4 wa[4];
#pragma unroll
    for (int i = 0; i < 4; i++) wa[i] = *(const u32x4*)(SK + ch0 * 4 + i * 8);
    SCHED_FENCE();
    const int swz = ((rp >> 2) ^ c) << 4;
    char* pk = sm + (8 * c) * 128 + swz + ((3 - (rp & 3)) << 2);
    char* px = sm + 8192 + (8 * c) * 128 + swz + ((rp & 3) << 2);
#pragma unroll
    for (int i = 0; i < 4; i++) {
      const u32 l01 = perm_lo(kk[0][i], kk[1][i]), l12 = perm_lo(kk[1][i], kk[2][i]), l23 = perm_lo(kk[2][i], kk[3][i]), l34 = perm_lo(kk[3][i], kk[4][i]);
      const u32 h01 = perm_hi(kk[0][i], kk[1][i]), h12 = perm_hi(kk[1][i], kk[2][i]), h23 = perm_hi(kk[2][i], kk[3][i]), h34 = perm_hi(kk[3][i], kk[4][i]);
      const float a1 = dot2(l01, wa[i][0], dot2(l23, wa[i][1], 0.f)) * bf_lo(v1[i]);
      const float a0 = dot2(l12, wa[i][0], dot2(l34, wa[i][1], 0.f)) * bf_lo(v0[i]);
      const float b1 = dot2(h01, wa[i][2], dot2(h23, wa[i][3], 0.f)) * bf_hi(v1[i]);
      const float b0 = dot2(h12, wa[i][2], dot2(h34, wa[i][3], 0.f)) * bf_hi(v0[i]);
      *(u32*)(pk + (2 * i) * 128) = pack_bf(a1, a0);
      *(u32*)(pk + (2 * i + 1) * 128) = pack_bf(b1, b0);
      *(u32*)(px + (2 * i) * 128) = perm_lo(x0[i], x1[i]);
      *(u32*)(px + (2 * i + 1) * 128) = perm_hi(x0[i], x1[i]);
    }
    __syncthreads();
#pragma unroll
    for (int i = 0; i < 2; i++) {
      const int u = t + 256 * i, ch = u >> 3, cc = u & 7;
      const u32x4 o = *(const u32x4*)(sm + ch * 128 + cc * 16);
      const int grp = cc ^ ((ch >> 3) & 7);
      const size_t go = (size_t)(dt * 64 + ch) * S + st * 64 + grp * 8;
      gstore16<K1_AUX>(ws, WS_KVT + go * 2, o);
      gstore16<K1_AUX>(ws, WS_XT + go * 2, *(const u32x4*)(sm + 8192 + ch * 128 + cc * 16));
    }
  } else {
    const int lrow = 256 * (b & 7) + 8 * (b >> 3) + 2 * (w - 4);
    u32x4 xr[2];
#pragma unroll
    for (int i = 0; i < 2; i++) xr[i] = *(const u32x4*)(X + (size_t)(lrow + i) * D + lane * 8);
    if (w == 4 && lane < 32) __hip_atomic_store((GAS u32*)(ws + WS_FLAG) + b * 64 + lane, 0u, RLX_AGENT);
    {
      const u32 dump = (u32)(size_t)(sm + 16384) + (w - 4) * 1024;
      const int chp = 64 * (b & 7) + 2 * (b >> 3);
      const __amdgpu_buffer_rsrc_t rh = mk_rsrc(HK + (size_t)chp * S, 2 * S * 2);
      glds16<false>(rh, lane * 16, (w - 4) * 2048, dump);
      glds16<false>(rh, lane * 16, (w - 4) * 2048 + 1024, dump);
    }
    code_prefetch((b >> 3) * 4 + (w - 4), 128, lane);
    __builtin_amdgcn_s_barrier();
#pragma unroll
    for (int q = 0; q < 2; q++) {
      float sum = 0.f;
#pragma unroll
      for (int i = 0; i < 4; i++) sum = dot2(xr[q][i], ONES, sum);
      const float mean = wave_sum(sum) * (1.f / D);
      float sq = 0.f;
#pragma unroll
      for (int i = 0; i < 4; i++) { const float d0 = bf_lo(xr[q][i]) - mean, d1 = bf_hi(xr[q][i]) - mean; sq += d0 * d0 + d1 * d1; }
      sq = wave_sum(sq);
      const float sd = rb(__builtin_amdgcn_sqrtf(rb(rb(sq * (1.f / D)) + 1e-5f)));
      const float r1 = __builtin_amdgcn_rcpf(sd);
      if (lane == 0) { f32x2 o2 = {r1, -rb(mean) * r1}; *(f32x2*)(ws + WS_ST1 + (size_t)(lrow + q) * 8) = o2; }
    }
    asm volatile("s_waitcnt vmcnt(0)" ::: "memory");
  }
}

constexpr int HC_STRIDE = 4608;
constexpr int HC_CH = 8 * HC_STRIDE;
constexpr int U_CH = (240 + 2048) * 2;
constexpr int K2_H = 0, K2_U = 2 * HC_CH, K2_R = K2_U + 2 * U_CH + 32, K2_F = K2_R + 4 * 4096, K2_END = K2_F + 32;
__device__ __forceinline__ constexpr int hoff(int c) {
  return c == 0 ? 4 : c == 1 ? 2 : c == 2 ? 8 : c == 3 ? 3 : c == 4 ? 7 : c == 5 ? 0 : c == 6 ? 15 : 12;
}
__device__ __forceinline__ int hoff_rt(int c) { return (0xCF073824u >> (4 * c)) & 15; }
__device__ __forceinline__ void lds_wait(const char* p, u32 want) {
  for (u32 spins = 0; spins < (1u << 22); spins++) {
    if (__builtin_amdgcn_readfirstlane(*(volatile LAS u32*)p) == want) break;
    __builtin_amdgcn_s_sleep(1);
  }
  asm volatile("" ::: "memory");
}

__global__ __launch_bounds__(512, 2) void k2_conv(const u16* __restrict__ HK, const u16* __restrict__ DV,
                                                  const u16* __restrict__ W1, const u16* __restrict__ W2, char* __restrict__ ws) {
  __shared__ __attribute__((aligned(16))) char sm[K2_END];
  const int b = blockIdx.x, t = threadIdx.x, lane = t & 63;
  const int w = __builtin_amdgcn_readfirstlane(t >> 6);
  const int chp = 64 * (b & 7) + 2 * (b >> 3);
  const int wcl = (w >> 1) & 1, rh = w & 1;
  const int n = lane & 15, g = lane >> 4;
  {
    const int cl = t >> 8, j = t & 255;
    const __amdgpu_buffer_rsrc_t rh_ = mk_rsrc(HK + (size_t)chp * S, 2 * S * 2);
    const u32 ho = (u32)(cl * S + 8 * j) * 2;
    u32x4 ha, hb, uu;
    const __amdgpu_buffer_rsrc_t ru = mk_rsrc(ws + WS_KVT + (size_t)chp * S * 2, 2 * S * 2);
    bload16_asm(ha, rh_, ho - 16, 0);
    bload16_asm(hb, rh_, ho, 0);
    bload16_asm(uu, ru, ho, 0);
    asm volatile("s_waitcnt vmcnt(0)" : "+v"(ha), "+v"(hb), "+v"(uu)::"memory");
    if (t < 8) *(volatile LAS u32*)(sm + K2_F + t * 4) = 0u;
    SCHED_FENCE();
    if (j == 0) ha = u32x4{0, 0, 0, 0};
    char* hbase = sm + K2_H + cl * HC_CH;
    const u32 W[8] = {ha[0], ha[1], ha[2], ha[3], hb[0], hb[1], hb[2], hb[3]};
#pragma unroll
    for (int c = 0; c < 8; c++) {
      u32x4 o;
#pragma unroll
      for (int a = 0; a < 4; a++) o[a] = (c & 1) ? __builtin_amdgcn_alignbit(W[(c >> 1) + a + 1], W[(c >> 1) + a], 16) : W[(c >> 1) + a];
      *(u32x4*)(hbase + c * HC_STRIDE + 16 * (j + 1 + hoff(c))) = o;
    }
    const u32x4 z4 = {0, 0, 0, 0};
    if (j < 8) *(u32x4*)(hbase + j * HC_STRIDE + 16 * hoff_rt(j)) = z4;
    if (j == 255) *(u32x4*)(hbase + 16 * (257 + hoff(0))) = hb;
    char* ub = sm + K2_U + cl * U_CH;
    *(u32x4*)(ub + 480 + 16 * j) = uu;
    if (j < 30) *(u32x4*)(ub + 16 * j) = z4;
  }
  u32x2 xq[4];
  f32x4 sa[4], sb[4];
  u32 dvr = 0;
  if (w < 4) {
    const __amdgpu_buffer_rsrc_t rs1 = mk_rsrc(ws + WS_ST1, S * 8);
    const __amdgpu_buffer_rsrc_t rx = mk_rsrc(ws + WS_XT + (size_t)(chp + wcl) * S * 2, S * 2);
    const u32 p0 = 1024 * rh + 16 * n + 4 * g;
#pragma unroll
    for (int i = 0; i < 4; i++) {
      asm volatile("buffer_load_dwordx2 %0, %1, %2, %3 offen" : "=v"(xq[i]) : "v"(p0 * 2), "s"(rx), "s"(i * 512) : "memory");
      asm volatile("buffer_load_dwordx4 %0, %1, %2, %3 offen" : "=v"(sa[i]) : "v"(p0 * 8), "s"(rs1), "s"(i * 2048) : "memory");
      asm volatile("buffer_load_dwordx4 %0, %1, %2, %3 offen offset:16" : "=v"(sb[i]) : "v"(p0 * 8), "s"(rs1), "s"(i * 2048) : "memory");
    }
    dvr = *(const u32*)(DV + chp) >> (16 * wcl);
  }
  __syncthreads();
  if (w < 4) {
    f32x4 acc[8];
#pragma unroll
    for (int i = 0; i < 8; i++) acc[i] = f32x4{0.f, 0.f, 0.f, 0.f};
    const int idx0 = 25 + n - 8 * g;
    const int c = idx0 & 7;
    const char* fbase = sm + K2_H + wcl * HC_CH + c * HC_STRIDE + 16 * ((idx0 >> 3) + hoff_rt(c)) + 64 * 4 * rh;
    const char* gbase = sm + K2_U + wcl * U_CH + 2 * (16 * n + 8 * g + 224) - 64 * 4 * rh;
    u32x4 Ff[2][8], Gf[2][8];
#pragma unroll
    for (int a = 0; a < 8; a++) { Gf[0][a] = *(const u32x4*)(gbase + 64 * (8 * a)); Ff[0][a] = *(const u32x4*)(fbase + 64 * (8 * a)); }
    SCHED_FENCE();
#pragma unroll
    for (int rr = 0; rr < 4; rr++) {
      const int cur = rr & 1, nxt = cur ^ 1;
      int m = 0;
#pragma unroll
      for (int bb = 0; bb < 8; bb++) {
#pragma unroll
        for (int a = 0; a + bb < 8; a++) {
          acc[a + bb] = mfma(Ff[cur][a], Gf[cur][bb], acc[a + bb]);
          if (rr < 3 && (m & 1) == 1 && m < 32) {
            const int q = m >> 2;
            SCHED_FENCE();
            if (m & 2) Ff[nxt][q] = *(const u32x4*)(fbase + 64 * (8 * q + rr + 1));
            else Gf[nxt][q] = *(const u32x4*)(gbase + 64 * (8 * q - rr - 1));
            SCHED_FENCE();
          }
          m++;
        }
      }
      SCHED_FENCE();
    }
    f32x4* rbuf = (f32x4*)(sm + K2_R) + w * 256;
    if (rh) {
#pragma unroll
      for (int i = 0; i < 4; i++) rbuf[i * 64 + lane] = acc[i];
#pragma unroll
      for (int i = 0; i < 4; i++) acc[i] = acc[4 + i];
    } else {
#pragma unroll
      for (int i = 0; i < 4; i++) rbuf[i * 64 + lane] = acc[4 + i];
    }
    asm volatile("s_waitcnt lgkmcnt(0)" ::: "memory");
    if (lane == 0) *(volatile LAS u32*)(sm + K2_F + w * 4) = 1u;
    const int ch = chp + wcl;
    const char* ub = sm + K2_U + wcl * U_CH;
    u32x2 uq[4];
#pragma unroll
    for (int i = 0; i < 4; i++) uq[i] = *(const u32x2*)(ub + 2 * (240 + 256 * (4 * rh + i) + 16 * n + 8 * (g >> 1) + ((g & 1) ? 0 : 4)));
    asm volatile("s_waitcnt vmcnt(0)" : "+v"(xq[0]), "+v"(xq[1]), "+v"(xq[2]), "+v"(xq[3]), "+v"(sa[0]), "+v"(sa[1]), "+v"(sa[2]), "+v"(sa[3]),
                 "+v"(sb[0]), "+v"(sb[1]), "+v"(sb[2]), "+v"(sb[3])::"memory");
    lds_wait(sm + K2_F + (w ^ 1) * 4, 1u);
    const float dv = bf_lo(dvr);
    const f32x4* pbuf = (const f32x4*)(sm + K2_R) + (w ^ 1) * 256;
#pragma unroll
    for (int i = 0; i < 4; i++) {
      const int T = 4 * rh + i;
      const f32x4 a = acc[i] + pbuf[i * 64 + lane];
      const int pos = 256 * T + 16 * n + 4 * g;
      const float y0 = fmaf(bf_hi(uq[i][1]), dv, a[0]), y1 = fmaf(bf_lo(uq[i][1]), dv, a[1]), y2 = fmaf(bf_hi(uq[i][0]), dv, a[2]), y3 = fmaf(bf_lo(uq[i][0]), dv, a[3]);
      const float x0 = bf_lo(xq[i][0]), x1 = bf_hi(xq[i][0]), x2 = bf_lo(xq[i][1]), x3 = bf_hi(xq[i][1]);
      const float h0 = fmaf(fmaf(x0, sa[i][0], sa[i][1]), y0, x0), h1 = fmaf(fmaf(x1, sa[i][2], sa[i][3]), y1, x1);
      const float h2 = fmaf(fmaf(x2, sb[i][0], sb[i][1]), y2, x2), h3 = fmaf(fmaf(x3, sb[i][2], sb[i][3]), y3, x3);
      const u32x2 o = {pack_bf(h0, h1), pack_bf(h2, h3)};
      gstore8<K2_AUX>(ws, WS_HT + ((size_t)ch * S + pos) * 2, o);
    }
  } else {
    const int unit = b + 256 * (w - 4);
    const int lc = lane & 15, lg = lane >> 4;
    if (unit < 176) {
      const __amdgpu_buffer_rsrc_t rw = mk_rsrc(W1, D * F * 2);
      const int blk = unit >> 4, ks = unit & 15;
      const int col = 128 * blk + 8 * lc;
      const u32 o0 = col < F ? (u32)((32 * ks + 8 * lg) * F + col) * 2 : 0x80000000u;
      u32x4 wl[8];
#pragma unroll
      for (int j = 0; j < 8; j++) wl[j] = bload16(rw, o0 + j * F * 2);
#pragma unroll
      for (int i = 0; i < 8; i++) {
        u32x4 o;
#pragma unroll
        for (int a = 0; a < 4; a++) o[a] = (i & 1) ? perm_hi(wl[2 * a][i >> 1], wl[2 * a + 1][i >> 1]) : perm_lo(wl[2 * a][i >> 1], wl[2 * a + 1][i >> 1]);
        gstore16<WT_AUX>(ws, WS_W1T + ((size_t)((i * 11 + blk) * 16 + ks) * 64 + lane) * 16, o);
        const float cs = sum_g(dot2(o[0], ONES, dot2(o[1], ONES, dot2(o[2], ONES, dot2(o[3], ONES, 0.f)))));
        if (lane < 16) *(float*)(ws + WS_CSP + ((size_t)(i * 176 + blk * 16 + lane) * 16 + ks) * 4) = cs;
      }
    } else if (unit < 528) {
      const __amdgpu_buffer_rsrc_t rw = mk_rsrc(W2, D * F * 2);
      const int u2 = unit - 176, ks = u2 >> 3, c2 = u2 & 7;
      u32 wx[8], wy[8];
#pragma unroll
      for (int j = 0; j < 8; j++) {
        const u32 o = (u32)(fcol(ks, lg, j) * D + 64 * c2 + 8 * (lc >> 2) + 2 * (lc & 3)) * 2;
        wx[j] = __builtin_amdgcn_raw_buffer_load_b32(rw, o, 0, 0);
        wy[j] = __builtin_amdgcn_raw_buffer_load_b32(rw, o + 64, 0, 0);
      }
#pragma unroll
      for (int i = 0; i < 4; i++) {
        u32x4 o;
#pragma unroll
        for (int a = 0; a < 4; a++) {
          const u32 e0 = (i >> 1) ? wy[2 * a] : wx[2 * a], e1 = (i >> 1) ? wy[2 * a + 1] : wx[2 * a + 1];
          o[a] = (i & 1) ? perm_hi(e0, e1) : perm_lo(e0, e1);
        }
        gstore16<WT_AUX>(ws, WS_W2T + ((size_t)(((c2 * 2 + (i >> 1)) * 44 + ks) * 2 + (i & 1)) * 64 + lane) * 16, o);
      }
    }
  }
}

constexpr int L_HT = 0;
constexpr int L_ST = 147456;
constexpr int L_CS = L_ST + 512;
constexpr int L_RES = L_CS + 768;
constexpr int L_END = L_RES + 8192;
constexpr int L_RED = 0;
constexpr int L_FLG = L_END;
constexpr int L_TOT = L_END + 16;
#define W1_PRE 4
#define W2_LOADS                                                                                                                   \
  "buffer_load_dwordx4 %0, %14, %15, %16 offen\n\tbuffer_load_dwordx4 %1, %14, %15, %16 offen offset:1024\n\t"                      \
  "buffer_load_dwordx4 %2, %14, %15, %16 offen offset:2048\n\tbuffer_load_dwordx4 %3, %14, %15, %16 offen offset:3072\n\t"          \
  "buffer_load_dwordx4 %4, %14, %15, %17 offen\n\tbuffer_load_dwordx4 %5, %14, %15, %17 offen offset:1024\n\t"                      \
  "buffer_load_dwordx4 %6, %14, %15, %17 offen offset:2048\n\tbuffer_load_dwordx4 %7, %14, %15, %17 offen offset:3072\n\t"          \
  "buffer_load_dwordx4 %8, %14, %15, %18 offen\n\tbuffer_load_dwordx4 %9, %14, %15, %18 offen offset:1024\n\t"                      \
  "buffer_load_dwordx4 %10, %14, %15, %18 offen offset:2048\n\tbuffer_load_dwordx4 %11, %14, %15, %18 offen offset:3072\n\t"        \
  "buffer_load_dwordx4 %12, %14, %15, %19 offen\n\tbuffer_load_dwordx4 %13, %14, %15, %19 offen offset:1024\n"
__device__ __forceinline__ void load_w2_tail_support(u32x4 (&w2)[22], __amdgpu_buffer_rsrc_t r, u32 voff, int w) {
  asm volatile("s_cmp_lt_u32 %20, 4\n\ts_cbranch_scc1 1f\n\t" W2_LOADS "1:"
               : "=&v"(w2[8]), "=&v"(w2[9]), "=&v"(w2[10]), "=&v"(w2[11]), "=&v"(w2[12]), "=&v"(w2[13]), "=&v"(w2[14]), "=&v"(w2[15]), "=&v"(w2[16]),
                 "=&v"(w2[17]), "=&v"(w2[18]), "=&v"(w2[19]), "=&v"(w2[20]), "=&v"(w2[21])
               : "v"(voff), "s"(r), "s"(8192), "s"(12288), "s"(16384), "s"(20480), "s"(w) : "memory", "scc");
}
__device__ __forceinline__ void load_w2_tail_main(u32x4 (&w2)[22], __amdgpu_buffer_rsrc_t r, u32 voff, int w) {
  asm volatile("s_cmp_lt_u32 %20, 4\n\ts_cbranch_scc0 1f\n\t" W2_LOADS "1:"
               : "+v"(w2[8]), "+v"(w2[9]), "+v"(w2[10]), "+v"(w2[11]), "+v"(w2[12]), "+v"(w2[13]), "+v"(w2[14]), "+v"(w2[15]), "+v"(w2[16]),
                 "+v"(w2[17]), "+v"(w2[18]), "+v"(w2[19]), "+v"(w2[20]), "+v"(w2[21])
               : "v"(voff), "s"(r), "s"(8192), "s"(12288), "s"(16384), "s"(20480), "s"(w) : "memory", "scc");
}
__device__ __forceinline__ float silu_val(float acc, float rs, float bias) {
  const float tt = fmaf(acc, rs, bias);
  const float e = __builtin_amdgcn_exp2f(tt);
  return tt * __builtin_amdgcn_rcpf(fmaf(e, -1.44269504089f, -1.44269504089f));
}

__global__ __launch_bounds__(512, 2) void k34_ffn(char* __restrict__ ws, u16* __restrict__ OUT) {
  __shared__ __attribute__((aligned(16))) char sm[L_TOT];
  const int b = blockIdx.x, t = threadIdx.x, lane = t & 63;
  const int w = __builtin_amdgcn_readfirstlane(t >> 6);
  const int rt = 8 * (b & 3) + ((b >> 3) & 7), c = 4 * ((b >> 2) & 1) + (b >> 6);
  const int n = lane & 15, g = lane >> 4;
  const int row0 = 64 * rt;
  {
    const __amdgpu_buffer_rsrc_t rh = mk_rsrc(ws + WS_HT + ((size_t)(64 * w) * S + row0) * 2, 64 * S * 2);
    const int f0 = 2 * ((lane >> 4) & 1);
    const u32 v0 = (u32)(lane >> 3) * S * 2 + (((lane & 7) ^ f0) << 4), v1 = (u32)(lane >> 3) * S * 2 + (((lane & 7) ^ (f0 + 4)) << 4);
#pragma unroll
    for (int i = 0; i < 8; i++) glds16<false>(rh, (i & 1) ? v1 : v0, i * 8 * S * 2, (u32)(size_t)(sm + L_HT) + (64 * w + 8 * i) * 128);
  }
  if (t < 4) *(volatile LAS u32*)(sm + L_FLG + t * 4) = 0u;
  __builtin_amdgcn_s_barrier();
  const __amdgpu_buffer_rsrc_t rw2 = mk_rsrc(ws + WS_W2T + (size_t)((c * 2 + (w & 1)) * 44 + 11 * (w >> 1)) * 2048, 22 * 1024);
  const int q4 = (lane & 15) >> 2, p4 = lane & 3;
  const int fsw = 2 * ((q4 >> 1) & 1) + 4 * (g & 1);
  auto save_residual = [&]() {
    const char* p = sm + L_HT + (8 * g + q4) * 128 + (((2 * (w >> 1) + (p4 >> 1)) ^ fsw) << 4) + 8 * (p4 & 1) + (2 * c + (w & 1)) * 4096;
    const u32x2 a = tr_read(p), bq = tr_read(p + 512);
    *(u32x4*)(sm + L_RES + (w * 64 + lane) * 16) = u32x4{a[0], a[1], bq[0], bq[1]};
  };
  if (w >= 4) {
    f32x4 csp[4];
    const __amdgpu_buffer_rsrc_t rc = mk_rsrc(ws + WS_CSP + (size_t)c * 176 * 64, 176 * 64);
    const int cj = (w - 4) * 64 + lane;
#pragma unroll
    for (int i = 0; i < 4; i++) csp[i] = __builtin_bit_cast(f32x4, bload16s(rc, (u32)(cj < 176 ? cj : 175) * 64, i * 16));
    asm volatile("s_waitcnt vmcnt(4) lgkmcnt(0)" ::: "memory");
    __builtin_amdgcn_s_barrier();
    __builtin_amdgcn_s_setprio(3);
    const char* p = sm + L_HT + (8 * g + q4) * 128 + (((2 * (w - 4) + (p4 >> 1)) ^ fsw) << 4) + 8 * (p4 & 1);
    u32x2 fa[16][2];
#pragma unroll
    for (int ks = 0; ks < 16; ks++) { fa[ks][0] = tr_read(p + ks * 4096); fa[ks][1] = tr_read(p + ks * 4096 + 512); }
    f32x4 q0 = {0.f, 0.f, 0.f, 0.f}, q1 = {0.f, 0.f, 0.f, 0.f};
    const u32x4 ones = {ONES, ONES, ONES, ONES};
#pragma unroll
    for (int ks = 0; ks < 16; ks++) {
      const u32x4 f0 = {fa[ks][0][0], fa[ks][0][1], fa[ks][1][0], fa[ks][1][1]};
      q0 = mfma(f0, f0, q0);
      q1 = mfma(ones, f0, q1);
    }
    {
      const float lo = (n & 1) ? q0[1] : q0[0], hi = (n & 1) ? q0[3] : q0[2];
      const float q = (n & 2) ? hi : lo;
      const float mu = q1[0] * (1.f / D);
      const float var = fmaxf(q * (1.f / D) - mu * mu, 0.f);
      const float rstd = __builtin_amdgcn_rcpf(rb(__builtin_amdgcn_sqrtf(rb(rb(var) + 1e-5f))));
      if (g == (n >> 2)) *(f32x2*)(sm + L_ST + ((w - 4) * 16 + n) * 8) = f32x2{-1.44269504089f * rstd, rb(mu) * rstd * 1.44269504089f};
    }
    {
      const f32x4 s4 = (csp[0] + csp[1]) + (csp[2] + csp[3]);
      if (cj < 192) *(float*)(sm + L_CS + cj * 4) = (s4[0] + s4[1]) + (s4[2] + s4[3]);
    }
    save_residual();
    asm volatile("s_waitcnt lgkmcnt(0)" ::: "memory");
    if (lane == 0) __hip_atomic_fetch_add((LAS u32*)(sm + L_FLG + 4), 1u, __ATOMIC_RELAXED, __HIP_MEMORY_SCOPE_WORKGROUP);
    __builtin_amdgcn_s_setprio(0);
    lds_wait(sm + L_FLG + 8, 1u);
  } else {
    constexpr int NT = 3, NW1 = 48, PRE = W1_PRE * NT;
    u32x4 w1[NW1];
    const __amdgpu_buffer_rsrc_t rw1 = mk_rsrc(ws + WS_W1T + (size_t)(c * 11 + 3 * w) * 16 * 1024, 48 * 1024);
    auto w1load = [&](int k) { w1[k] = bload16s(rw1, lane * 16, (k % NT) * 16384 + (k / NT) * 1024); };
    auto issued = [&](int ks) constexpr { const int v = PRE + NT * (ks + 1); return v < NW1 ? v : NW1; };
#pragma unroll
    for (int i = 0; i < PRE; i++) w1load(i);
    asm volatile("s_waitcnt vmcnt(%0) lgkmcnt(0)" ::"n"(PRE) : "memory");
    __builtin_amdgcn_s_barrier();
    const char* tb[4];
#pragma unroll
    for (int m = 0; m < 4; m++) tb[m] = sm + L_HT + (8 * g + q4) * 128 + (((2 * m + (p4 >> 1)) ^ fsw) << 4) + 8 * (p4 & 1);
    f32x4 acc[NT][4];
    u32x4 hb[3][4];
#pragma unroll
    for (int i = 0; i < NT; i++)
#pragma unroll
      for (int m = 0; m < 4; m++) acc[i][m] = f32x4{0.f, 0.f, 0.f, 0.f};
#pragma unroll
    for (int d = 0; d < 2; d++)
#pragma unroll
      for (int m = 0; m < 4; m++) {
        const u32x2 a = tr_read(tb[m] + d * 4096), bq = tr_read(tb[m] + d * 4096 + 512);
        hb[d][m] = u32x4{a[0], a[1], bq[0], bq[1]};
      }
#pragma unroll
    for (int ks = 0; ks < 16; ks++) {
#pragma unroll
      for (int m = 0; m < 4; m++) {
        acc[0][m] = mfma(w1[ks * NT], hb[ks % 3][m], acc[0][m]);
        SCHED_FENCE();
        if (ks + 2 < 16) { const u32x2 a = tr_read(tb[m] + (ks + 2) * 4096); hb[(ks + 2) % 3][m][0] = a[0]; hb[(ks + 2) % 3][m][1] = a[1]; }
        SCHED_FENCE();
        acc[1][m] = mfma(w1[ks * NT + 1], hb[ks % 3][m], acc[1][m]);
        SCHED_FENCE();
        if (ks + 2 < 16) { const u32x2 a = tr_read(tb[m] + (ks + 2) * 4096 + 512); hb[(ks + 2) % 3][m][2] = a[0]; hb[(ks + 2) % 3][m][3] = a[1]; }
        SCHED_FENCE();
        acc[2][m] = mfma(w1[ks * NT + 2], hb[ks % 3][m], acc[2][m]);
        SCHED_FENCE();
        if (m < NT && issued(ks - 1) + m < issued(ks)) w1load(issued(ks - 1) + m);
        SCHED_FENCE();
      }
      if (ks == 15 - W1_PRE && w == 0 && lane == 0) *(volatile LAS u32*)(sm + L_FLG + 8) = 1u;
    }
    save_residual();
    lds_wait(sm + L_FLG + 4, 4u);
    float rs[4], mr[4];
    f32x4 cs[NT];
#pragma unroll
    for (int m = 0; m < 4; m++) { const f32x2 v = *(const f32x2*)(sm + L_ST + (m * 16 + n) * 8); rs[m] = v[0]; mr[m] = v[1]; }
#pragma unroll
    for (int i = 0; i < NT; i++) cs[i] = *(const f32x4*)(sm + L_CS + ((3 * w + i) * 16 + 4 * g) * 4);
    const __amdgpu_buffer_rsrc_t hrs = mk_rsrc(ws + WS_HF + (size_t)rt * 44 * 4096, 44 * 4096);
    u32 o_t[NT];
#pragma unroll
    for (int i = 0; i < NT; i++) {
      const int tl = 3 * w + i;
      const int ks2 = tl < 10 ? 5 * c + (tl >> 1) : 40 + (c >> 1), par = tl < 10 ? (tl & 1) : (c & 1);
      o_t[i] = (tl < 11 && (g & 1) == par) ? (u32)(ks2 * 4096 + lane * 16) : 0x40000000u;
    }
#pragma unroll
    for (int m = 0; m < 4; m++) {
#pragma unroll
      for (int i = 0; i < NT; i++) {
        float h1[4];
#pragma unroll
        for (int r = 0; r < 4; r++) h1[r] = silu_val(acc[i][m][r], rs[m], mr[m] * cs[i][r]);
        const u32 p0 = pack_bf(h1[0], h1[1]), p1 = pack_bf(h1[2], h1[3]);
        const auto s0 = __builtin_amdgcn_permlane16_swap(p0, p0, false, false), s1 = __builtin_amdgcn_permlane16_swap(p1, p1, false, false);
        __builtin_amdgcn_raw_buffer_store_b128(u32x4{s0[0], s1[0], s0[1], s1[1]}, hrs, o_t[i] + m * 1024, 0, 16);
      }
      SCHED_FENCE();
    }
  }
  u32x4 w2[22];
#pragma unroll
  for (int i = 0; i < 8; i++) bload16_asm(w2[i], rw2, lane * 16, i * 1024);
  load_w2_tail_support(w2, rw2, lane * 16, w);
  asm volatile("s_cmp_lt_u32 %0, 4\n\ts_cbranch_scc0 1f\n\ts_waitcnt vmcnt(0)\n1:\n\ts_waitcnt lgkmcnt(0)" ::"s"(w) : "memory", "scc");
  __builtin_amdgcn_s_barrier();
  if (w == 7) {
    GAS u32* flags = (GAS u32*)(ws + WS_FLAG);
    if (lane < 32) __hip_atomic_store(flags + b * 64 + lane, 1u, RLX_AGENT);
    const int pb = (b & 3) + (b & 56) + 4 * ((lane >> 2) & 1) + 64 * (lane & 3);
    for (u32 spins = 0; spins < (1u << 20); spins++) {
      const u32 v = __hip_atomic_load(flags + pb * 64, RLX_AGENT);
      if (__all(v == 1u)) break;
      __builtin_amdgcn_s_sleep(1);
    }
  }
  if (w < 4) __builtin_amdgcn_s_sleep(2);
  load_w2_tail_main(w2, rw2, lane * 16, w);
  __builtin_amdgcn_s_barrier();

  const int dh = w & 1, kq = w >> 1;
  const __amdgpu_buffer_rsrc_t rhf = mk_rsrc(ws + WS_HF + (size_t)rt * 44 * 4096 + (size_t)(11 * kq * 4 + 2 * dh) * 1024, 11 * 4096);
  const u32 ldst = (u32)(size_t)sm + (kq * 4 + 2 * dh) * 1024;
  auto stage1 = [&](int s, int e) {
    const int slot = s < 9 ? s : s - 9;
    glds16<true>(rhf, lane * 16, s * 4096 + e * 1024, ldst + slot * 16384 + e * 1024);
  };
  auto stage = [&](int s) { stage1(s, 0); stage1(s, 1); };
  stage(0); stage(1); stage(2); stage(3);
  f32x4 acc2[2][4];
#pragma unroll
  for (int i = 0; i < 2; i++)
#pragma unroll
    for (int m = 0; m < 4; m++) acc2[i][m] = f32x4{0.f, 0.f, 0.f, 0.f};
  asm volatile("s_waitcnt vmcnt(4)"
               : "+v"(w2[0]), "+v"(w2[1]), "+v"(w2[2]), "+v"(w2[3]), "+v"(w2[4]), "+v"(w2[5]), "+v"(w2[6]), "+v"(w2[7]), "+v"(w2[8]), "+v"(w2[9]), "+v"(w2[10]),
                 "+v"(w2[11]), "+v"(w2[12]), "+v"(w2[13]), "+v"(w2[14]), "+v"(w2[15]), "+v"(w2[16]), "+v"(w2[17]), "+v"(w2[18]), "+v"(w2[19]), "+v"(w2[20]), "+v"(w2[21])
               ::"memory");
  __builtin_amdgcn_s_barrier();
  {
    const char* base = sm + kq * 4096 + lane * 16;
    u32x4 hb[2][4];
#pragma unroll
    for (int m = 0; m < 4; m++) hb[0][m] = *(const u32x4*)(base + m * 1024);
#pragma unroll
    for (int s = 0; s < 11; s++) {
      if (s == 1) { asm volatile("s_waitcnt vmcnt(4)" ::: "memory"); __builtin_amdgcn_s_barrier(); }
      else if (s == 3 || s == 5) { asm volatile("s_waitcnt vmcnt(6)" ::: "memory"); __builtin_amdgcn_s_barrier(); }
      else if (s == 7) { asm volatile("s_waitcnt vmcnt(2)" ::: "memory"); __builtin_amdgcn_s_barrier(); }
      else if (s == 9) { asm volatile("s_waitcnt vmcnt(0)" ::: "memory"); __builtin_amdgcn_s_barrier(); }
      const int slot = s + 1 < 9 ? s + 1 : s + 1 - 9;
#pragma unroll
      for (int m = 0; m < 4; m++) {
        acc2[0][m] = mfma(w2[2 * s], hb[s & 1][m], acc2[0][m]);
        SCHED_FENCE();
        if (s < 10) hb[(s + 1) & 1][m] = *(const u32x4*)(base + slot * 16384 + m * 1024);
        SCHED_FENCE();
        acc2[1][m] = mfma(w2[2 * s + 1], hb[s & 1][m], acc2[1][m]);
        SCHED_FENCE();
        if (s < 2) stage1(4 + 2 * s + (m >> 1), m & 1);
        else if (s < 5 && (m & 1)) stage1(s + 6, m >> 1);
        SCHED_FENCE();
      }
    }
  }
  asm volatile("s_waitcnt lgkmcnt(0)" ::: "memory");
  __builtin_amdgcn_s_barrier();
  {
    char* red = sm + L_RED + dh * 32768;
    f32x4 o0, o1;
    auto scatter = [&](auto KQ) {
      constexpr int k = decltype(KQ)::value;
#pragma unroll
      for (int m = 0; m < 4; m++) {
        if (m != k) {
#pragma unroll
          for (int i = 0; i < 2; i++) *(f32x4*)(red + (((k * 4 + m) * 2 + i) * 64 + lane) * 16) = acc2[i][m];
        }
      }
      o0 = acc2[0][k]; o1 = acc2[1][k];
      asm volatile("" : "+v"(o0), "+v"(o1));
    };
    if (kq == 0) scatter(IntC<0>{}); else if (kq == 1) scatter(IntC<1>{}); else if (kq == 2) scatter(IntC<2>{}); else scatter(IntC<3>{});
    __syncthreads();
#pragma unroll
    for (int j = 1; j < 4; j++) {
      const int src = (kq + j) & 3;
      o0 += *(const f32x4*)(red + (((src * 4 + kq) * 2 + 0) * 64 + lane) * 16);
      o1 += *(const f32x4*)(red + (((src * 4 + kq) * 2 + 1) * 64 + lane) * 16);
    }
    const u32x4 hres = *(const u32x4*)(sm + L_RES + (w * 64 + lane) * 16);
    u32x4 res;
#pragma unroll
    for (int r = 0; r < 4; r++) res[r] = pack_bf(bf_lo(hres[r]) + rb(o0[r]), bf_hi(hres[r]) + rb(o1[r]));
    __builtin_amdgcn_raw_buffer_store_b128(res, mk_rsrc(OUT, S * D * 2), (u32)((row0 + 16 * kq + n) * D + 64 * c + 32 * dh + 8 * g) * 2, 0, 2);
  }
  asm volatile("ffn_text_end:");
}

extern "C" void kernel_launch(void* const* d_in, const int* in_sizes, int n_in, void* d_out, int out_size, void* d_ws,
                              size_t ws_size, hipStream_t stream) {
  const u16* X = (const u16*)d_in[0];
  const u16* K = (const u16*)d_in[1];
  const u16* V = (const u16*)d_in[2];
  const u16* SK = (const u16*)d_in[3];
  const u16* HK = (const u16*)d_in[4];
  const u16* DV = (const u16*)d_in[5];
  const u16* W1 = (const u16*)d_in[7];
  const u16* W2 = (const u16*)d_in[8];
  char* ws = (char*)d_ws;
  k1_prep<<<256, 512, 0, stream>>>(X, K, V, SK, HK, ws);
  k2_conv<<<256, 512, 0, stream>>>(HK, DV, W1, W2, ws);
  k34_ffn<<<256, 512, 0, stream>>>(ws, (u16*)d_out);
}
```
